```python
import torch

def _softmax(x, axis=-1):
    return torch.softmax(x, dim=axis)
def _gelu(x):
    return torch.nn.functional.gelu(x, approximate="tanh")
def _silu(x):
    return torch.nn.functional.silu(x)
def _sigmoid(x):
    return torch.sigmoid(x)
def _layernorm(x, axis=-1, eps=1e-5):
    m = x.mean(dim=axis, keepdim=True)
    v = x.var(dim=axis, keepdim=True, unbiased=False)
    return (x - m) / torch.sqrt(v + eps)
def _scatter_add(tgt, idx, src, axis=0):
    return tgt.clone().index_add_(axis, idx, src)
def _segment_sum(src, seg, n):
    out = torch.zeros((n,) + src.shape[1:], dtype=src.dtype)
    return out.index_add_(0, seg, src)
def _pad(x, pw):
    flat = tuple(v for pair in reversed(pw) for v in pair)
    return torch.nn.functional.pad(x, flat)

kernel_name = 'recurrent_scan__spectral_fnet__swiglu_ffn__prompt'

def setup_inputs():

    _w = (lambda a: (lambda t: t.to(torch.bfloat16) if t.is_floating_point() else t)(torch.as_tensor(a)))
    def _A_setup():
        B, S, D = 1, 2048, 1024
        ns = torch.arange(S)[:, None] * torch.arange(S)[None, :]
        nd = torch.arange(D)[:, None] * torch.arange(D)[None, :]
        Ws_re = torch.cos(-2.0 * torch.pi * ns / S).to(float)
        Ws_im = torch.sin(-2.0 * torch.pi * ns / S).to(float)
        Wd_re = torch.cos(-2.0 * torch.pi * nd / D).to(float)
        Wd_im = torch.sin(-2.0 * torch.pi * nd / D).to(float)
        return {
            "x": _w((lambda *s: torch.randn(*s, dtype=torch.bfloat16) * 0.05)(B, S, D)),
            "dft_seq_re": _w(Ws_re),
            "dft_seq_im": _w(Ws_im),
            "dft_hid_re": _w(Wd_re),
            "dft_hid_im": _w(Wd_im),
        }
    _A_in = _A_setup()

    def _B_setup():
        B, S, D, F = 1, 2048, 1024, 3584
        return {
            "x": _w((lambda *s: torch.randn(*s, dtype=torch.bfloat16) * 0.05)(B, S, D)),
            "w_gate": _w((lambda *s: torch.randn(*s, dtype=torch.bfloat16) * 0.05)(D, F)),
            "w_up": _w((lambda *s: torch.randn(*s, dtype=torch.bfloat16) * 0.05)(D, F)),
            "w_down": _w((lambda *s: torch.randn(*s, dtype=torch.bfloat16) * 0.05)(F, D)),
        }
    _B_in = _B_setup()

    return {
        "A_x": _A_in["x"],
        "A_dft_seq_re": _A_in["dft_seq_re"],
        "A_dft_seq_im": _A_in["dft_seq_im"],
        "A_dft_hid_re": _A_in["dft_hid_re"],
        "A_dft_hid_im": _A_in["dft_hid_im"],
        "B_x": _B_in["x"],
        "B_w_gate": _B_in["w_gate"],
        "B_w_up": _B_in["w_up"],
        "B_w_down": _B_in["w_down"],
    }

def reference(A_x, A_dft_seq_re, A_dft_seq_im, A_dft_hid_re, A_dft_hid_im, B_x, B_w_gate, B_w_up, B_w_down):
    def _A_ref(x, dft_seq_re, dft_seq_im, dft_hid_re, dft_hid_im):
        a = torch.einsum("bsd,ed->bse", x, dft_hid_re)
        b = torch.einsum("bsd,ed->bse", x, dft_hid_im)
        y = torch.einsum("ts,bse->bte", dft_seq_re, a) - torch.einsum("ts,bse->bte", dft_seq_im, b)
        return y

    def _B_ref(x, w_gate, w_up, w_down):
        g = _silu(x @ w_gate)
        u = x @ w_up
        return (g * u) @ w_down

    x = A_x
    xn = _layernorm(x, axis=-1)
    s = _A_ref(xn, A_dft_seq_re, A_dft_seq_im, A_dft_hid_re, A_dft_hid_im)
    g = _silu(_B_ref(xn, B_w_gate, B_w_up, B_w_down))
    return x + s * g
```

```cpp
#include <hip/hip_runtime.h>
#include <hip/hip_bf16.h>
typedef unsigned short u16;
using bf16x8 = __attribute__((ext_vector_type(8))) short;
using f32x4  = __attribute__((ext_vector_type(4))) float;
constexpr int S=2048, D=1024, F=3584;
__device__ __forceinline__ float bf2f(u16 v){ return __uint_as_float(((unsigned)v)<<16); }
__device__ __forceinline__ u16 f2bf(float f){ __hip_bfloat16 b = __float2bfloat16(f); return *(u16*)&b; }
__device__ __forceinline__ float rb(float f){ return bf2f(f2bf(f)); }

__global__ __launch_bounds__(64) void ln_kernel(const u16* x, u16* xn){
  int row = blockIdx.x, l = threadIdx.x;
  const u16* p = x + (size_t)row*D + l*16;
  float v[16]; float s=0;
  for(int i=0;i<16;i++){ v[i]=bf2f(p[i]); s+=v[i]; }
  for(int o=32;o;o>>=1) s+=__shfl_xor(s,o);
  float m = s*(1.0f/D); float q=0;
  for(int i=0;i<16;i++){ float d=v[i]-m; q+=d*d; }
  for(int o=32;o;o>>=1) q+=__shfl_xor(q,o);
  float var = rb(q*(1.0f/D));
  float mb = rb(m);
  float sd = rb(sqrtf(rb(var+1e-5f)));
  u16* o = xn + (size_t)row*D + l*16;
  for(int i=0;i<16;i++) o[i]=f2bf(rb(v[i]-mb)/sd);
}
template<int BT, int MODE>
__global__ __launch_bounds__(256) void gemm(const u16* A, int lda, const u16* B, int ldb, const u16* B2, int K, u16* C, int ldc, const u16* aux0, const u16* aux1, const u16* aux2){
  __shared__ u16 sA[64][40], sB[64][40], sB2[64][40];
  int bm=blockIdx.y*64, bn=blockIdx.x*64;
  int t=threadIdx.x, w=t>>6, l=t&63, wr=w>>1, wc=w&1, fr=l&15, fq=l>>4;
  f32x4 acc[2][2]={}, acc2[2][2]={};
  for(int k0=0;k0<K;k0+=32){
    for(int e=t;e<64*32;e+=256){ int r=e>>5,c=e&31; sA[r][c]=A[(size_t)(bm+r)*lda+k0+c];
      if(BT){ sB[r][c]=B[(size_t)(bn+r)*ldb+k0+c]; }
    }
    if(!BT) for(int e=t;e<64*32;e+=256){ int c=e>>6,r=e&63; sB[r][c]=B[(size_t)(k0+c)*ldb+bn+r]; if(MODE==1) sB2[r][c]=B2[(size_t)(k0+c)*ldb+bn+r]; }
    __syncthreads();
    for(int m=0;m<2;m++)for(int n=0;n<2;n++){
      bf16x8 a=*(bf16x8*)&sA[wr*32+m*16+fr][fq*8]; bf16x8 b=*(bf16x8*)&sB[wc*32+n*16+fr][fq*8];
      acc[m][n]=__builtin_amdgcn_mfma_f32_16x16x32_bf16(a,b,acc[m][n],0,0,0);
      if(MODE==1){ bf16x8 b2=*(bf16x8*)&sB2[wc*32+n*16+fr][fq*8]; acc2[m][n]=__builtin_amdgcn_mfma_f32_16x16x32_bf16(a,b2,acc2[m][n],0,0,0);} }
    __syncthreads();
  }
  for(int m=0;m<2;m++)for(int n=0;n<2;n++)for(int j=0;j<4;j++){
    int r=bm+wr*32+m*16+fq*4+j, c=bn+wc*32+n*16+fr; float v=acc[m][n][j];
    if(MODE==1){ float g=rb(v); g=rb(g/(1.f+__expf(-g))); v=g*rb(acc2[m][n][j]); }
    if(MODE==2){
      float d=rb(v); float g=rb(d/(1.f+__expf(-d))); size_t o=(size_t)r*ldc+c;
      float y=rb(bf2f(aux0[o])-bf2f(aux1[o])); v=bf2f(aux2[o])+rb(y*g); }
    C[(size_t)r*ldc+c]=f2bf(v);
  }
}
extern "C" void kernel_launch(void* const* d_in, const int* in_sizes, int n_in, void* d_out, int out_size, void* d_ws, size_t ws_size, hipStream_t stream){
  const u16* x=(const u16*)d_in[0]; const u16* Cr=(const u16*)d_in[1]; const u16* Ci=(const u16*)d_in[2]; const u16* Dr=(const u16*)d_in[3]; const u16* Di=(const u16*)d_in[4];
  const u16* Wg=(const u16*)d_in[6]; const u16* Wu=(const u16*)d_in[7]; const u16* Wd=(const u16*)d_in[8];
  u16* ws=(u16*)d_ws; u16* xn=ws; u16* a=xn+S*D; u16* b=a+S*D; u16* p=b+S*D; u16* q=p+S*D; u16* h=q+S*D;
  ln_kernel<<<S,64,0,stream>>>(x,xn);
  gemm<1,0><<<dim3(D/64,S/64),256,0,stream>>>(xn,D,Dr,D,nullptr,D,a,D,nullptr,nullptr,nullptr);
  gemm<1,0><<<dim3(D/64,S/64),256,0,stream>>>(xn,D,Di,D,nullptr,D,b,D,nullptr,nullptr,nullptr);
  gemm<0,1><<<dim3(F/64,S/64),256,0,stream>>>(xn,D,Wg,F,Wu,D,h,F,nullptr,nullptr,nullptr);
  gemm<0,0><<<dim3(D/64,S/64),256,0,stream>>>(Cr,S,a,D,nullptr,S,p,D,nullptr,nullptr,nullptr);
  gemm<0,0><<<dim3(D/64,S/64),256,0,stream>>>(Ci,S,b,D,nullptr,S,q,D,nullptr,nullptr,nullptr);
  gemm<0,2><<<dim3(D/64,S/64),256,0,stream>>>(h,F,Wd,D,nullptr,F,(u16*)d_out,D,p,q,x);
}
```

```cpp
#include <hip/hip_runtime.h>
#include <hip/hip_bf16.h>

struct MegaArgs { const void* p[20]; };
extern "C" __global__ __launch_bounds__(256,1) void fused_kernel(MegaArgs args){
  __shared__ __attribute__((aligned(1024))) char smem[147456];
  unsigned tid=threadIdx.x;
  asm volatile(
    "s_load_dwordx2 s[6:7], %1, 144\n"
    "s_load_dwordx16 s[16:31], %1, 0\n"
    "s_load_dwordx2 s[52:53], %1, 112\n"
    "s_mov_b32 s8, %2\n"
    "v_mov_b32 v255, v0\n"
    "v_lshrrev_b32 v240, 6, v0\n"
    "s_nop 0\n"
    "v_readfirstlane_b32 s60, v240\n"
    "v_and_b32 v240, 63, v0\n"
    "v_and_b32 v241, 15, v240\n"
    "v_lshrrev_b32 v242, 4, v240\n"
    "s_nop 1\n"
    "s_lshr_b32 s61, s60, 1\n"
    "s_and_b32 s62, s60, 1\n"
    "s_getpc_b64 s[12:13]\n"
    "LPC0_%=:\n"
    "s_lshr_b32 s14, s8, 3\n"
    "s_lshl_b32 s14, s14, 2\n"
    "s_add_u32 s14, s14, s60\n"
    "s_lshl_b32 s14, s14, 2\n"
    "v_lshrrev_b32 v25, 4, v240\n"
    "v_add_u32 v25, s14, v25\n"
    "v_lshlrev_b32 v25, 7, v25\n"
    "s_mov_b32 s14, LCODE_END_%=-LPC0_%=-4\n"
    "v_min_u32 v25, s14, v25\n"
    "global_load_dword v26, v25, s[12:13]\n"
    "s_and_b32 s69, s8, 7\n"
    "s_lshr_b32 s70, s8, 3\n"
    "s_and_b32 s70, s70, 3\n"
    "s_lshl_b32 s70, s70, 3\n"
    "s_add_u32 s63, s69, s70\n"
    "s_lshr_b32 s64, s8, 5\n"
    "s_cmp_ge_u32 s63, 24\n"
    "s_cbranch_scc0 LDEV_%=\n"
    "s_and_b32 s69, s8, 7\n"
    "s_lshr_b32 s70, s69, 1\n"
    "s_add_u32 s63, s70, 24\n"
    "s_bitcmp0_b32 s69, 0\n"
    "s_cselect_b32 s70, 4, 0\n"
    "s_add_u32 s63, s63, s70\n"
    "LDEV_%=:\n"
    "v_lshlrev_b32 v24, 4, v240\n"
    "s_lshl_b32 s69, s64, 8\n"
    "s_lshl_b32 s70, s63, 3\n"
    "s_add_u32 s69, s69, s70\n"
    "s_lshl_b32 s70, s60, 1\n"
    "s_add_u32 s69, s69, s70\n"
    "s_lshl_b32 s99, s69, 11\n"
    "s_waitcnt lgkmcnt(0)\n"
    "s_and_b32 s53, s53, 0xffff\n"
    "s_mov_b32 s54, 4194304\n"
    "s_mov_b32 s55, 0x20000\n"
    "buffer_load_dwordx4 v[8:11], v24, s[52:55], s99 offen offset:0\n"
    "buffer_load_dwordx4 v[12:15], v24, s[52:55], s99 offen offset:1024\n"
    "buffer_load_dwordx4 v[16:19], v24, s[52:55], s99 offen offset:2048\n"
    "buffer_load_dwordx4 v[20:23], v24, s[52:55], s99 offen offset:3072\n"
    "s_load_dwordx2 s[4:5], s[6:7], 40960\n"
    "s_cmp_ge_u32 s63, 28\n"
    "s_cbranch_scc1 LP1D_%=\n"
    "v_lshrrev_b32 v243, 3, v240\n"
    "v_and_b32 v244, 7, v240\n"
    "v_xor_b32 v244, v244, v243\n"
    "v_lshlrev_b32 v244, 4, v244\n"
    "v_lshl_add_u32 v245, v243, 11, v244\n"
    "s_lshl_b32 s69, s61, 18\n"
    "s_lshl_b32 s70, s62, 16\n"
    "s_add_u32 s69, s69, s70\n"
    "s_add_u32 s70, s69, 0\n"
    "v_add_u32 v220, s70, v245\n"
    "s_add_u32 s70, s69, 15360\n"
    "v_add_u32 v221, s70, v245\n"
    "s_add_u32 s70, s69, 30720\n"
    "v_add_u32 v222, s70, v245\n"
    "s_add_u32 s70, s69, 46080\n"
    "v_add_u32 v223, s70, v245\n"
    "s_add_u32 s70, s69, 131072\n"
    "v_add_u32 v224, s70, v245\n"
    "s_add_u32 s70, s69, 146432\n"
    "v_add_u32 v225, s70, v245\n"
    "s_add_u32 s70, s69, 161792\n"
    "v_add_u32 v226, s70, v245\n"
    "s_add_u32 s70, s69, 177152\n"
    "v_add_u32 v227, s70, v245\n"
    "s_and_b32 s69, s60, 1\n"
    "s_lshl_b32 s69, s69, 3\n"
    "s_lshl_b32 s70, s63, 4\n"
    "s_add_u32 s69, s69, s70\n"
    "s_lshl_b32 s69, s69, 11\n"
    "v_add_u32 v232, s69, v245\n"
    "s_mul_i32 s69, s60, 114688\n"
    "v_mul_u32_u24 v246, 7168, v242\n"
    "v_lshl_or_b32 v247, v242, 2, 0\n"
    "v_xor_b32 v247, v241, v247\n"
    "v_lshl_add_u32 v247, v247, 4, v246\n"
    "s_add_u32 s70, s69, 0\n"
    "v_add_u32 v228, s70, v247\n"
    "v_lshl_or_b32 v247, v242, 2, 1\n"
    "v_xor_b32 v247, v241, v247\n"
    "v_lshl_add_u32 v247, v247, 4, v246\n"
    "s_add_u32 s70, s69, 27648\n"
    "v_add_u32 v229, s70, v247\n"
    "v_lshl_or_b32 v247, v242, 2, 2\n"
    "v_xor_b32 v247, v241, v247\n"
    "v_lshl_add_u32 v247, v247, 4, v246\n"
    "s_add_u32 s70, s69, 55296\n"
    "v_add_u32 v230, s70, v247\n"
    "v_lshl_or_b32 v247, v242, 2, 3\n"
    "v_xor_b32 v247, v241, v247\n"
    "v_lshl_add_u32 v247, v247, 4, v246\n"
    "s_add_u32 s70, s69, 82944\n"
    "v_add_u32 v231, s70, v247\n"
    "s_lshl_b32 s69, s61, 14\n"
    "s_lshl_b32 s70, s62, 12\n"
    "s_add_u32 s69, s69, s70\n"
    "s_add_u32 s72, s69, 0\n"
    "s_add_u32 s73, s69, 8192\n"
    "s_add_u32 s74, s69, 32768\n"
    "s_add_u32 s75, s69, 40960\n"
    "s_lshl_b32 s69, s60, 12\n"
    "s_add_u32 s76, s69, 65536\n"
    "s_add_u32 s78, s69, 81920\n"
    "s_add_u32 s77, s69, 98304\n"
    "s_add_u32 s79, s69, 114688\n"
    "s_lshl_b32 s69, s60, 10\n"
    "s_add_u32 s80, s69, 131072\n"
    "s_add_u32 s81, s69, 135168\n"
    "s_mov_b32 s65, 0\n"
    "s_mov_b32 s66, 0\n"
    "s_waitcnt lgkmcnt(0)\n"
    "s_lshl_b32 s69, s64, 19\n"
    "s_add_u32 s36, s16, s69\n"
    "s_addc_u32 s37, s17, 0\n"
    "s_and_b32 s37, s37, 0xffff\n"
    "s_sub_u32 s38, 4194304, s69\n"
    "s_mov_b32 s39, 0x20000\n"
    "s_lshl_b32 s69, s63, 8\n"
    "s_add_u32 s40, s18, s69\n"
    "s_addc_u32 s41, s19, 0\n"
    "s_and_b32 s41, s41, 0xffff\n"
    "s_sub_u32 s42, 7340032, s69\n"
    "s_mov_b32 s43, 0x20000\n"
    "s_add_u32 s44, s20, s69\n"
    "s_addc_u32 s45, s21, 0\n"
    "s_and_b32 s45, s45, 0xffff\n"
    "s_sub_u32 s46, 7340032, s69\n"
    "s_mov_b32 s47, 0x20000\n"
    "s_cmp_lt_u32 s60, 2\n"
    "s_cselect_b32 s48, s22, s24\n"
    "s_cselect_b32 s49, s23, s25\n"
    "s_and_b32 s49, s49, 0xffff\n"
    "s_mov_b32 s50, 2097152\n"
    "s_mov_b32 s51, 0x20000\n"
    "s_mul_hi_u32 s10, s4, 1284865837\n"
    "s_mul_i32 s11, s4, 1481765933\n"
    "s_add_u32 s10, s10, s11\n"
    "s_mul_i32 s11, s5, 1284865837\n"
    "s_add_u32 s10, s10, s11\n"
    "s_mul_i32 s11, s4, 1284865837\n"
    "s_add_u32 s4, s11, 4150755663\n"
    "s_addc_u32 s5, s10, 335903614\n"
    "s_mov_b32 s56, s16\n"
    "s_and_b32 s57, s17, 0xffff\n"
    "s_mov_b32 s58, 4194304\n"
    "s_mov_b32 s59, 0x20000\n"
    "s_waitcnt vmcnt(0)\n"
    "s_mov_b32 s98, 0xffff0000\n"
    "v_lshlrev_b32 v32, 16, v8\n"
    "v_and_b32 v33, s98, v8\n"
    "v_lshlrev_b32 v34, 16, v9\n"
    "v_and_b32 v35, s98, v9\n"
    "v_lshlrev_b32 v36, 16, v10\n"
    "v_and_b32 v37, s98, v10\n"
    "v_lshlrev_b32 v38, 16, v11\n"
    "v_and_b32 v39, s98, v11\n"
    "v_lshlrev_b32 v40, 16, v12\n"
    "v_and_b32 v41, s98, v12\n"
    "v_lshlrev_b32 v42, 16, v13\n"
    "v_and_b32 v43, s98, v13\n"
    "v_lshlrev_b32 v44, 16, v14\n"
    "v_and_b32 v45, s98, v14\n"
    "v_lshlrev_b32 v46, 16, v15\n"
    "v_and_b32 v47, s98, v15\n"
    "v_lshlrev_b32 v48, 16, v16\n"
    "v_and_b32 v49, s98, v16\n"
    "v_lshlrev_b32 v50, 16, v17\n"
    "v_and_b32 v51, s98, v17\n"
    "v_lshlrev_b32 v52, 16, v18\n"
    "v_and_b32 v53, s98, v18\n"
    "v_lshlrev_b32 v54, 16, v19\n"
    "v_and_b32 v55, s98, v19\n"
    "v_lshlrev_b32 v56, 16, v20\n"
    "v_and_b32 v57, s98, v20\n"
    "v_lshlrev_b32 v58, 16, v21\n"
    "v_and_b32 v59, s98, v21\n"
    "v_lshlrev_b32 v60, 16, v22\n"
    "v_and_b32 v61, s98, v22\n"
    "v_lshlrev_b32 v62, 16, v23\n"
    "v_and_b32 v63, s98, v23\n"
    "v_pk_add_f32 v[64:65], v[32:33], v[34:35]\n"
    "v_pk_add_f32 v[66:67], v[36:37], v[38:39]\n"
    "v_pk_add_f32 v[68:69], v[48:49], v[50:51]\n"
    "v_pk_add_f32 v[70:71], v[52:53], v[54:55]\n"
    "v_pk_add_f32 v[64:65], v[64:65], v[40:41]\n"
    "v_pk_add_f32 v[66:67], v[66:67], v[42:43]\n"
    "v_pk_add_f32 v[68:69], v[68:69], v[56:57]\n"
    "v_pk_add_f32 v[70:71], v[70:71], v[58:59]\n"
    "v_pk_add_f32 v[64:65], v[64:65], v[44:45]\n"
    "v_pk_add_f32 v[66:67], v[66:67], v[46:47]\n"
    "v_pk_add_f32 v[68:69], v[68:69], v[60:61]\n"
    "v_pk_add_f32 v[70:71], v[70:71], v[62:63]\n"
    "v_pk_add_f32 v[64:65], v[64:65], v[66:67]\n"
    "v_pk_add_f32 v[68:69], v[68:69], v[70:71]\n"
    "v_add_f32 v64, v64, v65\n"
    "v_add_f32 v68, v68, v69\n"
    "s_nop 1\n"
    "v_add_f32_dpp v64, v64, v64 row_ror:8 row_mask:0xf bank_mask:0xf\n"
    "v_add_f32_dpp v68, v68, v68 row_ror:8 row_mask:0xf bank_mask:0xf\n"
    "s_nop 1\n"
    "v_add_f32_dpp v64, v64, v64 row_ror:4 row_mask:0xf bank_mask:0xf\n"
    "v_add_f32_dpp v68, v68, v68 row_ror:4 row_mask:0xf bank_mask:0xf\n"
    "s_nop 1\n"
    "v_add_f32_dpp v64, v64, v64 row_ror:2 row_mask:0xf bank_mask:0xf\n"
    "v_add_f32_dpp v68, v68, v68 row_ror:2 row_mask:0xf bank_mask:0xf\n"
    "s_nop 1\n"
    "v_add_f32_dpp v64, v64, v64 row_ror:1 row_mask:0xf bank_mask:0xf\n"
    "v_add_f32_dpp v68, v68, v68 row_ror:1 row_mask:0xf bank_mask:0xf\n"
    "v_mov_b32 v65, v64\n"
    "v_mov_b32 v69, v68\n"
    "s_nop 1\n"
    "v_permlane16_swap_b32 v64, v65\n"
    "v_permlane16_swap_b32 v68, v69\n"
    "s_nop 1\n"
    "v_add_f32 v64, v64, v65\n"
    "v_add_f32 v68, v68, v69\n"
    "v_mov_b32 v65, v64\n"
    "v_mov_b32 v69, v68\n"
    "s_nop 1\n"
    "v_permlane32_swap_b32 v64, v65\n"
    "v_permlane32_swap_b32 v68, v69\n"
    "s_nop 1\n"
    "v_add_f32 v64, v64, v65\n"
    "v_add_f32 v68, v68, v69\n"
    "v_mul_f32 v64, 0xba800000, v64\n"
    "v_mul_f32 v68, 0xba800000, v68\n"
    "v_pk_add_f32 v[32:33], v[32:33], v[64:65] op_sel_hi:[1,0]\n"
    "v_pk_add_f32 v[34:35], v[34:35], v[64:65] op_sel_hi:[1,0]\n"
    "v_pk_add_f32 v[36:37], v[36:37], v[64:65] op_sel_hi:[1,0]\n"
    "v_pk_add_f32 v[38:39], v[38:39], v[64:65] op_sel_hi:[1,0]\n"
    "v_pk_add_f32 v[40:41], v[40:41], v[64:65] op_sel_hi:[1,0]\n"
    "v_pk_add_f32 v[42:43], v[42:43], v[64:65] op_sel_hi:[1,0]\n"
    "v_pk_add_f32 v[44:45], v[44:45], v[64:65] op_sel_hi:[1,0]\n"
    "v_pk_add_f32 v[46:47], v[46:47], v[64:65] op_sel_hi:[1,0]\n"
    "v_pk_add_f32 v[48:49], v[48:49], v[68:69] op_sel_hi:[1,0]\n"
    "v_pk_add_f32 v[50:51], v[50:51], v[68:69] op_sel_hi:[1,0]\n"
    "v_pk_add_f32 v[52:53], v[52:53], v[68:69] op_sel_hi:[1,0]\n"
    "v_pk_add_f32 v[54:55], v[54:55], v[68:69] op_sel_hi:[1,0]\n"
    "v_pk_add_f32 v[56:57], v[56:57], v[68:69] op_sel_hi:[1,0]\n"
    "v_pk_add_f32 v[58:59], v[58:59], v[68:69] op_sel_hi:[1,0]\n"
    "v_pk_add_f32 v[60:61], v[60:61], v[68:69] op_sel_hi:[1,0]\n"
    "v_pk_add_f32 v[62:63], v[62:63], v[68:69] op_sel_hi:[1,0]\n"
    "v_pk_mul_f32 v[72:73], v[32:33], v[32:33]\n"
    "v_pk_mul_f32 v[74:75], v[34:35], v[34:35]\n"
    "v_pk_mul_f32 v[76:77], v[48:49], v[48:49]\n"
    "v_pk_mul_f32 v[78:79], v[50:51], v[50:51]\n"
    "v_pk_fma_f32 v[72:73], v[36:37], v[36:37], v[72:73]\n"
    "v_pk_fma_f32 v[74:75], v[38:39], v[38:39], v[74:75]\n"
    "v_pk_fma_f32 v[76:77], v[52:53], v[52:53], v[76:77]\n"
    "v_pk_fma_f32 v[78:79], v[54:55], v[54:55], v[78:79]\n"
    "v_pk_fma_f32 v[72:73], v[40:41], v[40:41], v[72:73]\n"
    "v_pk_fma_f32 v[74:75], v[42:43], v[42:43], v[74:75]\n"
    "v_pk_fma_f32 v[76:77], v[56:57], v[56:57], v[76:77]\n"
    "v_pk_fma_f32 v[78:79], v[58:59], v[58:59], v[78:79]\n"
    "v_pk_fma_f32 v[72:73], v[44:45], v[44:45], v[72:73]\n"
    "v_pk_fma_f32 v[74:75], v[46:47], v[46:47], v[74:75]\n"
    "v_pk_fma_f32 v[76:77], v[60:61], v[60:61], v[76:77]\n"
    "v_pk_fma_f32 v[78:79], v[62:63], v[62:63], v[78:79]\n"
    "v_pk_add_f32 v[72:73], v[72:73], v[74:75]\n"
    "v_pk_add_f32 v[76:77], v[76:77], v[78:79]\n"
    "v_add_f32 v72, v72, v73\n"
    "v_add_f32 v76, v76, v77\n"
    "s_nop 1\n"
    "v_add_f32_dpp v72, v72, v72 row_ror:8 row_mask:0xf bank_mask:0xf\n"
    "v_add_f32_dpp v76, v76, v76 row_ror:8 row_mask:0xf bank_mask:0xf\n"
    "s_nop 1\n"
    "v_add_f32_dpp v72, v72, v72 row_ror:4 row_mask:0xf bank_mask:0xf\n"
    "v_add_f32_dpp v76, v76, v76 row_ror:4 row_mask:0xf bank_mask:0xf\n"
    "s_nop 1\n"
    "v_add_f32_dpp v72, v72, v72 row_ror:2 row_mask:0xf bank_mask:0xf\n"
    "v_add_f32_dpp v76, v76, v76 row_ror:2 row_mask:0xf bank_mask:0xf\n"
    "s_nop 1\n"
    "v_add_f32_dpp v72, v72, v72 row_ror:1 row_mask:0xf bank_mask:0xf\n"
    "v_add_f32_dpp v76, v76, v76 row_ror:1 row_mask:0xf bank_mask:0xf\n"
    "v_mov_b32 v73, v72\n"
    "v_mov_b32 v77, v76\n"
    "s_nop 1\n"
    "v_permlane16_swap_b32 v72, v73\n"
    "v_permlane16_swap_b32 v76, v77\n"
    "s_nop 1\n"
    "v_add_f32 v72, v72, v73\n"
    "v_add_f32 v76, v76, v77\n"
    "v_mov_b32 v73, v72\n"
    "v_mov_b32 v77, v76\n"
    "s_nop 1\n"
    "v_permlane32_swap_b32 v72, v73\n"
    "v_permlane32_swap_b32 v76, v77\n"
    "s_nop 1\n"
    "v_add_f32 v72, v72, v73\n"
    "v_add_f32 v76, v76, v77\n"
    "v_mul_f32 v72, 0x3a800000, v72\n"
    "v_mul_f32 v76, 0x3a800000, v76\n"
    "v_cvt_pk_bf16_f32 v72, v72, v72\n"
    "v_lshlrev_b32 v72, 16, v72\n"
    "v_cvt_pk_bf16_f32 v76, v76, v76\n"
    "v_lshlrev_b32 v76, 16, v76\n"
    "v_add_f32 v72, 0x3727c5ac, v72\n"
    "v_add_f32 v76, 0x3727c5ac, v76\n"
    "v_cvt_pk_bf16_f32 v72, v72, v72\n"
    "v_lshlrev_b32 v72, 16, v72\n"
    "v_cvt_pk_bf16_f32 v76, v76, v76\n"
    "v_lshlrev_b32 v76, 16, v76\n"
    "v_sqrt_f32 v72, v72\n"
    "v_sqrt_f32 v76, v76\n"
    "s_nop 0\n"
    "v_cvt_pk_bf16_f32 v72, v72, v72\n"
    "v_lshlrev_b32 v72, 16, v72\n"
    "v_cvt_pk_bf16_f32 v76, v76, v76\n"
    "v_lshlrev_b32 v76, 16, v76\n"
    "v_rcp_f32 v72, v72\n"
    "v_rcp_f32 v76, v76\n"
    "s_nop 0\n"
    "v_pk_mul_f32 v[32:33], v[32:33], v[72:73] op_sel_hi:[1,0]\n"
    "v_pk_mul_f32 v[34:35], v[34:35], v[72:73] op_sel_hi:[1,0]\n"
    "v_pk_mul_f32 v[36:37], v[36:37], v[72:73] op_sel_hi:[1,0]\n"
    "v_pk_mul_f32 v[38:39], v[38:39], v[72:73] op_sel_hi:[1,0]\n"
    "v_pk_mul_f32 v[40:41], v[40:41], v[72:73] op_sel_hi:[1,0]\n"
    "v_pk_mul_f32 v[42:43], v[42:43], v[72:73] op_sel_hi:[1,0]\n"
    "v_pk_mul_f32 v[44:45], v[44:45], v[72:73] op_sel_hi:[1,0]\n"
    "v_pk_mul_f32 v[46:47], v[46:47], v[72:73] op_sel_hi:[1,0]\n"
    "v_cvt_pk_bf16_f32 v8, v32, v33\n"
    "v_cvt_pk_bf16_f32 v9, v34, v35\n"
    "v_cvt_pk_bf16_f32 v10, v36, v37\n"
    "v_cvt_pk_bf16_f32 v11, v38, v39\n"
    "v_cvt_pk_bf16_f32 v12, v40, v41\n"
    "v_cvt_pk_bf16_f32 v13, v42, v43\n"
    "v_cvt_pk_bf16_f32 v14, v44, v45\n"
    "v_cvt_pk_bf16_f32 v15, v46, v47\n"
    "v_pk_mul_f32 v[48:49], v[48:49], v[76:77] op_sel_hi:[1,0]\n"
    "v_pk_mul_f32 v[50:51], v[50:51], v[76:77] op_sel_hi:[1,0]\n"
    "v_pk_mul_f32 v[52:53], v[52:53], v[76:77] op_sel_hi:[1,0]\n"
    "v_pk_mul_f32 v[54:55], v[54:55], v[76:77] op_sel_hi:[1,0]\n"
    "v_pk_mul_f32 v[56:57], v[56:57], v[76:77] op_sel_hi:[1,0]\n"
    "v_pk_mul_f32 v[58:59], v[58:59], v[76:77] op_sel_hi:[1,0]\n"
    "v_pk_mul_f32 v[60:61], v[60:61], v[76:77] op_sel_hi:[1,0]\n"
    "v_pk_mul_f32 v[62:63], v[62:63], v[76:77] op_sel_hi:[1,0]\n"
    "v_cvt_pk_bf16_f32 v16, v48, v49\n"
    "v_cvt_pk_bf16_f32 v17, v50, v51\n"
    "v_cvt_pk_bf16_f32 v18, v52, v53\n"
    "v_cvt_pk_bf16_f32 v19, v54, v55\n"
    "v_cvt_pk_bf16_f32 v20, v56, v57\n"
    "v_cvt_pk_bf16_f32 v21, v58, v59\n"
    "v_cvt_pk_bf16_f32 v22, v60, v61\n"
    "v_cvt_pk_bf16_f32 v23, v62, v63\n"
    "buffer_store_dwordx4 v[8:11], v24, s[56:59], s99 offen offset:0 sc1\n"
    "buffer_store_dwordx4 v[12:15], v24, s[56:59], s99 offen offset:1024 sc1\n"
    "buffer_store_dwordx4 v[16:19], v24, s[56:59], s99 offen offset:2048 sc1\n"
    "buffer_store_dwordx4 v[20:23], v24, s[56:59], s99 offen offset:3072 sc1\n"
    "s_mov_b32 s65, 0\n"
    "s_mov_b32 s66, 0\n"
    "s_mov_b32 m0, s76\n"
    "s_nop 0\n"
    "buffer_load_dwordx4 v228, s[40:43], s66 offen offset:0 nt lds\n"
    "buffer_load_dwordx4 v229, s[40:43], s66 offen offset:1024 nt lds\n"
    "buffer_load_dwordx4 v230, s[40:43], s66 offen offset:2048 nt lds\n"
    "buffer_load_dwordx4 v231, s[40:43], s66 offen offset:3072 nt lds\n"
    "s_mov_b32 m0, s78\n"
    "s_nop 0\n"
    "buffer_load_dwordx4 v228, s[44:47], s66 offen offset:0 nt lds\n"
    "buffer_load_dwordx4 v229, s[44:47], s66 offen offset:1024 nt lds\n"
    "buffer_load_dwordx4 v230, s[44:47], s66 offen offset:2048 nt lds\n"
    "buffer_load_dwordx4 v231, s[44:47], s66 offen offset:3072 nt lds\n"
    "s_mov_b32 m0, s80\n"
    "s_nop 0\n"
    "buffer_load_dwordx4 v232, s[48:51], s65 offen nt lds\n"
    "s_mov_b32 s65, 128\n"
    "s_mov_b32 s66, 458752\n"
    "s_mov_b32 m0, s77\n"
    "s_nop 0\n"
    "buffer_load_dwordx4 v228, s[40:43], s66 offen offset:0 nt lds\n"
    "buffer_load_dwordx4 v229, s[40:43], s66 offen offset:1024 nt lds\n"
    "buffer_load_dwordx4 v230, s[40:43], s66 offen offset:2048 nt lds\n"
    "buffer_load_dwordx4 v231, s[40:43], s66 offen offset:3072 nt lds\n"
    "s_mov_b32 m0, s79\n"
    "s_nop 0\n"
    "buffer_load_dwordx4 v228, s[44:47], s66 offen offset:0 nt lds\n"
    "buffer_load_dwordx4 v229, s[44:47], s66 offen offset:1024 nt lds\n"
    "buffer_load_dwordx4 v230, s[44:47], s66 offen offset:2048 nt lds\n"
    "buffer_load_dwordx4 v231, s[44:47], s66 offen offset:3072 nt lds\n"
    "s_mov_b32 m0, s81\n"
    "s_nop 0\n"
    "buffer_load_dwordx4 v232, s[48:51], s65 offen nt lds\n"
    "s_load_dwordx2 s[16:17], %1, 64\n"
    "s_load_dwordx2 s[18:19], %1, 72\n"
    "s_load_dwordx2 s[20:21], %1, 104\n"
    "v_and_b32 v243, 7, v241\n"
    "v_xor_b32 v243, v242, v243\n"
    "v_xor_b32 v244, 4, v243\n"
    "s_lshl_b32 s69, s61, 14\n"
    "v_lshlrev_b32 v245, 7, v241\n"
    "v_add_u32 v246, s69, v245\n"
    "v_lshl_add_u32 v208, v243, 4, v246\n"
    "v_lshl_add_u32 v209, v244, 4, v246\n"
    "s_lshl_b32 s69, s62, 11\n"
    "s_add_u32 s69, s69, 131072\n"
    "v_add_u32 v246, s69, v245\n"
    "v_lshl_add_u32 v218, v243, 4, v246\n"
    "v_lshl_add_u32 v219, v244, 4, v246\n"
    "v_lshrrev_b32 v243, 2, v241\n"
    "v_and_b32 v244, 3, v240\n"
    "v_lshl_add_u32 v245, v242, 3, v243\n"
    "v_lshlrev_b32 v246, 1, v242\n"
    "v_and_b32 v246, 3, v246\n"
    "v_lshl_or_b32 v246, v243, 2, v246\n"
    "v_and_b32 v247, 1, v244\n"
    "v_lshlrev_b32 v247, 3, v247\n"
    "v_lshl_add_u32 v247, v245, 8, v247\n"
    "v_add_u32 v247, 0x10000, v247\n"
    "s_lshl_b32 s69, s62, 3\n"
    "s_add_u32 s69, s69, 0\n"
    "v_lshrrev_b32 v248, 1, v244\n"
    "v_add_u32 v248, s69, v248\n"
    "v_xor_b32 v248, v248, v246\n"
    "v_lshl_add_u32 v210, v248, 4, v247\n"
    "s_lshl_b32 s69, s62, 3\n"
    "s_add_u32 s69, s69, 2\n"
    "v_lshrrev_b32 v248, 1, v244\n"
    "v_add_u32 v248, s69, v248\n"
    "v_xor_b32 v248, v248, v246\n"
    "v_lshl_add_u32 v212, v248, 4, v247\n"
    "s_lshl_b32 s69, s62, 3\n"
    "s_add_u32 s69, s69, 4\n"
    "v_lshrrev_b32 v248, 1, v244\n"
    "v_add_u32 v248, s69, v248\n"
    "v_xor_b32 v248, v248, v246\n"
    "v_lshl_add_u32 v214, v248, 4, v247\n"
    "s_lshl_b32 s69, s62, 3\n"
    "s_add_u32 s69, s69, 6\n"
    "v_lshrrev_b32 v248, 1, v244\n"
    "v_add_u32 v248, s69, v248\n"
    "v_xor_b32 v248, v248, v246\n"
    "v_lshl_add_u32 v216, v248, 4, v247\n"
    "v_lshl_add_u32 v245, v242, 3, v243\n"
    "v_add_u32 v245, 4, v245\n"
    "v_lshlrev_b32 v246, 1, v242\n"
    "v_add_u32 v246, 1, v246\n"
    "v_and_b32 v246, 3, v246\n"
    "v_lshl_or_b32 v246, v243, 2, v246\n"
    "v_and_b32 v247, 1, v244\n"
    "v_lshlrev_b32 v247, 3, v247\n"
    "v_lshl_add_u32 v247, v245, 8, v247\n"
    "v_add_u32 v247, 0x10000, v247\n"
    "s_lshl_b32 s69, s62, 3\n"
    "s_add_u32 s69, s69, 0\n"
    "v_lshrrev_b32 v248, 1, v244\n"
    "v_add_u32 v248, s69, v248\n"
    "v_xor_b32 v248, v248, v246\n"
    "v_lshl_add_u32 v211, v248, 4, v247\n"
    "s_lshl_b32 s69, s62, 3\n"
    "s_add_u32 s69, s69, 2\n"
    "v_lshrrev_b32 v248, 1, v244\n"
    "v_add_u32 v248, s69, v248\n"
    "v_xor_b32 v248, v248, v246\n"
    "v_lshl_add_u32 v213, v248, 4, v247\n"
    "s_lshl_b32 s69, s62, 3\n"
    "s_add_u32 s69, s69, 4\n"
    "v_lshrrev_b32 v248, 1, v244\n"
    "v_add_u32 v248, s69, v248\n"
    "v_xor_b32 v248, v248, v246\n"
    "v_lshl_add_u32 v215, v248, 4, v247\n"
    "s_lshl_b32 s69, s62, 3\n"
    "s_add_u32 s69, s69, 6\n"
    "v_lshrrev_b32 v248, 1, v244\n"
    "v_add_u32 v248, s69, v248\n"
    "v_xor_b32 v248, v248, v246\n"
    "v_lshl_add_u32 v217, v248, 4, v247\n"
    "s_cmp_eq_u32 s60, 0\n"
    "s_cbranch_scc0 LTS0_%=_0\n"
    "s_lshl_b32 s69, s8, 1\n"
    "s_add_u32 s69, s69, 0\n"
    "s_lshl_b32 s69, s69, 6\n"
    "v_add_u32 v243, s69, v240\n"
    "v_lshrrev_b32 v244, 3, v243\n"
    "v_and_b32 v244, 2047, v244\n"
    "v_lshlrev_b32 v244, 12, v244\n"
    "v_and_b32 v243, 7, v243\n"
    "v_add_u32 v243, 16, v243\n"
    "v_lshl_add_u32 v237, v243, 7, v244\n"
    "v_mov_b32 v254, 0\n"
    "v_mov_b32 v246, 0\n"
    "s_branch LTSE_%=_0\n"
    "LTS0_%=_0:\n"
    "s_cmp_eq_u32 s60, 1\n"
    "s_cbranch_scc0 LTS1_%=_0\n"
    "s_lshl_b32 s69, s8, 1\n"
    "s_add_u32 s69, s69, 0\n"
    "s_lshl_b32 s69, s69, 6\n"
    "v_add_u32 v243, s69, v240\n"
    "v_lshrrev_b32 v244, 3, v243\n"
    "v_and_b32 v244, 2047, v244\n"
    "v_lshlrev_b32 v244, 12, v244\n"
    "v_and_b32 v243, 7, v243\n"
    "v_add_u32 v243, 0, v243\n"
    "v_lshl_add_u32 v254, v243, 7, v244\n"
    "s_lshl_b32 s69, s8, 1\n"
    "s_add_u32 s69, s69, 1\n"
    "s_lshl_b32 s69, s69, 6\n"
    "v_add_u32 v243, s69, v240\n"
    "v_lshrrev_b32 v244, 3, v243\n"
    "v_and_b32 v244, 2047, v244\n"
    "v_lshlrev_b32 v244, 12, v244\n"
    "v_and_b32 v243, 7, v243\n"
    "v_add_u32 v243, 16, v243\n"
    "v_lshl_add_u32 v237, v243, 7, v244\n"
    "v_mov_b32 v246, 0\n"
    "s_branch LTSE_%=_0\n"
    "LTS1_%=_0:\n"
    "s_cmp_eq_u32 s60, 2\n"
    "s_cbranch_scc0 LTS2_%=_0\n"
    "s_lshl_b32 s69, s8, 1\n"
    "s_add_u32 s69, s69, 1\n"
    "s_lshl_b32 s69, s69, 6\n"
    "v_add_u32 v243, s69, v240\n"
    "v_lshrrev_b32 v244, 3, v243\n"
    "v_and_b32 v244, 2047, v244\n"
    "v_lshlrev_b32 v244, 12, v244\n"
    "v_and_b32 v243, 7, v243\n"
    "v_add_u32 v243, 0, v243\n"
    "v_lshl_add_u32 v254, v243, 7, v244\n"
    "s_lshl_b32 s69, s8, 1\n"
    "s_add_u32 s69, s69, 0\n"
    "s_lshl_b32 s69, s69, 6\n"
    "v_add_u32 v243, s69, v240\n"
    "v_lshrrev_b32 v244, 3, v243\n"
    "v_and_b32 v244, 2047, v244\n"
    "v_lshlrev_b32 v244, 12, v244\n"
    "v_and_b32 v243, 7, v243\n"
    "v_add_u32 v243, 8, v243\n"
    "v_lshl_add_u32 v246, v243, 7, v244\n"
    "s_lshl_b32 s69, s8, 6\n"
    "v_add_u32 v243, s69, v240\n"
    "v_lshrrev_b32 v244, 13, v243\n"
    "v_mul_u32_u24 v244, 1792, v244\n"
    "v_lshrrev_b32 v237, 4, v243\n"
    "v_and_b32 v237, 511, v237\n"
    "v_add_u32 v244, v244, v237\n"
    "v_add_u32 v244, 512, v244\n"
    "v_lshlrev_b32 v244, 11, v244\n"
    "v_and_b32 v237, 15, v243\n"
    "v_lshl_add_u32 v237, v237, 7, v244\n"
    "s_branch LTSE_%=_0\n"
    "LTS2_%=_0:\n"
    "s_cmp_eq_u32 s60, 3\n"
    "s_cbranch_scc0 LTS3_%=_0\n"
    "s_lshl_b32 s69, s8, 6\n"
    "v_add_u32 v243, s69, v240\n"
    "v_lshrrev_b32 v244, 13, v243\n"
    "v_mul_u32_u24 v244, 1792, v244\n"
    "v_lshrrev_b32 v254, 4, v243\n"
    "v_and_b32 v254, 511, v254\n"
    "v_add_u32 v244, v244, v254\n"
    "v_add_u32 v244, 0, v244\n"
    "v_lshlrev_b32 v244, 11, v244\n"
    "v_and_b32 v254, 15, v243\n"
    "v_lshl_add_u32 v254, v254, 7, v244\n"
    "s_lshl_b32 s69, s8, 1\n"
    "s_add_u32 s69, s69, 1\n"
    "s_lshl_b32 s69, s69, 6\n"
    "v_add_u32 v243, s69, v240\n"
    "v_lshrrev_b32 v244, 3, v243\n"
    "v_and_b32 v244, 2047, v244\n"
    "v_lshlrev_b32 v244, 12, v244\n"
    "v_and_b32 v243, 7, v243\n"
    "v_add_u32 v243, 8, v243\n"
    "v_lshl_add_u32 v246, v243, 7, v244\n"
    "v_mov_b32 v237, 0\n"
    "s_branch LTSE_%=_0\n"
    "LTS3_%=_0:\n"
    "LTSE_%=_0:\n"
    "s_waitcnt lgkmcnt(0)\n"
    "s_cmp_lt_u32 s8, 128\n"
    "s_cselect_b32 s96, s16, s18\n"
    "s_cselect_b32 s97, s17, s19\n"
    "s_mov_b32 s15, 0x20000\n"
    "s_mov_b32 s9, 0x40000000\n"
    "s_cmp_eq_u32 s60, 2\n"
    "s_cselect_b32 s9, 4294967292, s9\n"
    "s_cmp_eq_u32 s60, 3\n"
    "s_cselect_b32 s9, 4294967287, s9\n"
    "s_lshl_b32 s69, s64, 5\n"
    "s_add_u32 s69, s69, s63\n"
    "s_lshl_b32 s69, s69, 3\n"
    "s_add_u32 s69, s69, 0\n"
    "s_waitcnt vmcnt(18)\n"
    "s_barrier\n"
    "s_cmp_lg_u32 s60, 0\n"
    "s_cbranch_scc1 LLNP_%=_1\n"
    "v_mov_b32 v249, s69\n"
    "v_mov_b32 v252, s4\n"
    "v_mov_b32 v253, s5\n"
    "s_mov_b64 s[10:11], exec\n"
    "s_mov_b64 exec, 1\n"
    "global_store_dwordx2 v249, v[252:253], s[6:7] sc1\n"
    "s_mov_b64 exec, s[10:11]\n"
    "LLNP_%=_1:\n"
    "s_cmp_eq_u32 s60, 0\n"
    "s_cbranch_scc1 LTL_%=_2\n"
    "s_cmp_eq_u32 s60, 3\n"
    "s_cselect_b32 s12, s20, s96\n"
    "s_cselect_b32 s13, s21, s97\n"
    "s_mov_b32 s14, 8388608\n"
    "s_cselect_b32 s14, 7340032, s14\n"
    "buffer_load_dword v253, v254, s[12:15], 0 offen\n"
    "LTL_%=_2:\n"
    "s_mov_b32 s12, s96\n"
    "s_mov_b32 s13, s97\n"
    "s_mov_b32 s14, 0\n"
    "v_mov_b32 v254, v246\n"
    "s_cmp_lg_u32 s60, 0\n"
    "s_cbranch_scc1 LLNW_%=_3\n"
    "s_lshl_b32 s69, s64, 8\n"
    "s_add_u32 s69, s69, 0\n"
    "v_lshlrev_b32 v249, 3, v240\n"
    "v_add_u32 v249, s69, v249\n"
    "s_mov_b64 s[10:11], exec\n"
    "s_mov_b32 exec_lo, -1\n"
    "s_mov_b32 exec_hi, 0\n"
    "s_mov_b32 s3, 0\n"
    "global_load_dwordx2 v[252:253], v249, s[6:7] sc1\n"
    "LWP_%=_4:\n"
    "global_load_dwordx2 v[206:207], v249, s[6:7] sc1\n"
    "s_waitcnt vmcnt(1)\n"
    "v_cmp_ne_u64 vcc, s[4:5], v[252:253]\n"
    "s_nop 4\n"
    "s_cbranch_vccz LWD_%=_4\n"
    "global_load_dwordx2 v[252:253], v249, s[6:7] sc1\n"
    "s_waitcnt vmcnt(1)\n"
    "v_cmp_ne_u64 vcc, s[4:5], v[206:207]\n"
    "s_nop 4\n"
    "s_cbranch_vccz LWD_%=_4\n"
    "s_add_u32 s3, s3, 1\n"
    "s_cmp_lt_u32 s3, 0x10000\n"
    "s_cbranch_scc1 LWP_%=_4\n"
    "LWD_%=_4:\n"
    "s_mov_b64 exec, s[10:11]\n"
    "LLNW_%=_3:\n"
    "s_barrier\n"
    "s_mov_b32 s65, 0\n"
    "s_mov_b32 m0, s72\n"
    "s_nop 0\n"
    "buffer_load_dwordx4 v220, s[36:39], s65 offen offset:0 sc1 lds\n"
    "buffer_load_dwordx4 v221, s[36:39], s65 offen offset:1024 sc1 lds\n"
    "buffer_load_dwordx4 v222, s[36:39], s65 offen offset:2048 sc1 lds\n"
    "buffer_load_dwordx4 v223, s[36:39], s65 offen offset:3072 sc1 lds\n"
    "s_mov_b32 m0, s73\n"
    "s_nop 0\n"
    "buffer_load_dwordx4 v224, s[36:39], s65 offen offset:0 sc1 lds\n"
    "buffer_load_dwordx4 v225, s[36:39], s65 offen offset:1024 sc1 lds\n"
    "buffer_load_dwordx4 v226, s[36:39], s65 offen offset:2048 sc1 lds\n"
    "buffer_load_dwordx4 v227, s[36:39], s65 offen offset:3072 sc1 lds\n"
    "s_mov_b32 s65, 128\n"
    "s_mov_b32 m0, s74\n"
    "s_nop 0\n"
    "buffer_load_dwordx4 v220, s[36:39], s65 offen offset:0 sc1 lds\n"
    "buffer_load_dwordx4 v221, s[36:39], s65 offen offset:1024 sc1 lds\n"
    "buffer_load_dwordx4 v222, s[36:39], s65 offen offset:2048 sc1 lds\n"
    "buffer_load_dwordx4 v223, s[36:39], s65 offen offset:3072 sc1 lds\n"
    "s_mov_b32 m0, s75\n"
    "s_nop 0\n"
    "buffer_load_dwordx4 v224, s[36:39], s65 offen offset:0 sc1 lds\n"
    "buffer_load_dwordx4 v225, s[36:39], s65 offen offset:1024 sc1 lds\n"
    "buffer_load_dwordx4 v226, s[36:39], s65 offen offset:2048 sc1 lds\n"
    "buffer_load_dwordx4 v227, s[36:39], s65 offen offset:3072 sc1 lds\n"
    "s_mov_b32 s65, 256\n"
    "s_mov_b32 s66, 917504\n"
    "s_waitcnt vmcnt(12)\n"
    "s_barrier\n"
    "ds_read_b128 a[80:83], v208 offset:0\n"
    "ds_read_b128 a[84:87], v209 offset:0\n"
    "ds_read_b128 a[88:91], v208 offset:2048\n"
    "ds_read_b128 a[92:95], v209 offset:2048\n"
    "ds_read_b128 a[96:99], v208 offset:4096\n"
    "ds_read_b128 a[100:103], v209 offset:4096\n"
    "ds_read_b128 a[104:107], v208 offset:6144\n"
    "ds_read_b128 a[108:111], v209 offset:6144\n"
    "ds_read_b64_tr_b16 a[144:145], v210 offset:0\n"
    "ds_read_b64_tr_b16 a[146:147], v211 offset:0\n"
    "ds_read_b64_tr_b16 a[148:149], v210 offset:8192\n"
    "ds_read_b64_tr_b16 a[150:151], v211 offset:8192\n"
    "ds_read_b64_tr_b16 a[152:153], v212 offset:0\n"
    "ds_read_b64_tr_b16 a[154:155], v213 offset:0\n"
    "ds_read_b64_tr_b16 a[156:157], v212 offset:8192\n"
    "ds_read_b64_tr_b16 a[158:159], v213 offset:8192\n"
    "ds_read_b64_tr_b16 a[160:161], v214 offset:0\n"
    "ds_read_b64_tr_b16 a[162:163], v215 offset:0\n"
    "ds_read_b64_tr_b16 a[164:165], v214 offset:8192\n"
    "ds_read_b64_tr_b16 a[166:167], v215 offset:8192\n"
    "ds_read_b64_tr_b16 a[168:169], v216 offset:0\n"
    "ds_read_b64_tr_b16 a[170:171], v217 offset:0\n"
    "ds_read_b64_tr_b16 a[172:173], v216 offset:8192\n"
    "ds_read_b64_tr_b16 a[174:175], v217 offset:8192\n"
    "ds_read_b64_tr_b16 a[176:177], v210 offset:16384\n"
    "ds_read_b64_tr_b16 a[178:179], v211 offset:16384\n"
    "ds_read_b64_tr_b16 a[180:181], v210 offset:24576\n"
    "ds_read_b64_tr_b16 a[182:183], v211 offset:24576\n"
    "ds_read_b64_tr_b16 a[184:185], v212 offset:16384\n"
    "ds_read_b64_tr_b16 a[186:187], v213 offset:16384\n"
    "ds_read_b64_tr_b16 a[188:189], v212 offset:24576\n"
    "ds_read_b64_tr_b16 a[190:191], v213 offset:24576\n"
    "ds_read_b64_tr_b16 a[192:193], v214 offset:16384\n"
    "ds_read_b64_tr_b16 a[194:195], v215 offset:16384\n"
    "ds_read_b64_tr_b16 a[196:197], v214 offset:24576\n"
    "ds_read_b64_tr_b16 a[198:199], v215 offset:24576\n"
    "ds_read_b64_tr_b16 a[200:201], v216 offset:16384\n"
    "ds_read_b64_tr_b16 a[202:203], v217 offset:16384\n"
    "ds_read_b64_tr_b16 a[204:205], v216 offset:24576\n"
    "ds_read_b64_tr_b16 a[206:207], v217 offset:24576\n"
    "ds_read_b128 a[208:211], v218 offset:0\n"
    "ds_read_b128 a[212:215], v219 offset:0\n"
    "s_waitcnt lgkmcnt(15)\n"
    "v_mfma_f32_16x16x32_bf16 v[0:3], a[144:147], a[80:83], 0\n"
    "v_mov_b32 v112, 0\n"
    "v_mov_b32 v113, 0\n"
    "v_mov_b32 v114, 0\n"
    "v_mfma_f32_16x16x32_bf16 v[0:3], a[148:151], a[84:87], v[0:3]\n"
    "v_mov_b32 v115, 0\n"
    "v_mov_b32 v116, 0\n"
    "v_mfma_f32_16x16x32_bf16 v[28:31], a[144:147], a[88:91], 0\n"
    "v_mov_b32 v117, 0\n"
    "v_mov_b32 v118, 0\n"
    "v_mfma_f32_16x16x32_bf16 v[28:31], a[148:151], a[92:95], v[28:31]\n"
    "v_mov_b32 v119, 0\n"
    "v_mov_b32 v120, 0\n"
    "v_mfma_f32_16x16x32_bf16 v[56:59], a[144:147], a[96:99], 0\n"
    "v_mov_b32 v121, 0\n"
    "v_mov_b32 v122, 0\n"
    "v_mfma_f32_16x16x32_bf16 v[56:59], a[148:151], a[100:103], v[56:59]\n"
    "v_mov_b32 v123, 0\n"
    "v_mov_b32 v124, 0\n"
    "v_mov_b32 v125, 0\n"
    "v_mfma_f32_16x16x32_bf16 v[84:87], a[144:147], a[104:107], 0\n"
    "v_mov_b32 v126, 0\n"
    "v_mov_b32 v127, 0\n"
    "v_mfma_f32_16x16x32_bf16 v[84:87], a[148:151], a[108:111], v[84:87]\n"
    "v_mov_b32 v128, 0\n"
    "v_mov_b32 v129, 0\n"
    "v_mfma_f32_16x16x32_bf16 v[4:7], a[152:155], a[80:83], 0\n"
    "v_mov_b32 v130, 0\n"
    "v_mov_b32 v131, 0\n"
    "v_mfma_f32_16x16x32_bf16 v[4:7], a[156:159], a[84:87], v[4:7]\n"
    "v_mov_b32 v132, 0\n"
    "v_mov_b32 v133, 0\n"
    "v_mfma_f32_16x16x32_bf16 v[32:35], a[152:155], a[88:91], 0\n"
    "v_mov_b32 v134, 0\n"
    "v_mov_b32 v135, 0\n"
    "v_mfma_f32_16x16x32_bf16 v[32:35], a[156:159], a[92:95], v[32:35]\n"
    "s_waitcnt vmcnt(8) lgkmcnt(0)\n"
    "s_barrier\n"
    "buffer_load_dword v253, v254, s[12:15], 0 offen\n"
    "v_accvgpr_write_b32 a64, 0\n"
    "v_accvgpr_write_b32 a65, 0\n"
    "v_accvgpr_write_b32 a66, 0\n"
    "v_mfma_f32_16x16x32_bf16 v[60:63], a[152:155], a[96:99], 0\n"
    "ds_read_b128 a[112:115], v208 offset:8192\n"
    "v_accvgpr_write_b32 a67, 0\n"
    "v_accvgpr_write_b32 a48, 0\n"
    "v_mfma_f32_16x16x32_bf16 v[60:63], a[156:159], a[100:103], v[60:63]\n"
    "s_mov_b32 m0, s76\n"
    "s_nop 0\n"
    "buffer_load_dwordx4 v228, s[40:43], s66 offen offset:0 nt lds\n"
    "v_accvgpr_write_b32 a49, 0\n"
    "v_accvgpr_write_b32 a50, 0\n"
    "v_mfma_f32_16x16x32_bf16 v[88:91], a[152:155], a[104:107], 0\n"
    "ds_read_b128 a[116:119], v209 offset:8192\n"
    "v_accvgpr_write_b32 a51, 0\n"
    "v_accvgpr_write_b32 a16, 0\n"
    "v_mfma_f32_16x16x32_bf16 v[88:91], a[156:159], a[108:111], v[88:91]\n"
    "v_accvgpr_write_b32 a17, 0\n"
    "v_accvgpr_write_b32 a18, 0\n"
    "v_mfma_f32_16x16x32_bf16 v[8:11], a[160:163], a[80:83], 0\n"
    "ds_read_b128 a[120:123], v208 offset:10240\n"
    "v_accvgpr_write_b32 a19, 0\n"
    "v_mov_b32 v136, 0\n"
    "v_mov_b32 v137, 0\n"
    "v_mfma_f32_16x16x32_bf16 v[8:11], a[164:167], a[84:87], v[8:11]\n"
    "v_mov_b32 v138, 0\n"
    "v_mov_b32 v139, 0\n"
    "v_mfma_f32_16x16x32_bf16 v[36:39], a[160:163], a[88:91], 0\n"
    "ds_read_b128 a[124:127], v209 offset:10240\n"
    "v_mov_b32 v140, 0\n"
    "v_mov_b32 v141, 0\n"
    "v_mfma_f32_16x16x32_bf16 v[36:39], a[164:167], a[92:95], v[36:39]\n"
    "buffer_load_dwordx4 v229, s[40:43], s66 offen offset:1024 nt lds\n"
    "v_mov_b32 v142, 0\n"
    "v_mov_b32 v143, 0\n"
    "v_mfma_f32_16x16x32_bf16 v[64:67], a[160:163], a[96:99], 0\n"
    "ds_read_b128 a[128:131], v208 offset:12288\n"
    "v_mov_b32 v144, 0\n"
    "v_mov_b32 v145, 0\n"
    "v_mfma_f32_16x16x32_bf16 v[64:67], a[164:167], a[100:103], v[64:67]\n"
    "v_mov_b32 v146, 0\n"
    "v_mov_b32 v147, 0\n"
    "v_mfma_f32_16x16x32_bf16 v[92:95], a[160:163], a[104:107], 0\n"
    "ds_read_b128 a[132:135], v209 offset:12288\n"
    "v_mov_b32 v148, 0\n"
    "v_mov_b32 v149, 0\n"
    "v_mov_b32 v150, 0\n"
    "v_mfma_f32_16x16x32_bf16 v[92:95], a[164:167], a[108:111], v[92:95]\n"
    "v_mov_b32 v151, 0\n"
    "v_mov_b32 v152, 0\n"
    "v_mfma_f32_16x16x32_bf16 v[12:15], a[168:171], a[80:83], 0\n"
    "ds_read_b128 a[136:139], v208 offset:14336\n"
    "v_mov_b32 v153, 0\n"
    "v_mov_b32 v154, 0\n"
    "v_mfma_f32_16x16x32_bf16 v[12:15], a[172:175], a[84:87], v[12:15]\n"
    "v_mov_b32 v155, 0\n"
    "v_mov_b32 v156, 0\n"
    "v_mfma_f32_16x16x32_bf16 v[40:43], a[168:171], a[88:91], 0\n"
    "ds_read_b128 a[140:143], v209 offset:14336\n"
    "buffer_load_dwordx4 v230, s[40:43], s66 offen offset:2048 nt lds\n"
    "v_mov_b32 v157, 0\n"
    "v_mov_b32 v158, 0\n"
    "v_mfma_f32_16x16x32_bf16 v[40:43], a[172:175], a[92:95], v[40:43]\n"
    "v_mov_b32 v159, 0\n"
    "v_accvgpr_write_b32 a68, 0\n"
    "v_accvgpr_write_b32 a69, 0\n"
    "v_mfma_f32_16x16x32_bf16 v[68:71], a[168:171], a[96:99], 0\n"
    "v_accvgpr_write_b32 a70, 0\n"
    "v_accvgpr_write_b32 a71, 0\n"
    "v_mfma_f32_16x16x32_bf16 v[68:71], a[172:175], a[100:103], v[68:71]\n"
    "v_accvgpr_write_b32 a52, 0\n"
    "v_accvgpr_write_b32 a53, 0\n"
    "v_mfma_f32_16x16x32_bf16 v[96:99], a[168:171], a[104:107], 0\n"
    "v_accvgpr_write_b32 a54, 0\n"
    "v_accvgpr_write_b32 a55, 0\n"
    "v_mfma_f32_16x16x32_bf16 v[96:99], a[172:175], a[108:111], v[96:99]\n"
    "v_accvgpr_write_b32 a20, 0\n"
    "v_accvgpr_write_b32 a21, 0\n"
    "v_mfma_f32_16x16x32_bf16 v[16:19], a[176:179], a[80:83], 0\n"
    "buffer_load_dwordx4 v231, s[40:43], s66 offen offset:3072 nt lds\n"
    "v_accvgpr_write_b32 a22, 0\n"
    "v_accvgpr_write_b32 a23, 0\n"
    "v_mfma_f32_16x16x32_bf16 v[16:19], a[180:183], a[84:87], v[16:19]\n"
    "v_mov_b32 v160, 0\n"
    "v_mov_b32 v161, 0\n"
    "v_mov_b32 v162, 0\n"
    "v_mfma_f32_16x16x32_bf16 v[44:47], a[176:179], a[88:91], 0\n"
    "v_mov_b32 v163, 0\n"
    "v_mov_b32 v164, 0\n"
    "v_mfma_f32_16x16x32_bf16 v[44:47], a[180:183], a[92:95], v[44:47]\n"
    "v_mov_b32 v165, 0\n"
    "v_mov_b32 v166, 0\n"
    "v_mfma_f32_16x16x32_bf16 v[72:75], a[176:179], a[96:99], 0\n"
    "v_mov_b32 v167, 0\n"
    "v_mov_b32 v168, 0\n"
    "v_mfma_f32_16x16x32_bf16 v[72:75], a[180:183], a[100:103], v[72:75]\n"
    "v_mov_b32 v169, 0\n"
    "v_mov_b32 v170, 0\n"
    "v_mfma_f32_16x16x32_bf16 v[100:103], a[176:179], a[104:107], 0\n"
    "v_mov_b32 v171, 0\n"
    "v_mov_b32 v172, 0\n"
    "v_mov_b32 v173, 0\n"
    "v_mfma_f32_16x16x32_bf16 v[100:103], a[180:183], a[108:111], v[100:103]\n"
    "s_mov_b32 m0, s78\n"
    "s_nop 0\n"
    "buffer_load_dwordx4 v228, s[44:47], s66 offen offset:0 nt lds\n"
    "v_mov_b32 v174, 0\n"
    "v_mov_b32 v175, 0\n"
    "v_mfma_f32_16x16x32_bf16 v[20:23], a[184:187], a[80:83], 0\n"
    "s_add_u32 s9, s9, 1\n"
    "s_cmp_eq_u32 s9, 0\n"
    "s_cselect_b32 s14, 8388608, 0\n"
    "v_mov_b32 v176, 0\n"
    "v_mov_b32 v177, 0\n"
    "v_mfma_f32_16x16x32_bf16 v[20:23], a[188:191], a[84:87], v[20:23]\n"
    "v_mov_b32 v178, 0\n"
    "v_mov_b32 v179, 0\n"
    "v_mfma_f32_16x16x32_bf16 v[48:51], a[184:187], a[88:91], 0\n"
    "v_mov_b32 v180, 0\n"
    "v_mov_b32 v181, 0\n"
    "v_mfma_f32_16x16x32_bf16 v[48:51], a[188:191], a[92:95], v[48:51]\n"
    "v_mov_b32 v182, 0\n"
    "v_mov_b32 v183, 0\n"
    "v_mfma_f32_16x16x32_bf16 v[76:79], a[184:187], a[96:99], 0\n"
    "v_accvgpr_write_b32 a72, 0\n"
    "v_accvgpr_write_b32 a73, 0\n"
    "v_accvgpr_write_b32 a74, 0\n"
    "v_mfma_f32_16x16x32_bf16 v[76:79], a[188:191], a[100:103], v[76:79]\n"
    "buffer_load_dwordx4 v229, s[44:47], s66 offen offset:1024 nt lds\n"
    "v_accvgpr_write_b32 a75, 0\n"
    "v_accvgpr_write_b32 a56, 0\n"
    "v_mfma_f32_16x16x32_bf16 v[104:107], a[184:187], a[104:107], 0\n"
    "v_accvgpr_write_b32 a57, 0\n"
    "v_accvgpr_write_b32 a58, 0\n"
    "v_mfma_f32_16x16x32_bf16 v[104:107], a[188:191], a[108:111], v[104:107]\n"
    "v_accvgpr_write_b32 a59, 0\n"
    "v_accvgpr_write_b32 a24, 0\n"
    "v_mfma_f32_16x16x32_bf16 v[24:27], a[192:195], a[80:83], 0\n"
    "v_accvgpr_write_b32 a25, 0\n"
    "v_accvgpr_write_b32 a26, 0\n"
    "v_mfma_f32_16x16x32_bf16 v[24:27], a[196:199], a[84:87], v[24:27]\n"
    "v_accvgpr_write_b32 a27, 0\n"
    "v_mov_b32 v184, 0\n"
    "v_mov_b32 v185, 0\n"
    "v_mfma_f32_16x16x32_bf16 v[52:55], a[192:195], a[88:91], 0\n"
    "v_mov_b32 v186, 0\n"
    "v_mov_b32 v187, 0\n"
    "v_mfma_f32_16x16x32_bf16 v[52:55], a[196:199], a[92:95], v[52:55]\n"
    "v_mov_b32 v188, 0\n"
    "v_mov_b32 v189, 0\n"
    "v_mfma_f32_16x16x32_bf16 v[80:83], a[192:195], a[96:99], 0\n"
    "buffer_load_dwordx4 v230, s[44:47], s66 offen offset:2048 nt lds\n"
    "v_mov_b32 v190, 0\n"
    "v_mov_b32 v191, 0\n"
    "v_mfma_f32_16x16x32_bf16 v[80:83], a[196:199], a[100:103], v[80:83]\n"
    "v_mov_b32 v192, 0\n"
    "v_mov_b32 v193, 0\n"
    "v_mfma_f32_16x16x32_bf16 v[108:111], a[192:195], a[104:107], 0\n"
    "v_mov_b32 v194, 0\n"
    "v_mov_b32 v195, 0\n"
    "v_mfma_f32_16x16x32_bf16 v[108:111], a[196:199], a[108:111], v[108:111]\n"
    "v_mov_b32 v196, 0\n"
    "v_mov_b32 v197, 0\n"
    "v_mov_b32 v198, 0\n"
    "v_mfma_f32_16x16x32_bf16 a[32:35], a[200:203], a[80:83], 0\n"
    "v_mov_b32 v199, 0\n"
    "v_mov_b32 v200, 0\n"
    "v_mfma_f32_16x16x32_bf16 a[32:35], a[204:207], a[84:87], a[32:35]\n"
    "v_mov_b32 v201, 0\n"
    "v_mov_b32 v202, 0\n"
    "v_mfma_f32_16x16x32_bf16 a[36:39], a[200:203], a[88:91], 0\n"
    "buffer_load_dwordx4 v231, s[44:47], s66 offen offset:3072 nt lds\n"
    "v_mov_b32 v203, 0\n"
    "v_mov_b32 v204, 0\n"
    "v_mfma_f32_16x16x32_bf16 a[36:39], a[204:207], a[92:95], a[36:39]\n"
    "v_mov_b32 v205, 0\n"
    "v_mov_b32 v206, 0\n"
    "v_mfma_f32_16x16x32_bf16 a[40:43], a[200:203], a[96:99], 0\n"
    "v_mov_b32 v207, 0\n"
    "v_accvgpr_write_b32 a76, 0\n"
    "v_accvgpr_write_b32 a77, 0\n"
    "v_mfma_f32_16x16x32_bf16 a[40:43], a[204:207], a[100:103], a[40:43]\n"
    "v_accvgpr_write_b32 a78, 0\n"
    "v_accvgpr_write_b32 a79, 0\n"
    "v_mfma_f32_16x16x32_bf16 a[44:47], a[200:203], a[104:107], 0\n"
    "v_accvgpr_write_b32 a60, 0\n"
    "v_accvgpr_write_b32 a61, 0\n"
    "v_mfma_f32_16x16x32_bf16 a[44:47], a[204:207], a[108:111], a[44:47]\n"
    "v_accvgpr_write_b32 a62, 0\n"
    "v_accvgpr_write_b32 a63, 0\n"
    "v_mfma_f32_16x16x32_bf16 a[0:3], a[80:83], a[208:211], 0\n"
    "v_accvgpr_write_b32 a28, 0\n"
    "v_accvgpr_write_b32 a29, 0\n"
    "v_mfma_f32_16x16x32_bf16 a[0:3], a[84:87], a[212:215], a[0:3]\n"
    "s_mov_b32 m0, s80\n"
    "s_nop 0\n"
    "buffer_load_dwordx4 v232, s[48:51], s65 offen nt lds\n"
    "v_accvgpr_write_b32 a30, 0\n"
    "v_accvgpr_write_b32 a31, 0\n"
    "v_mfma_f32_16x16x32_bf16 a[4:7], a[88:91], a[208:211], 0\n"
    "v_mfma_f32_16x16x32_bf16 a[4:7], a[92:95], a[212:215], a[4:7]\n"
    "v_mfma_f32_16x16x32_bf16 a[8:11], a[96:99], a[208:211], 0\n"
    "v_mfma_f32_16x16x32_bf16 a[8:11], a[100:103], a[212:215], a[8:11]\n"
    "v_mfma_f32_16x16x32_bf16 a[12:15], a[104:107], a[208:211], 0\n"
    "v_mfma_f32_16x16x32_bf16 a[12:15], a[108:111], a[212:215], a[12:15]\n"
    "s_mov_b32 s67, 7\n"
    "LG1_loop_%=:\n"
    "s_waitcnt vmcnt(14) lgkmcnt(0)\n"
    "s_barrier\n"
    "v_mfma_f32_16x16x32_bf16 v[112:115], a[144:147], a[112:115], v[112:115]\n"
    "ds_read_b128 a[80:83], v208 offset:32768\n"
    "v_mfma_f32_16x16x32_bf16 v[112:115], a[148:151], a[116:119], v[112:115]\n"
    "ds_read_b128 a[84:87], v209 offset:32768\n"
    "v_mfma_f32_16x16x32_bf16 v[136:139], a[144:147], a[120:123], v[136:139]\n"
    "ds_read_b128 a[88:91], v208 offset:34816\n"
    "s_mov_b32 m0, s72\n"
    "s_nop 0\n"
    "buffer_load_dwordx4 v220, s[36:39], s65 offen offset:0 sc1 lds\n"
    "v_mfma_f32_16x16x32_bf16 v[136:139], a[148:151], a[124:127], v[136:139]\n"
    "ds_read_b128 a[92:95], v209 offset:34816\n"
    "v_mfma_f32_16x16x32_bf16 v[160:163], a[144:147], a[128:131], v[160:163]\n"
    "ds_read_b128 a[96:99], v208 offset:36864\n"
    "v_mfma_f32_16x16x32_bf16 v[160:163], a[148:151], a[132:135], v[160:163]\n"
    "ds_read_b128 a[100:103], v209 offset:36864\n"
    "v_mfma_f32_16x16x32_bf16 v[184:187], a[144:147], a[136:139], v[184:187]\n"
    "ds_read_b128 a[104:107], v208 offset:38912\n"
    "v_mfma_f32_16x16x32_bf16 v[184:187], a[148:151], a[140:143], v[184:187]\n"
    "ds_read_b128 a[108:111], v209 offset:38912\n"
    "v_mfma_f32_16x16x32_bf16 v[116:119], a[152:155], a[112:115], v[116:119]\n"
    "ds_read_b64_tr_b16 a[144:145], v210 offset:32768\n"
    "v_mfma_f32_16x16x32_bf16 v[116:119], a[156:159], a[116:119], v[116:119]\n"
    "v_mfma_f32_16x16x32_bf16 v[140:143], a[152:155], a[120:123], v[140:143]\n"
    "ds_read_b64_tr_b16 a[146:147], v211 offset:32768\n"
    "buffer_load_dwordx4 v221, s[36:39], s65 offen offset:1024 sc1 lds\n"
    "v_mfma_f32_16x16x32_bf16 v[140:143], a[156:159], a[124:127], v[140:143]\n"
    "v_mfma_f32_16x16x32_bf16 v[164:167], a[152:155], a[128:131], v[164:167]\n"
    "ds_read_b64_tr_b16 a[148:149], v210 offset:40960\n"
    "v_mfma_f32_16x16x32_bf16 v[164:167], a[156:159], a[132:135], v[164:167]\n"
    "v_mfma_f32_16x16x32_bf16 v[188:191], a[152:155], a[136:139], v[188:191]\n"
    "ds_read_b64_tr_b16 a[150:151], v211 offset:40960\n"
    "v_mfma_f32_16x16x32_bf16 v[188:191], a[156:159], a[140:143], v[188:191]\n"
    "v_mfma_f32_16x16x32_bf16 v[120:123], a[160:163], a[112:115], v[120:123]\n"
    "ds_read_b64_tr_b16 a[152:153], v212 offset:32768\n"
    "v_mfma_f32_16x16x32_bf16 v[120:123], a[164:167], a[116:119], v[120:123]\n"
    "v_mfma_f32_16x16x32_bf16 v[144:147], a[160:163], a[120:123], v[144:147]\n"
    "ds_read_b64_tr_b16 a[154:155], v213 offset:32768\n"
    "v_mfma_f32_16x16x32_bf16 v[144:147], a[164:167], a[124:127], v[144:147]\n"
    "buffer_load_dwordx4 v222, s[36:39], s65 offen offset:2048 sc1 lds\n"
    "v_mfma_f32_16x16x32_bf16 v[168:171], a[160:163], a[128:131], v[168:171]\n"
    "ds_read_b64_tr_b16 a[156:157], v212 offset:40960\n"
    "v_mfma_f32_16x16x32_bf16 v[168:171], a[164:167], a[132:135], v[168:171]\n"
    "v_mfma_f32_16x16x32_bf16 v[192:195], a[160:163], a[136:139], v[192:195]\n"
    "ds_read_b64_tr_b16 a[158:159], v213 offset:40960\n"
    "v_mfma_f32_16x16x32_bf16 v[192:195], a[164:167], a[140:143], v[192:195]\n"
    "v_mfma_f32_16x16x32_bf16 v[124:127], a[168:171], a[112:115], v[124:127]\n"
    "ds_read_b64_tr_b16 a[160:161], v214 offset:32768\n"
    "v_mfma_f32_16x16x32_bf16 v[124:127], a[172:175], a[116:119], v[124:127]\n"
    "v_mfma_f32_16x16x32_bf16 v[148:151], a[168:171], a[120:123], v[148:151]\n"
    "ds_read_b64_tr_b16 a[162:163], v215 offset:32768\n"
    "v_mfma_f32_16x16x32_bf16 v[148:151], a[172:175], a[124:127], v[148:151]\n"
    "buffer_load_dwordx4 v223, s[36:39], s65 offen offset:3072 sc1 lds\n"
    "v_mfma_f32_16x16x32_bf16 v[172:175], a[168:171], a[128:131], v[172:175]\n"
    "ds_read_b64_tr_b16 a[164:165], v214 offset:40960\n"
    "v_mfma_f32_16x16x32_bf16 v[172:175], a[172:175], a[132:135], v[172:175]\n"
    "v_mfma_f32_16x16x32_bf16 v[196:199], a[168:171], a[136:139], v[196:199]\n"
    "ds_read_b64_tr_b16 a[166:167], v215 offset:40960\n"
    "v_mfma_f32_16x16x32_bf16 v[196:199], a[172:175], a[140:143], v[196:199]\n"
    "v_mfma_f32_16x16x32_bf16 v[128:131], a[176:179], a[112:115], v[128:131]\n"
    "ds_read_b64_tr_b16 a[168:169], v216 offset:32768\n"
    "v_mfma_f32_16x16x32_bf16 v[128:131], a[180:183], a[116:119], v[128:131]\n"
    "v_mfma_f32_16x16x32_bf16 v[152:155], a[176:179], a[120:123], v[152:155]\n"
    "ds_read_b64_tr_b16 a[170:171], v217 offset:32768\n"
    "v_mfma_f32_16x16x32_bf16 v[152:155], a[180:183], a[124:127], v[152:155]\n"
    "v_mfma_f32_16x16x32_bf16 v[176:179], a[176:179], a[128:131], v[176:179]\n"
    "ds_read_b64_tr_b16 a[172:173], v216 offset:40960\n"
    "s_mov_b32 m0, s73\n"
    "s_nop 0\n"
    "buffer_load_dwordx4 v224, s[36:39], s65 offen offset:0 sc1 lds\n"
    "v_mfma_f32_16x16x32_bf16 v[176:179], a[180:183], a[132:135], v[176:179]\n"
    "v_mfma_f32_16x16x32_bf16 v[200:203], a[176:179], a[136:139], v[200:203]\n"
    "ds_read_b64_tr_b16 a[174:175], v217 offset:40960\n"
    "v_mfma_f32_16x16x32_bf16 v[200:203], a[180:183], a[140:143], v[200:203]\n"
    "v_mfma_f32_16x16x32_bf16 v[132:135], a[184:187], a[112:115], v[132:135]\n"
    "ds_read_b64_tr_b16 a[176:177], v210 offset:49152\n"
    "v_mfma_f32_16x16x32_bf16 v[132:135], a[188:191], a[116:119], v[132:135]\n"
    "v_mfma_f32_16x16x32_bf16 v[156:159], a[184:187], a[120:123], v[156:159]\n"
    "ds_read_b64_tr_b16 a[178:179], v211 offset:49152\n"
    "v_mfma_f32_16x16x32_bf16 v[156:159], a[188:191], a[124:127], v[156:159]\n"
    "v_mfma_f32_16x16x32_bf16 v[180:183], a[184:187], a[128:131], v[180:183]\n"
    "ds_read_b64_tr_b16 a[180:181], v210 offset:57344\n"
    "buffer_load_dwordx4 v225, s[36:39], s65 offen offset:1024 sc1 lds\n"
    "v_mfma_f32_16x16x32_bf16 v[180:183], a[188:191], a[132:135], v[180:183]\n"
    "v_mfma_f32_16x16x32_bf16 v[204:207], a[184:187], a[136:139], v[204:207]\n"
    "ds_read_b64_tr_b16 a[182:183], v211 offset:57344\n"
    "v_mfma_f32_16x16x32_bf16 v[204:207], a[188:191], a[140:143], v[204:207]\n"
    "v_mfma_f32_16x16x32_bf16 a[64:67], a[192:195], a[112:115], a[64:67]\n"
    "ds_read_b64_tr_b16 a[184:185], v212 offset:49152\n"
    "v_mfma_f32_16x16x32_bf16 a[64:67], a[196:199], a[116:119], a[64:67]\n"
    "v_mfma_f32_16x16x32_bf16 a[68:71], a[192:195], a[120:123], a[68:71]\n"
    "ds_read_b64_tr_b16 a[186:187], v213 offset:49152\n"
    "v_mfma_f32_16x16x32_bf16 a[68:71], a[196:199], a[124:127], a[68:71]\n"
    "v_mfma_f32_16x16x32_bf16 a[72:75], a[192:195], a[128:131], a[72:75]\n"
    "ds_read_b64_tr_b16 a[188:189], v212 offset:57344\n"
    "v_mfma_f32_16x16x32_bf16 a[72:75], a[196:199], a[132:135], a[72:75]\n"
    "buffer_load_dwordx4 v226, s[36:39], s65 offen offset:2048 sc1 lds\n"
    "v_mfma_f32_16x16x32_bf16 a[76:79], a[192:195], a[136:139], a[76:79]\n"
    "ds_read_b64_tr_b16 a[190:191], v213 offset:57344\n"
    "v_mfma_f32_16x16x32_bf16 a[76:79], a[196:199], a[140:143], a[76:79]\n"
    "v_mfma_f32_16x16x32_bf16 a[48:51], a[200:203], a[112:115], a[48:51]\n"
    "ds_read_b64_tr_b16 a[192:193], v214 offset:49152\n"
    "v_mfma_f32_16x16x32_bf16 a[48:51], a[204:207], a[116:119], a[48:51]\n"
    "v_mfma_f32_16x16x32_bf16 a[52:55], a[200:203], a[120:123], a[52:55]\n"
    "ds_read_b64_tr_b16 a[194:195], v215 offset:49152\n"
    "v_mfma_f32_16x16x32_bf16 a[52:55], a[204:207], a[124:127], a[52:55]\n"
    "v_mfma_f32_16x16x32_bf16 a[56:59], a[200:203], a[128:131], a[56:59]\n"
    "ds_read_b64_tr_b16 a[196:197], v214 offset:57344\n"
    "v_mfma_f32_16x16x32_bf16 a[56:59], a[204:207], a[132:135], a[56:59]\n"
    "buffer_load_dwordx4 v227, s[36:39], s65 offen offset:3072 sc1 lds\n"
    "v_mfma_f32_16x16x32_bf16 a[60:63], a[200:203], a[136:139], a[60:63]\n"
    "ds_read_b64_tr_b16 a[198:199], v215 offset:57344\n"
    "v_mfma_f32_16x16x32_bf16 a[60:63], a[204:207], a[140:143], a[60:63]\n"
    "v_mfma_f32_16x16x32_bf16 a[16:19], a[112:115], a[208:211], a[16:19]\n"
    "ds_read_b64_tr_b16 a[200:201], v216 offset:49152\n"
    "v_mfma_f32_16x16x32_bf16 a[16:19], a[116:119], a[212:215], a[16:19]\n"
    "v_mfma_f32_16x16x32_bf16 a[20:23], a[120:123], a[208:211], a[20:23]\n"
    "ds_read_b64_tr_b16 a[202:203], v217 offset:49152\n"
    "v_mfma_f32_16x16x32_bf16 a[20:23], a[124:127], a[212:215], a[20:23]\n"
    "v_mfma_f32_16x16x32_bf16 a[24:27], a[128:131], a[208:211], a[24:27]\n"
    "ds_read_b64_tr_b16 a[204:205], v216 offset:57344\n"
    "v_mfma_f32_16x16x32_bf16 a[24:27], a[132:135], a[212:215], a[24:27]\n"
    "v_mfma_f32_16x16x32_bf16 a[28:31], a[136:139], a[208:211], a[28:31]\n"
    "ds_read_b64_tr_b16 a[206:207], v217 offset:57344\n"
    "s_add_u32 s65, s65, 128\n"
    "s_add_u32 s66, s66, 458752\n"
    "s_cmp_ge_u32 s65, 2048\n"
    "s_cselect_b32 s38, 0, s38\n"
    "s_cselect_b32 s42, 0, s42\n"
    "s_cselect_b32 s46, 0, s46\n"
    "s_cselect_b32 s50, 0, s50\n"
    "v_mfma_f32_16x16x32_bf16 a[28:31], a[140:143], a[212:215], a[28:31]\n"
    "s_waitcnt lgkmcnt(15)\n"
    "v_mfma_f32_16x16x32_bf16 v[0:3], a[144:147], a[80:83], v[0:3]\n"
    "ds_read_b128 a[208:211], v218 offset:4096\n"
    "v_mfma_f32_16x16x32_bf16 v[0:3], a[148:151], a[84:87], v[0:3]\n"
    "ds_read_b128 a[212:215], v219 offset:4096\n"
    "v_mfma_f32_16x16x32_bf16 v[28:31], a[144:147], a[88:91], v[28:31]\n"
    "v_mfma_f32_16x16x32_bf16 v[28:31], a[148:151], a[92:95], v[28:31]\n"
    "v_mfma_f32_16x16x32_bf16 v[56:59], a[144:147], a[96:99], v[56:59]\n"
    "v_mfma_f32_16x16x32_bf16 v[56:59], a[148:151], a[100:103], v[56:59]\n"
    "v_mfma_f32_16x16x32_bf16 v[84:87], a[144:147], a[104:107], v[84:87]\n"
    "v_mfma_f32_16x16x32_bf16 v[84:87], a[148:151], a[108:111], v[84:87]\n"
    "v_mfma_f32_16x16x32_bf16 v[4:7], a[152:155], a[80:83], v[4:7]\n"
    "v_mfma_f32_16x16x32_bf16 v[4:7], a[156:159], a[84:87], v[4:7]\n"
    "v_mfma_f32_16x16x32_bf16 v[32:35], a[152:155], a[88:91], v[32:35]\n"
    "v_mfma_f32_16x16x32_bf16 v[32:35], a[156:159], a[92:95], v[32:35]\n"
    "s_waitcnt vmcnt(18) lgkmcnt(0)\n"
    "s_barrier\n"
    "buffer_load_dword v253, v254, s[12:15], 0 offen\n"
    "v_mfma_f32_16x16x32_bf16 v[60:63], a[152:155], a[96:99], v[60:63]\n"
    "ds_read_b128 a[112:115], v208 offset:40960\n"
    "v_mfma_f32_16x16x32_bf16 v[60:63], a[156:159], a[100:103], v[60:63]\n"
    "s_mov_b32 m0, s77\n"
    "s_nop 0\n"
    "buffer_load_dwordx4 v228, s[40:43], s66 offen offset:0 nt lds\n"
    "v_mfma_f32_16x16x32_bf16 v[88:91], a[152:155], a[104:107], v[88:91]\n"
    "ds_read_b128 a[116:119], v209 offset:40960\n"
    "v_mfma_f32_16x16x32_bf16 v[88:91], a[156:159], a[108:111], v[88:91]\n"
    "v_mfma_f32_16x16x32_bf16 v[8:11], a[160:163], a[80:83], v[8:11]\n"
    "ds_read_b128 a[120:123], v208 offset:43008\n"
    "v_mfma_f32_16x16x32_bf16 v[8:11], a[164:167], a[84:87], v[8:11]\n"
    "v_mfma_f32_16x16x32_bf16 v[36:39], a[160:163], a[88:91], v[36:39]\n"
    "ds_read_b128 a[124:127], v209 offset:43008\n"
    "v_mfma_f32_16x16x32_bf16 v[36:39], a[164:167], a[92:95], v[36:39]\n"
    "buffer_load_dwordx4 v229, s[40:43], s66 offen offset:1024 nt lds\n"
    "v_mfma_f32_16x16x32_bf16 v[64:67], a[160:163], a[96:99], v[64:67]\n"
    "ds_read_b128 a[128:131], v208 offset:45056\n"
    "v_mfma_f32_16x16x32_bf16 v[64:67], a[164:167], a[100:103], v[64:67]\n"
    "v_mfma_f32_16x16x32_bf16 v[92:95], a[160:163], a[104:107], v[92:95]\n"
    "ds_read_b128 a[132:135], v209 offset:45056\n"
    "v_mfma_f32_16x16x32_bf16 v[92:95], a[164:167], a[108:111], v[92:95]\n"
    "v_mfma_f32_16x16x32_bf16 v[12:15], a[168:171], a[80:83], v[12:15]\n"
    "ds_read_b128 a[136:139], v208 offset:47104\n"
    "v_mfma_f32_16x16x32_bf16 v[12:15], a[172:175], a[84:87], v[12:15]\n"
    "v_mfma_f32_16x16x32_bf16 v[40:43], a[168:171], a[88:91], v[40:43]\n"
    "ds_read_b128 a[140:143], v209 offset:47104\n"
    "buffer_load_dwordx4 v230, s[40:43], s66 offen offset:2048 nt lds\n"
    "v_mfma_f32_16x16x32_bf16 v[40:43], a[172:175], a[92:95], v[40:43]\n"
    "v_mfma_f32_16x16x32_bf16 v[68:71], a[168:171], a[96:99], v[68:71]\n"
    "v_mfma_f32_16x16x32_bf16 v[68:71], a[172:175], a[100:103], v[68:71]\n"
    "v_mfma_f32_16x16x32_bf16 v[96:99], a[168:171], a[104:107], v[96:99]\n"
    "v_mfma_f32_16x16x32_bf16 v[96:99], a[172:175], a[108:111], v[96:99]\n"
    "v_mfma_f32_16x16x32_bf16 v[16:19], a[176:179], a[80:83], v[16:19]\n"
    "buffer_load_dwordx4 v231, s[40:43], s66 offen offset:3072 nt lds\n"
    "v_mfma_f32_16x16x32_bf16 v[16:19], a[180:183], a[84:87], v[16:19]\n"
    "v_mfma_f32_16x16x32_bf16 v[44:47], a[176:179], a[88:91], v[44:47]\n"
    "v_mfma_f32_16x16x32_bf16 v[44:47], a[180:183], a[92:95], v[44:47]\n"
    "v_mfma_f32_16x16x32_bf16 v[72:75], a[176:179], a[96:99], v[72:75]\n"
    "v_mfma_f32_16x16x32_bf16 v[72:75], a[180:183], a[100:103], v[72:75]\n"
    "v_mfma_f32_16x16x32_bf16 v[100:103], a[176:179], a[104:107], v[100:103]\n"
    "v_mfma_f32_16x16x32_bf16 v[100:103], a[180:183], a[108:111], v[100:103]\n"
    "s_mov_b32 m0, s79\n"
    "s_nop 0\n"
    "buffer_load_dwordx4 v228, s[44:47], s66 offen offset:0 nt lds\n"
    "v_mfma_f32_16x16x32_bf16 v[20:23], a[184:187], a[80:83], v[20:23]\n"
    "s_add_u32 s9, s9, 1\n"
    "s_cmp_eq_u32 s9, 0\n"
    "s_cselect_b32 s14, 8388608, 0\n"
    "v_mfma_f32_16x16x32_bf16 v[20:23], a[188:191], a[84:87], v[20:23]\n"
    "v_mfma_f32_16x16x32_bf16 v[48:51], a[184:187], a[88:91], v[48:51]\n"
    "v_mfma_f32_16x16x32_bf16 v[48:51], a[188:191], a[92:95], v[48:51]\n"
    "v_mfma_f32_16x16x32_bf16 v[76:79], a[184:187], a[96:99], v[76:79]\n"
    "v_mfma_f32_16x16x32_bf16 v[76:79], a[188:191], a[100:103], v[76:79]\n"
    "buffer_load_dwordx4 v229, s[44:47], s66 offen offset:1024 nt lds\n"
    "v_mfma_f32_16x16x32_bf16 v[104:107], a[184:187], a[104:107], v[104:107]\n"
    "v_mfma_f32_16x16x32_bf16 v[104:107], a[188:191], a[108:111], v[104:107]\n"
    "v_mfma_f32_16x16x32_bf16 v[24:27], a[192:195], a[80:83], v[24:27]\n"
    "v_mfma_f32_16x16x32_bf16 v[24:27], a[196:199], a[84:87], v[24:27]\n"
    "v_mfma_f32_16x16x32_bf16 v[52:55], a[192:195], a[88:91], v[52:55]\n"
    "v_mfma_f32_16x16x32_bf16 v[52:55], a[196:199], a[92:95], v[52:55]\n"
    "v_mfma_f32_16x16x32_bf16 v[80:83], a[192:195], a[96:99], v[80:83]\n"
    "buffer_load_dwordx4 v230, s[44:47], s66 offen offset:2048 nt lds\n"
    "v_mfma_f32_16x16x32_bf16 v[80:83], a[196:199], a[100:103], v[80:83]\n"
    "v_mfma_f32_16x16x32_bf16 v[108:111], a[192:195], a[104:107], v[108:111]\n"
    "v_mfma_f32_16x16x32_bf16 v[108:111], a[196:199], a[108:111], v[108:111]\n"
    "v_mfma_f32_16x16x32_bf16 a[32:35], a[200:203], a[80:83], a[32:35]\n"
    "v_mfma_f32_16x16x32_bf16 a[32:35], a[204:207], a[84:87], a[32:35]\n"
    "v_mfma_f32_16x16x32_bf16 a[36:39], a[200:203], a[88:91], a[36:39]\n"
    "buffer_load_dwordx4 v231, s[44:47], s66 offen offset:3072 nt lds\n"
    "v_mfma_f32_16x16x32_bf16 a[36:39], a[204:207], a[92:95], a[36:39]\n"
    "v_mfma_f32_16x16x32_bf16 a[40:43], a[200:203], a[96:99], a[40:43]\n"
    "v_mfma_f32_16x16x32_bf16 a[40:43], a[204:207], a[100:103], a[40:43]\n"
    "v_mfma_f32_16x16x32_bf16 a[44:47], a[200:203], a[104:107], a[44:47]\n"
    "v_mfma_f32_16x16x32_bf16 a[44:47], a[204:207], a[108:111], a[44:47]\n"
    "v_mfma_f32_16x16x32_bf16 a[0:3], a[80:83], a[208:211], a[0:3]\n"
    "v_mfma_f32_16x16x32_bf16 a[0:3], a[84:87], a[212:215], a[0:3]\n"
    "s_mov_b32 m0, s81\n"
    "s_nop 0\n"
    "buffer_load_dwordx4 v232, s[48:51], s65 offen nt lds\n"
    "v_mfma_f32_16x16x32_bf16 a[4:7], a[88:91], a[208:211], a[4:7]\n"
    "v_mfma_f32_16x16x32_bf16 a[4:7], a[92:95], a[212:215], a[4:7]\n"
    "v_mfma_f32_16x16x32_bf16 a[8:11], a[96:99], a[208:211], a[8:11]\n"
    "v_mfma_f32_16x16x32_bf16 a[8:11], a[100:103], a[212:215], a[8:11]\n"
    "v_mfma_f32_16x16x32_bf16 a[12:15], a[104:107], a[208:211], a[12:15]\n"
    "v_mfma_f32_16x16x32_bf16 a[12:15], a[108:111], a[212:215], a[12:15]\n"
    "s_waitcnt vmcnt(14) lgkmcnt(0)\n"
    "s_barrier\n"
    "v_mfma_f32_16x16x32_bf16 v[112:115], a[144:147], a[112:115], v[112:115]\n"
    "ds_read_b128 a[80:83], v208 offset:0\n"
    "v_mfma_f32_16x16x32_bf16 v[112:115], a[148:151], a[116:119], v[112:115]\n"
    "ds_read_b128 a[84:87], v209 offset:0\n"
    "v_mfma_f32_16x16x32_bf16 v[136:139], a[144:147], a[120:123], v[136:139]\n"
    "ds_read_b128 a[88:91], v208 offset:2048\n"
    "s_mov_b32 m0, s74\n"
    "s_nop 0\n"
    "buffer_load_dwordx4 v220, s[36:39], s65 offen offset:0 sc1 lds\n"
    "v_mfma_f32_16x16x32_bf16 v[136:139], a[148:151], a[124:127], v[136:139]\n"
    "ds_read_b128 a[92:95], v209 offset:2048\n"
    "v_mfma_f32_16x16x32_bf16 v[160:163], a[144:147], a[128:131], v[160:163]\n"
    "ds_read_b128 a[96:99], v208 offset:4096\n"
    "v_mfma_f32_16x16x32_bf16 v[160:163], a[148:151], a[132:135], v[160:163]\n"
    "ds_read_b128 a[100:103], v209 offset:4096\n"
    "v_mfma_f32_16x16x32_bf16 v[184:187], a[144:147], a[136:139], v[184:187]\n"
    "ds_read_b128 a[104:107], v208 offset:6144\n"
    "v_mfma_f32_16x16x32_bf16 v[184:187], a[148:151], a[140:143], v[184:187]\n"
    "ds_read_b128 a[108:111], v209 offset:6144\n"
    "v_mfma_f32_16x16x32_bf16 v[116:119], a[152:155], a[112:115], v[116:119]\n"
    "ds_read_b64_tr_b16 a[144:145], v210 offset:0\n"
    "v_mfma_f32_16x16x32_bf16 v[116:119], a[156:159], a[116:119], v[116:119]\n"
    "v_mfma_f32_16x16x32_bf16 v[140:143], a[152:155], a[120:123], v[140:143]\n"
    "ds_read_b64_tr_b16 a[146:147], v211 offset:0\n"
    "buffer_load_dwordx4 v221, s[36:39], s65 offen offset:1024 sc1 lds\n"
    "v_mfma_f32_16x16x32_bf16 v[140:143], a[156:159], a[124:127], v[140:143]\n"
    "v_mfma_f32_16x16x32_bf16 v[164:167], a[152:155], a[128:131], v[164:167]\n"
    "ds_read_b64_tr_b16 a[148:149], v210 offset:8192\n"
    "v_mfma_f32_16x16x32_bf16 v[164:167], a[156:159], a[132:135], v[164:167]\n"
    "v_mfma_f32_16x16x32_bf16 v[188:191], a[152:155], a[136:139], v[188:191]\n"
    "ds_read_b64_tr_b16 a[150:151], v211 offset:8192\n"
    "v_mfma_f32_16x16x32_bf16 v[188:191], a[156:159], a[140:143], v[188:191]\n"
    "v_mfma_f32_16x16x32_bf16 v[120:123], a[160:163], a[112:115], v[120:123]\n"
    "ds_read_b64_tr_b16 a[152:153], v212 offset:0\n"
    "v_mfma_f32_16x16x32_bf16 v[120:123], a[164:167], a[116:119], v[120:123]\n"
    "v_mfma_f32_16x16x32_bf16 v[144:147], a[160:163], a[120:123], v[144:147]\n"
    "ds_read_b64_tr_b16 a[154:155], v213 offset:0\n"
    "v_mfma_f32_16x16x32_bf16 v[144:147], a[164:167], a[124:127], v[144:147]\n"
    "buffer_load_dwordx4 v222, s[36:39], s65 offen offset:2048 sc1 lds\n"
    "v_mfma_f32_16x16x32_bf16 v[168:171], a[160:163], a[128:131], v[168:171]\n"
    "ds_read_b64_tr_b16 a[156:157], v212 offset:8192\n"
    "v_mfma_f32_16x16x32_bf16 v[168:171], a[164:167], a[132:135], v[168:171]\n"
    "v_mfma_f32_16x16x32_bf16 v[192:195], a[160:163], a[136:139], v[192:195]\n"
    "ds_read_b64_tr_b16 a[158:159], v213 offset:8192\n"
    "v_mfma_f32_16x16x32_bf16 v[192:195], a[164:167], a[140:143], v[192:195]\n"
    "v_mfma_f32_16x16x32_bf16 v[124:127], a[168:171], a[112:115], v[124:127]\n"
    "ds_read_b64_tr_b16 a[160:161], v214 offset:0\n"
    "v_mfma_f32_16x16x32_bf16 v[124:127], a[172:175], a[116:119], v[124:127]\n"
    "v_mfma_f32_16x16x32_bf16 v[148:151], a[168:171], a[120:123], v[148:151]\n"
    "ds_read_b64_tr_b16 a[162:163], v215 offset:0\n"
    "v_mfma_f32_16x16x32_bf16 v[148:151], a[172:175], a[124:127], v[148:151]\n"
    "buffer_load_dwordx4 v223, s[36:39], s65 offen offset:3072 sc1 lds\n"
    "v_mfma_f32_16x16x32_bf16 v[172:175], a[168:171], a[128:131], v[172:175]\n"
    "ds_read_b64_tr_b16 a[164:165], v214 offset:8192\n"
    "v_mfma_f32_16x16x32_bf16 v[172:175], a[172:175], a[132:135], v[172:175]\n"
    "v_mfma_f32_16x16x32_bf16 v[196:199], a[168:171], a[136:139], v[196:199]\n"
    "ds_read_b64_tr_b16 a[166:167], v215 offset:8192\n"
    "v_mfma_f32_16x16x32_bf16 v[196:199], a[172:175], a[140:143], v[196:199]\n"
    "v_mfma_f32_16x16x32_bf16 v[128:131], a[176:179], a[112:115], v[128:131]\n"
    "ds_read_b64_tr_b16 a[168:169], v216 offset:0\n"
    "v_mfma_f32_16x16x32_bf16 v[128:131], a[180:183], a[116:119], v[128:131]\n"
    "v_mfma_f32_16x16x32_bf16 v[152:155], a[176:179], a[120:123], v[152:155]\n"
    "ds_read_b64_tr_b16 a[170:171], v217 offset:0\n"
    "v_mfma_f32_16x16x32_bf16 v[152:155], a[180:183], a[124:127], v[152:155]\n"
    "v_mfma_f32_16x16x32_bf16 v[176:179], a[176:179], a[128:131], v[176:179]\n"
    "ds_read_b64_tr_b16 a[172:173], v216 offset:8192\n"
    "s_mov_b32 m0, s75\n"
    "s_nop 0\n"
    "buffer_load_dwordx4 v224, s[36:39], s65 offen offset:0 sc1 lds\n"
    "v_mfma_f32_16x16x32_bf16 v[176:179], a[180:183], a[132:135], v[176:179]\n"
    "v_mfma_f32_16x16x32_bf16 v[200:203], a[176:179], a[136:139], v[200:203]\n"
    "ds_read_b64_tr_b16 a[174:175], v217 offset:8192\n"
    "v_mfma_f32_16x16x32_bf16 v[200:203], a[180:183], a[140:143], v[200:203]\n"
    "v_mfma_f32_16x16x32_bf16 v[132:135], a[184:187], a[112:115], v[132:135]\n"
    "ds_read_b64_tr_b16 a[176:177], v210 offset:16384\n"
    "v_mfma_f32_16x16x32_bf16 v[132:135], a[188:191], a[116:119], v[132:135]\n"
    "v_mfma_f32_16x16x32_bf16 v[156:159], a[184:187], a[120:123], v[156:159]\n"
    "ds_read_b64_tr_b16 a[178:179], v211 offset:16384\n"
    "v_mfma_f32_16x16x32_bf16 v[156:159], a[188:191], a[124:127], v[156:159]\n"
    "v_mfma_f32_16x16x32_bf16 v[180:183], a[184:187], a[128:131], v[180:183]\n"
    "ds_read_b64_tr_b16 a[180:181], v210 offset:24576\n"
    "buffer_load_dwordx4 v225, s[36:39], s65 offen offset:1024 sc1 lds\n"
    "v_mfma_f32_16x16x32_bf16 v[180:183], a[188:191], a[132:135], v[180:183]\n"
    "v_mfma_f32_16x16x32_bf16 v[204:207], a[184:187], a[136:139], v[204:207]\n"
    "ds_read_b64_tr_b16 a[182:183], v211 offset:24576\n"
    "v_mfma_f32_16x16x32_bf16 v[204:207], a[188:191], a[140:143], v[204:207]\n"
    "v_mfma_f32_16x16x32_bf16 a[64:67], a[192:195], a[112:115], a[64:67]\n"
    "ds_read_b64_tr_b16 a[184:185], v212 offset:16384\n"
    "v_mfma_f32_16x16x32_bf16 a[64:67], a[196:199], a[116:119], a[64:67]\n"
    "v_mfma_f32_16x16x32_bf16 a[68:71], a[192:195], a[120:123], a[68:71]\n"
    "ds_read_b64_tr_b16 a[186:187], v213 offset:16384\n"
    "v_mfma_f32_16x16x32_bf16 a[68:71], a[196:199], a[124:127], a[68:71]\n"
    "v_mfma_f32_16x16x32_bf16 a[72:75], a[192:195], a[128:131], a[72:75]\n"
    "ds_read_b64_tr_b16 a[188:189], v212 offset:24576\n"
    "v_mfma_f32_16x16x32_bf16 a[72:75], a[196:199], a[132:135], a[72:75]\n"
    "buffer_load_dwordx4 v226, s[36:39], s65 offen offset:2048 sc1 lds\n"
    "v_mfma_f32_16x16x32_bf16 a[76:79], a[192:195], a[136:139], a[76:79]\n"
    "ds_read_b64_tr_b16 a[190:191], v213 offset:24576\n"
    "v_mfma_f32_16x16x32_bf16 a[76:79], a[196:199], a[140:143], a[76:79]\n"
    "v_mfma_f32_16x16x32_bf16 a[48:51], a[200:203], a[112:115], a[48:51]\n"
    "ds_read_b64_tr_b16 a[192:193], v214 offset:16384\n"
    "v_mfma_f32_16x16x32_bf16 a[48:51], a[204:207], a[116:119], a[48:51]\n"
    "v_mfma_f32_16x16x32_bf16 a[52:55], a[200:203], a[120:123], a[52:55]\n"
    "ds_read_b64_tr_b16 a[194:195], v215 offset:16384\n"
    "v_mfma_f32_16x16x32_bf16 a[52:55], a[204:207], a[124:127], a[52:55]\n"
    "v_mfma_f32_16x16x32_bf16 a[56:59], a[200:203], a[128:131], a[56:59]\n"
    "ds_read_b64_tr_b16 a[196:197], v214 offset:24576\n"
    "v_mfma_f32_16x16x32_bf16 a[56:59], a[204:207], a[132:135], a[56:59]\n"
    "buffer_load_dwordx4 v227, s[36:39], s65 offen offset:3072 sc1 lds\n"
    "v_mfma_f32_16x16x32_bf16 a[60:63], a[200:203], a[136:139], a[60:63]\n"
    "ds_read_b64_tr_b16 a[198:199], v215 offset:24576\n"
    "v_mfma_f32_16x16x32_bf16 a[60:63], a[204:207], a[140:143], a[60:63]\n"
    "v_mfma_f32_16x16x32_bf16 a[16:19], a[112:115], a[208:211], a[16:19]\n"
    "ds_read_b64_tr_b16 a[200:201], v216 offset:16384\n"
    "v_mfma_f32_16x16x32_bf16 a[16:19], a[116:119], a[212:215], a[16:19]\n"
    "v_mfma_f32_16x16x32_bf16 a[20:23], a[120:123], a[208:211], a[20:23]\n"
    "ds_read_b64_tr_b16 a[202:203], v217 offset:16384\n"
    "v_mfma_f32_16x16x32_bf16 a[20:23], a[124:127], a[212:215], a[20:23]\n"
    "v_mfma_f32_16x16x32_bf16 a[24:27], a[128:131], a[208:211], a[24:27]\n"
    "ds_read_b64_tr_b16 a[204:205], v216 offset:24576\n"
    "v_mfma_f32_16x16x32_bf16 a[24:27], a[132:135], a[212:215], a[24:27]\n"
    "v_mfma_f32_16x16x32_bf16 a[28:31], a[136:139], a[208:211], a[28:31]\n"
    "ds_read_b64_tr_b16 a[206:207], v217 offset:24576\n"
    "s_add_u32 s65, s65, 128\n"
    "s_add_u32 s66, s66, 458752\n"
    "s_cmp_ge_u32 s65, 2048\n"
    "s_cselect_b32 s38, 0, s38\n"
    "s_cselect_b32 s42, 0, s42\n"
    "s_cselect_b32 s46, 0, s46\n"
    "s_cselect_b32 s50, 0, s50\n"
    "v_mfma_f32_16x16x32_bf16 a[28:31], a[140:143], a[212:215], a[28:31]\n"
    "s_sub_u32 s67, s67, 1\n"
    "s_cmp_eq_u32 s67, 0\n"
    "s_cbranch_scc1 LG1_exit_%=\n"
    "s_waitcnt lgkmcnt(15)\n"
    "v_mfma_f32_16x16x32_bf16 v[0:3], a[144:147], a[80:83], v[0:3]\n"
    "ds_read_b128 a[208:211], v218 offset:0\n"
    "v_mfma_f32_16x16x32_bf16 v[0:3], a[148:151], a[84:87], v[0:3]\n"
    "ds_read_b128 a[212:215], v219 offset:0\n"
    "v_mfma_f32_16x16x32_bf16 v[28:31], a[144:147], a[88:91], v[28:31]\n"
    "v_mfma_f32_16x16x32_bf16 v[28:31], a[148:151], a[92:95], v[28:31]\n"
    "v_mfma_f32_16x16x32_bf16 v[56:59], a[144:147], a[96:99], v[56:59]\n"
    "v_mfma_f32_16x16x32_bf16 v[56:59], a[148:151], a[100:103], v[56:59]\n"
    "v_mfma_f32_16x16x32_bf16 v[84:87], a[144:147], a[104:107], v[84:87]\n"
    "v_mfma_f32_16x16x32_bf16 v[84:87], a[148:151], a[108:111], v[84:87]\n"
    "v_mfma_f32_16x16x32_bf16 v[4:7], a[152:155], a[80:83], v[4:7]\n"
    "v_mfma_f32_16x16x32_bf16 v[4:7], a[156:159], a[84:87], v[4:7]\n"
    "v_mfma_f32_16x16x32_bf16 v[32:35], a[152:155], a[88:91], v[32:35]\n"
    "v_mfma_f32_16x16x32_bf16 v[32:35], a[156:159], a[92:95], v[32:35]\n"
    "s_waitcnt vmcnt(18) lgkmcnt(0)\n"
    "s_barrier\n"
    "buffer_load_dword v253, v254, s[12:15], 0 offen\n"
    "v_mfma_f32_16x16x32_bf16 v[60:63], a[152:155], a[96:99], v[60:63]\n"
    "ds_read_b128 a[112:115], v208 offset:8192\n"
    "v_mfma_f32_16x16x32_bf16 v[60:63], a[156:159], a[100:103], v[60:63]\n"
    "s_mov_b32 m0, s76\n"
    "s_nop 0\n"
    "buffer_load_dwordx4 v228, s[40:43], s66 offen offset:0 nt lds\n"
    "v_mfma_f32_16x16x32_bf16 v[88:91], a[152:155], a[104:107], v[88:91]\n"
    "ds_read_b128 a[116:119], v209 offset:8192\n"
    "v_mfma_f32_16x16x32_bf16 v[88:91], a[156:159], a[108:111], v[88:91]\n"
    "v_mfma_f32_16x16x32_bf16 v[8:11], a[160:163], a[80:83], v[8:11]\n"
    "ds_read_b128 a[120:123], v208 offset:10240\n"
    "v_mfma_f32_16x16x32_bf16 v[8:11], a[164:167], a[84:87], v[8:11]\n"
    "v_mfma_f32_16x16x32_bf16 v[36:39], a[160:163], a[88:91], v[36:39]\n"
    "ds_read_b128 a[124:127], v209 offset:10240\n"
    "v_mfma_f32_16x16x32_bf16 v[36:39], a[164:167], a[92:95], v[36:39]\n"
    "buffer_load_dwordx4 v229, s[40:43], s66 offen offset:1024 nt lds\n"
    "v_mfma_f32_16x16x32_bf16 v[64:67], a[160:163], a[96:99], v[64:67]\n"
    "ds_read_b128 a[128:131], v208 offset:12288\n"
    "v_mfma_f32_16x16x32_bf16 v[64:67], a[164:167], a[100:103], v[64:67]\n"
    "v_mfma_f32_16x16x32_bf16 v[92:95], a[160:163], a[104:107], v[92:95]\n"
    "ds_read_b128 a[132:135], v209 offset:12288\n"
    "v_mfma_f32_16x16x32_bf16 v[92:95], a[164:167], a[108:111], v[92:95]\n"
    "v_mfma_f32_16x16x32_bf16 v[12:15], a[168:171], a[80:83], v[12:15]\n"
    "ds_read_b128 a[136:139], v208 offset:14336\n"
    "v_mfma_f32_16x16x32_bf16 v[12:15], a[172:175], a[84:87], v[12:15]\n"
    "v_mfma_f32_16x16x32_bf16 v[40:43], a[168:171], a[88:91], v[40:43]\n"
    "ds_read_b128 a[140:143], v209 offset:14336\n"
    "buffer_load_dwordx4 v230, s[40:43], s66 offen offset:2048 nt lds\n"
    "v_mfma_f32_16x16x32_bf16 v[40:43], a[172:175], a[92:95], v[40:43]\n"
    "v_mfma_f32_16x16x32_bf16 v[68:71], a[168:171], a[96:99], v[68:71]\n"
    "v_mfma_f32_16x16x32_bf16 v[68:71], a[172:175], a[100:103], v[68:71]\n"
    "v_mfma_f32_16x16x32_bf16 v[96:99], a[168:171], a[104:107], v[96:99]\n"
    "v_mfma_f32_16x16x32_bf16 v[96:99], a[172:175], a[108:111], v[96:99]\n"
    "v_mfma_f32_16x16x32_bf16 v[16:19], a[176:179], a[80:83], v[16:19]\n"
    "buffer_load_dwordx4 v231, s[40:43], s66 offen offset:3072 nt lds\n"
    "v_mfma_f32_16x16x32_bf16 v[16:19], a[180:183], a[84:87], v[16:19]\n"
    "v_mfma_f32_16x16x32_bf16 v[44:47], a[176:179], a[88:91], v[44:47]\n"
    "v_mfma_f32_16x16x32_bf16 v[44:47], a[180:183], a[92:95], v[44:47]\n"
    "v_mfma_f32_16x16x32_bf16 v[72:75], a[176:179], a[96:99], v[72:75]\n"
    "v_mfma_f32_16x16x32_bf16 v[72:75], a[180:183], a[100:103], v[72:75]\n"
    "v_mfma_f32_16x16x32_bf16 v[100:103], a[176:179], a[104:107], v[100:103]\n"
    "v_mfma_f32_16x16x32_bf16 v[100:103], a[180:183], a[108:111], v[100:103]\n"
    "s_mov_b32 m0, s78\n"
    "s_nop 0\n"
    "buffer_load_dwordx4 v228, s[44:47], s66 offen offset:0 nt lds\n"
    "v_mfma_f32_16x16x32_bf16 v[20:23], a[184:187], a[80:83], v[20:23]\n"
    "s_add_u32 s9, s9, 1\n"
    "s_cmp_eq_u32 s9, 0\n"
    "s_cselect_b32 s14, 8388608, 0\n"
    "v_mfma_f32_16x16x32_bf16 v[20:23], a[188:191], a[84:87], v[20:23]\n"
    "v_mfma_f32_16x16x32_bf16 v[48:51], a[184:187], a[88:91], v[48:51]\n"
    "v_mfma_f32_16x16x32_bf16 v[48:51], a[188:191], a[92:95], v[48:51]\n"
    "v_mfma_f32_16x16x32_bf16 v[76:79], a[184:187], a[96:99], v[76:79]\n"
    "v_mfma_f32_16x16x32_bf16 v[76:79], a[188:191], a[100:103], v[76:79]\n"
    "buffer_load_dwordx4 v229, s[44:47], s66 offen offset:1024 nt lds\n"
    "v_mfma_f32_16x16x32_bf16 v[104:107], a[184:187], a[104:107], v[104:107]\n"
    "v_mfma_f32_16x16x32_bf16 v[104:107], a[188:191], a[108:111], v[104:107]\n"
    "v_mfma_f32_16x16x32_bf16 v[24:27], a[192:195], a[80:83], v[24:27]\n"
    "v_mfma_f32_16x16x32_bf16 v[24:27], a[196:199], a[84:87], v[24:27]\n"
    "v_mfma_f32_16x16x32_bf16 v[52:55], a[192:195], a[88:91], v[52:55]\n"
    "v_mfma_f32_16x16x32_bf16 v[52:55], a[196:199], a[92:95], v[52:55]\n"
    "v_mfma_f32_16x16x32_bf16 v[80:83], a[192:195], a[96:99], v[80:83]\n"
    "buffer_load_dwordx4 v230, s[44:47], s66 offen offset:2048 nt lds\n"
    "v_mfma_f32_16x16x32_bf16 v[80:83], a[196:199], a[100:103], v[80:83]\n"
    "v_mfma_f32_16x16x32_bf16 v[108:111], a[192:195], a[104:107], v[108:111]\n"
    "v_mfma_f32_16x16x32_bf16 v[108:111], a[196:199], a[108:111], v[108:111]\n"
    "v_mfma_f32_16x16x32_bf16 a[32:35], a[200:203], a[80:83], a[32:35]\n"
    "v_mfma_f32_16x16x32_bf16 a[32:35], a[204:207], a[84:87], a[32:35]\n"
    "v_mfma_f32_16x16x32_bf16 a[36:39], a[200:203], a[88:91], a[36:39]\n"
    "buffer_load_dwordx4 v231, s[44:47], s66 offen offset:3072 nt lds\n"
    "v_mfma_f32_16x16x32_bf16 a[36:39], a[204:207], a[92:95], a[36:39]\n"
    "v_mfma_f32_16x16x32_bf16 a[40:43], a[200:203], a[96:99], a[40:43]\n"
    "v_mfma_f32_16x16x32_bf16 a[40:43], a[204:207], a[100:103], a[40:43]\n"
    "v_mfma_f32_16x16x32_bf16 a[44:47], a[200:203], a[104:107], a[44:47]\n"
    "v_mfma_f32_16x16x32_bf16 a[44:47], a[204:207], a[108:111], a[44:47]\n"
    "v_mfma_f32_16x16x32_bf16 a[0:3], a[80:83], a[208:211], a[0:3]\n"
    "v_mfma_f32_16x16x32_bf16 a[0:3], a[84:87], a[212:215], a[0:3]\n"
    "s_mov_b32 m0, s80\n"
    "s_nop 0\n"
    "buffer_load_dwordx4 v232, s[48:51], s65 offen nt lds\n"
    "v_mfma_f32_16x16x32_bf16 a[4:7], a[88:91], a[208:211], a[4:7]\n"
    "v_mfma_f32_16x16x32_bf16 a[4:7], a[92:95], a[212:215], a[4:7]\n"
    "v_mfma_f32_16x16x32_bf16 a[8:11], a[96:99], a[208:211], a[8:11]\n"
    "v_mfma_f32_16x16x32_bf16 a[8:11], a[100:103], a[212:215], a[8:11]\n"
    "v_mfma_f32_16x16x32_bf16 a[12:15], a[104:107], a[208:211], a[12:15]\n"
    "v_mfma_f32_16x16x32_bf16 a[12:15], a[108:111], a[212:215], a[12:15]\n"
    "s_branch LG1_loop_%=\n"
    "LG1_exit_%=:\n"
    "s_waitcnt lgkmcnt(15)\n"
    "v_mfma_f32_16x16x32_bf16 v[0:3], a[144:147], a[80:83], v[0:3]\n"
    "ds_read_b128 a[208:211], v218 offset:0\n"
    "v_mfma_f32_16x16x32_bf16 v[0:3], a[148:151], a[84:87], v[0:3]\n"
    "ds_read_b128 a[212:215], v219 offset:0\n"
    "v_mfma_f32_16x16x32_bf16 v[28:31], a[144:147], a[88:91], v[28:31]\n"
    "v_mfma_f32_16x16x32_bf16 v[28:31], a[148:151], a[92:95], v[28:31]\n"
    "v_mfma_f32_16x16x32_bf16 v[56:59], a[144:147], a[96:99], v[56:59]\n"
    "v_mfma_f32_16x16x32_bf16 v[56:59], a[148:151], a[100:103], v[56:59]\n"
    "v_mfma_f32_16x16x32_bf16 v[84:87], a[144:147], a[104:107], v[84:87]\n"
    "s_waitcnt vmcnt(8)\n"
    "s_barrier\n"
    "v_mfma_f32_16x16x32_bf16 v[84:87], a[148:151], a[108:111], v[84:87]\n"
    "v_mov_b32 v254, v237\n"
    "s_cmp_eq_u32 s60, 2\n"
    "s_cselect_b32 s12, s20, s96\n"
    "s_cselect_b32 s13, s21, s97\n"
    "s_mov_b32 s14, 8388608\n"
    "s_cselect_b32 s14, 7340032, s14\n"
    "s_cmp_eq_u32 s60, 3\n"
    "s_cselect_b32 s14, 0, s14\n"
    "buffer_load_dword v253, v254, s[12:15], 0 offen\n"
    "v_mfma_f32_16x16x32_bf16 v[4:7], a[152:155], a[80:83], v[4:7]\n"
    "ds_read_b64_tr_b16 a[144:145], v210 offset:32768\n"
    "v_mfma_f32_16x16x32_bf16 v[4:7], a[156:159], a[84:87], v[4:7]\n"
    "v_mfma_f32_16x16x32_bf16 v[32:35], a[152:155], a[88:91], v[32:35]\n"
    "ds_read_b64_tr_b16 a[146:147], v211 offset:32768\n"
    "v_mfma_f32_16x16x32_bf16 v[32:35], a[156:159], a[92:95], v[32:35]\n"
    "v_mfma_f32_16x16x32_bf16 v[60:63], a[152:155], a[96:99], v[60:63]\n"
    "ds_read_b64_tr_b16 a[148:149], v210 offset:40960\n"
    "v_mfma_f32_16x16x32_bf16 v[60:63], a[156:159], a[100:103], v[60:63]\n"
    "v_mfma_f32_16x16x32_bf16 v[88:91], a[152:155], a[104:107], v[88:91]\n"
    "ds_read_b64_tr_b16 a[150:151], v211 offset:40960\n"
    "v_mfma_f32_16x16x32_bf16 v[88:91], a[156:159], a[108:111], v[88:91]\n"
    "v_mfma_f32_16x16x32_bf16 v[8:11], a[160:163], a[80:83], v[8:11]\n"
    "ds_read_b64_tr_b16 a[152:153], v212 offset:32768\n"
    "v_mfma_f32_16x16x32_bf16 v[8:11], a[164:167], a[84:87], v[8:11]\n"
    "v_mfma_f32_16x16x32_bf16 v[36:39], a[160:163], a[88:91], v[36:39]\n"
    "ds_read_b64_tr_b16 a[154:155], v213 offset:32768\n"
    "v_mfma_f32_16x16x32_bf16 v[36:39], a[164:167], a[92:95], v[36:39]\n"
    "v_mfma_f32_16x16x32_bf16 v[64:67], a[160:163], a[96:99], v[64:67]\n"
    "ds_read_b64_tr_b16 a[156:157], v212 offset:40960\n"
    "v_mfma_f32_16x16x32_bf16 v[64:67], a[164:167], a[100:103], v[64:67]\n"
    "v_mfma_f32_16x16x32_bf16 v[92:95], a[160:163], a[104:107], v[92:95]\n"
    "ds_read_b64_tr_b16 a[158:159], v213 offset:40960\n"
    "v_mfma_f32_16x16x32_bf16 v[92:95], a[164:167], a[108:111], v[92:95]\n"
    "v_mfma_f32_16x16x32_bf16 v[12:15], a[168:171], a[80:83], v[12:15]\n"
    "ds_read_b64_tr_b16 a[160:161], v214 offset:32768\n"
    "v_mfma_f32_16x16x32_bf16 v[12:15], a[172:175], a[84:87], v[12:15]\n"
    "v_mfma_f32_16x16x32_bf16 v[40:43], a[168:171], a[88:91], v[40:43]\n"
    "ds_read_b64_tr_b16 a[162:163], v215 offset:32768\n"
    "v_mfma_f32_16x16x32_bf16 v[40:43], a[172:175], a[92:95], v[40:43]\n"
    "v_mfma_f32_16x16x32_bf16 v[68:71], a[168:171], a[96:99], v[68:71]\n"
    "ds_read_b64_tr_b16 a[164:165], v214 offset:40960\n"
    "v_mfma_f32_16x16x32_bf16 v[68:71], a[172:175], a[100:103], v[68:71]\n"
    "v_mfma_f32_16x16x32_bf16 v[96:99], a[168:171], a[104:107], v[96:99]\n"
    "ds_read_b64_tr_b16 a[166:167], v215 offset:40960\n"
    "v_mfma_f32_16x16x32_bf16 v[96:99], a[172:175], a[108:111], v[96:99]\n"
    "s_waitcnt lgkmcnt(15)\n"
    "v_mfma_f32_16x16x32_bf16 v[16:19], a[176:179], a[80:83], v[16:19]\n"
    "ds_read_b64_tr_b16 a[168:169], v216 offset:32768\n"
    "v_mfma_f32_16x16x32_bf16 v[16:19], a[180:183], a[84:87], v[16:19]\n"
    "v_mfma_f32_16x16x32_bf16 v[44:47], a[176:179], a[88:91], v[44:47]\n"
    "ds_read_b64_tr_b16 a[170:171], v217 offset:32768\n"
    "v_mfma_f32_16x16x32_bf16 v[44:47], a[180:183], a[92:95], v[44:47]\n"
    "v_mfma_f32_16x16x32_bf16 v[72:75], a[176:179], a[96:99], v[72:75]\n"
    "ds_read_b64_tr_b16 a[172:173], v216 offset:40960\n"
    "v_mfma_f32_16x16x32_bf16 v[72:75], a[180:183], a[100:103], v[72:75]\n"
    "v_mfma_f32_16x16x32_bf16 v[100:103], a[176:179], a[104:107], v[100:103]\n"
    "ds_read_b64_tr_b16 a[174:175], v217 offset:40960\n"
    "v_mfma_f32_16x16x32_bf16 v[100:103], a[180:183], a[108:111], v[100:103]\n"
    "v_mfma_f32_16x16x32_bf16 v[20:23], a[184:187], a[80:83], v[20:23]\n"
    "ds_read_b64_tr_b16 a[176:177], v210 offset:49152\n"
    "s_waitcnt vmcnt(5)\n"
    "s_barrier\n"
    "v_mfma_f32_16x16x32_bf16 v[20:23], a[188:191], a[84:87], v[20:23]\n"
    "v_mfma_f32_16x16x32_bf16 v[48:51], a[184:187], a[88:91], v[48:51]\n"
    "ds_read_b64_tr_b16 a[178:179], v211 offset:49152\n"
    "ds_read_b128 a[112:115], v208 offset:32768\n"
    "v_mfma_f32_16x16x32_bf16 v[48:51], a[188:191], a[92:95], v[48:51]\n"
    "v_mfma_f32_16x16x32_bf16 v[76:79], a[184:187], a[96:99], v[76:79]\n"
    "ds_read_b64_tr_b16 a[180:181], v210 offset:57344\n"
    "ds_read_b128 a[116:119], v209 offset:32768\n"
    "v_mfma_f32_16x16x32_bf16 v[76:79], a[188:191], a[100:103], v[76:79]\n"
    "v_mfma_f32_16x16x32_bf16 v[104:107], a[184:187], a[104:107], v[104:107]\n"
    "ds_read_b64_tr_b16 a[182:183], v211 offset:57344\n"
    "v_mfma_f32_16x16x32_bf16 v[104:107], a[188:191], a[108:111], v[104:107]\n"
    "ds_read_b128 a[120:123], v208 offset:34816\n"
    "v_mfma_f32_16x16x32_bf16 v[24:27], a[192:195], a[80:83], v[24:27]\n"
    "ds_read_b64_tr_b16 a[184:185], v212 offset:49152\n"
    "v_mfma_f32_16x16x32_bf16 v[24:27], a[196:199], a[84:87], v[24:27]\n"
    "ds_read_b128 a[124:127], v209 offset:34816\n"
    "v_mfma_f32_16x16x32_bf16 v[52:55], a[192:195], a[88:91], v[52:55]\n"
    "ds_read_b64_tr_b16 a[186:187], v213 offset:49152\n"
    "v_mfma_f32_16x16x32_bf16 v[52:55], a[196:199], a[92:95], v[52:55]\n"
    "v_mfma_f32_16x16x32_bf16 v[80:83], a[192:195], a[96:99], v[80:83]\n"
    "ds_read_b64_tr_b16 a[188:189], v212 offset:57344\n"
    "ds_read_b128 a[128:131], v208 offset:36864\n"
    "v_mfma_f32_16x16x32_bf16 v[80:83], a[196:199], a[100:103], v[80:83]\n"
    "v_mfma_f32_16x16x32_bf16 v[108:111], a[192:195], a[104:107], v[108:111]\n"
    "ds_read_b64_tr_b16 a[190:191], v213 offset:57344\n"
    "ds_read_b128 a[132:135], v209 offset:36864\n"
    "v_mfma_f32_16x16x32_bf16 v[108:111], a[196:199], a[108:111], v[108:111]\n"
    "v_mfma_f32_16x16x32_bf16 a[32:35], a[200:203], a[80:83], a[32:35]\n"
    "ds_read_b64_tr_b16 a[192:193], v214 offset:49152\n"
    "s_waitcnt lgkmcnt(15)\n"
    "v_mfma_f32_16x16x32_bf16 a[32:35], a[204:207], a[84:87], a[32:35]\n"
    "ds_read_b128 a[136:139], v208 offset:38912\n"
    "v_mfma_f32_16x16x32_bf16 a[36:39], a[200:203], a[88:91], a[36:39]\n"
    "ds_read_b64_tr_b16 a[194:195], v215 offset:49152\n"
    "v_mfma_f32_16x16x32_bf16 a[36:39], a[204:207], a[92:95], a[36:39]\n"
    "ds_read_b128 a[140:143], v209 offset:38912\n"
    "v_mfma_f32_16x16x32_bf16 a[40:43], a[200:203], a[96:99], a[40:43]\n"
    "ds_read_b64_tr_b16 a[196:197], v214 offset:57344\n"
    "v_mfma_f32_16x16x32_bf16 a[40:43], a[204:207], a[100:103], a[40:43]\n"
    "v_mfma_f32_16x16x32_bf16 a[44:47], a[200:203], a[104:107], a[44:47]\n"
    "ds_read_b64_tr_b16 a[198:199], v215 offset:57344\n"
    "v_mfma_f32_16x16x32_bf16 a[44:47], a[204:207], a[108:111], a[44:47]\n"
    "v_mfma_f32_16x16x32_bf16 a[0:3], a[80:83], a[208:211], a[0:3]\n"
    "ds_read_b64_tr_b16 a[200:201], v216 offset:49152\n"
    "v_mfma_f32_16x16x32_bf16 a[0:3], a[84:87], a[212:215], a[0:3]\n"
    "v_mfma_f32_16x16x32_bf16 a[4:7], a[88:91], a[208:211], a[4:7]\n"
    "ds_read_b64_tr_b16 a[202:203], v217 offset:49152\n"
    "v_mfma_f32_16x16x32_bf16 a[4:7], a[92:95], a[212:215], a[4:7]\n"
    "v_mfma_f32_16x16x32_bf16 a[8:11], a[96:99], a[208:211], a[8:11]\n"
    "ds_read_b64_tr_b16 a[204:205], v216 offset:57344\n"
    "v_mfma_f32_16x16x32_bf16 a[8:11], a[100:103], a[212:215], a[8:11]\n"
    "v_mfma_f32_16x16x32_bf16 a[12:15], a[104:107], a[208:211], a[12:15]\n"
    "ds_read_b64_tr_b16 a[206:207], v217 offset:57344\n"
    "v_mfma_f32_16x16x32_bf16 a[12:15], a[108:111], a[212:215], a[12:15]\n"
    "s_waitcnt lgkmcnt(8)\n"
    "v_mfma_f32_16x16x32_bf16 v[0:3], a[144:147], a[112:115], v[0:3]\n"
    "ds_read_b128 a[208:211], v218 offset:4096\n"
    "v_mfma_f32_16x16x32_bf16 v[0:3], a[148:151], a[116:119], v[0:3]\n"
    "ds_read_b128 a[212:215], v219 offset:4096\n"
    "v_mfma_f32_16x16x32_bf16 v[28:31], a[144:147], a[120:123], v[28:31]\n"
    "ds_read_b128 a[80:83], v208 offset:8192\n"
    "v_mfma_f32_16x16x32_bf16 v[28:31], a[148:151], a[124:127], v[28:31]\n"
    "v_mfma_f32_16x16x32_bf16 v[56:59], a[144:147], a[128:131], v[56:59]\n"
    "ds_read_b128 a[84:87], v209 offset:8192\n"
    "v_mfma_f32_16x16x32_bf16 v[56:59], a[148:151], a[132:135], v[56:59]\n"
    "v_mfma_f32_16x16x32_bf16 v[84:87], a[144:147], a[136:139], v[84:87]\n"
    "s_waitcnt lgkmcnt(10)\n"
    "v_mfma_f32_16x16x32_bf16 v[84:87], a[148:151], a[140:143], v[84:87]\n"
    "ds_read_b128 a[88:91], v208 offset:10240\n"
    "v_mfma_f32_16x16x32_bf16 v[4:7], a[152:155], a[112:115], v[4:7]\n"
    "ds_read_b64_tr_b16 a[144:145], v210 offset:0\n"
    "v_mfma_f32_16x16x32_bf16 v[4:7], a[156:159], a[116:119], v[4:7]\n"
    "ds_read_b128 a[92:95], v209 offset:10240\n"
    "v_mfma_f32_16x16x32_bf16 v[32:35], a[152:155], a[120:123], v[32:35]\n"
    "ds_read_b64_tr_b16 a[146:147], v211 offset:0\n"
    "v_mfma_f32_16x16x32_bf16 v[32:35], a[156:159], a[124:127], v[32:35]\n"
    "v_mfma_f32_16x16x32_bf16 v[60:63], a[152:155], a[128:131], v[60:63]\n"
    "ds_read_b128 a[96:99], v208 offset:12288\n"
    "ds_read_b64_tr_b16 a[148:149], v210 offset:8192\n"
    "v_mfma_f32_16x16x32_bf16 v[60:63], a[156:159], a[132:135], v[60:63]\n"
    "v_mfma_f32_16x16x32_bf16 v[88:91], a[152:155], a[136:139], v[88:91]\n"
    "ds_read_b128 a[100:103], v209 offset:12288\n"
    "ds_read_b64_tr_b16 a[150:151], v211 offset:8192\n"
    "v_mfma_f32_16x16x32_bf16 v[88:91], a[156:159], a[140:143], v[88:91]\n"
    "v_mfma_f32_16x16x32_bf16 v[8:11], a[160:163], a[112:115], v[8:11]\n"
    "ds_read_b64_tr_b16 a[152:153], v212 offset:0\n"
    "v_mfma_f32_16x16x32_bf16 v[8:11], a[164:167], a[116:119], v[8:11]\n"
    "ds_read_b128 a[104:107], v208 offset:14336\n"
    "v_mfma_f32_16x16x32_bf16 v[36:39], a[160:163], a[120:123], v[36:39]\n"
    "ds_read_b64_tr_b16 a[154:155], v213 offset:0\n"
    "v_mfma_f32_16x16x32_bf16 v[36:39], a[164:167], a[124:127], v[36:39]\n"
    "ds_read_b128 a[108:111], v209 offset:14336\n"
    "v_mfma_f32_16x16x32_bf16 v[64:67], a[160:163], a[128:131], v[64:67]\n"
    "ds_read_b64_tr_b16 a[156:157], v212 offset:8192\n"
    "v_mfma_f32_16x16x32_bf16 v[64:67], a[164:167], a[132:135], v[64:67]\n"
    "v_mfma_f32_16x16x32_bf16 v[92:95], a[160:163], a[136:139], v[92:95]\n"
    "ds_read_b64_tr_b16 a[158:159], v213 offset:8192\n"
    "v_mfma_f32_16x16x32_bf16 v[92:95], a[164:167], a[140:143], v[92:95]\n"
    "v_mfma_f32_16x16x32_bf16 v[12:15], a[168:171], a[112:115], v[12:15]\n"
    "ds_read_b64_tr_b16 a[160:161], v214 offset:0\n"
    "v_and_b32 v236, 1, v242\n"
    "v_mfma_f32_16x16x32_bf16 v[12:15], a[172:175], a[116:119], v[12:15]\n"
    "v_lshlrev_b32 v236, 1, v236\n"
    "v_mfma_f32_16x16x32_bf16 v[40:43], a[168:171], a[120:123], v[40:43]\n"
    "ds_read_b64_tr_b16 a[162:163], v215 offset:0\n"
    "v_lshrrev_b32 v237, 1, v242\n"
    "v_mfma_f32_16x16x32_bf16 v[40:43], a[172:175], a[124:127], v[40:43]\n"
    "v_or_b32 v236, v236, v237\n"
    "v_mfma_f32_16x16x32_bf16 v[68:71], a[168:171], a[128:131], v[68:71]\n"
    "ds_read_b64_tr_b16 a[164:165], v214 offset:8192\n"
    "v_lshlrev_b32 v236, 8, v236\n"
    "v_mfma_f32_16x16x32_bf16 v[68:71], a[172:175], a[132:135], v[68:71]\n"
    "v_lshl_add_u32 v243, v241, 4, v236\n"
    "v_mfma_f32_16x16x32_bf16 v[96:99], a[168:171], a[136:139], v[96:99]\n"
    "ds_read_b64_tr_b16 a[166:167], v215 offset:8192\n"
    "v_and_b32 v236, 1, v242\n"
    "v_mfma_f32_16x16x32_bf16 v[96:99], a[172:175], a[140:143], v[96:99]\n"
    "v_lshl_add_u32 v236, v236, 4, v241\n"
    "v_mfma_f32_16x16x32_bf16 v[16:19], a[176:179], a[112:115], v[16:19]\n"
    "ds_read_b64_tr_b16 a[168:169], v216 offset:0\n"
    "v_lshlrev_b32 v238, 2, v237\n"
    "v_mfma_f32_16x16x32_bf16 v[16:19], a[180:183], a[116:119], v[16:19]\n"
    "v_xor_b32 v236, v236, v238\n"
    "v_mfma_f32_16x16x32_bf16 v[44:47], a[176:179], a[120:123], v[44:47]\n"
    "ds_read_b64_tr_b16 a[170:171], v217 offset:0\n"
    "v_lshl_add_u32 v236, v237, 5, v236\n"
    "v_mfma_f32_16x16x32_bf16 v[44:47], a[180:183], a[124:127], v[44:47]\n"
    "v_lshlrev_b32 v239, 4, v236\n"
    "v_mfma_f32_16x16x32_bf16 v[72:75], a[176:179], a[128:131], v[72:75]\n"
    "ds_read_b64_tr_b16 a[172:173], v216 offset:8192\n"
    "s_cmp_eq_u32 s62, 0\n"
    "v_mfma_f32_16x16x32_bf16 v[72:75], a[180:183], a[132:135], v[72:75]\n"
    "s_cselect_b32 s56, s28, s30\n"
    "v_mfma_f32_16x16x32_bf16 v[100:103], a[176:179], a[136:139], v[100:103]\n"
    "ds_read_b64_tr_b16 a[174:175], v217 offset:8192\n"
    "s_cselect_b32 s57, s29, s31\n"
    "v_mfma_f32_16x16x32_bf16 v[100:103], a[180:183], a[140:143], v[100:103]\n"
    "s_and_b32 s57, s57, 0xffff\n"
    "v_mfma_f32_16x16x32_bf16 v[20:23], a[184:187], a[112:115], v[20:23]\n"
    "ds_read_b64_tr_b16 a[176:177], v210 offset:16384\n"
    "s_mov_b32 s58, 4194304\n"
    "v_mfma_f32_16x16x32_bf16 v[20:23], a[188:191], a[116:119], v[20:23]\n"
    "s_mov_b32 s59, 0x20000\n"
    "v_mfma_f32_16x16x32_bf16 v[48:51], a[184:187], a[120:123], v[48:51]\n"
    "ds_read_b64_tr_b16 a[178:179], v211 offset:16384\n"
    "s_lshl_b32 s66, s64, 19\n"
    "v_mfma_f32_16x16x32_bf16 v[48:51], a[188:191], a[124:127], v[48:51]\n"
    "s_lshl_b32 s69, s61, 18\n"
    "v_mfma_f32_16x16x32_bf16 v[76:79], a[184:187], a[128:131], v[76:79]\n"
    "ds_read_b64_tr_b16 a[180:181], v210 offset:24576\n"
    "s_add_u32 s66, s66, s69\n"
    "v_mfma_f32_16x16x32_bf16 v[76:79], a[188:191], a[132:135], v[76:79]\n"
    "s_lshl_b32 s69, s63, 11\n"
    "v_mfma_f32_16x16x32_bf16 v[104:107], a[184:187], a[136:139], v[104:107]\n"
    "ds_read_b64_tr_b16 a[182:183], v211 offset:24576\n"
    "s_add_u32 s66, s66, s69\n"
    "v_mfma_f32_16x16x32_bf16 v[104:107], a[188:191], a[140:143], v[104:107]\n"
    "s_mov_b32 s52, s26\n"
    "v_mfma_f32_16x16x32_bf16 v[24:27], a[192:195], a[112:115], v[24:27]\n"
    "ds_read_b64_tr_b16 a[184:185], v212 offset:16384\n"
    "s_and_b32 s53, s27, 0xffff\n"
    "s_waitcnt lgkmcnt(15)\n"
    "v_mfma_f32_16x16x32_bf16 v[24:27], a[196:199], a[116:119], v[24:27]\n"
    "s_mov_b32 s54, 14680064\n"
    "v_mfma_f32_16x16x32_bf16 v[52:55], a[192:195], a[120:123], v[52:55]\n"
    "ds_read_b64_tr_b16 a[186:187], v213 offset:16384\n"
    "s_mov_b32 s55, 0x20000\n"
    "v_mfma_f32_16x16x32_bf16 v[52:55], a[196:199], a[124:127], v[52:55]\n"
    "s_lshl_b32 s65, s63, 1\n"
    "v_mfma_f32_16x16x32_bf16 v[80:83], a[192:195], a[128:131], v[80:83]\n"
    "ds_read_b64_tr_b16 a[188:189], v212 offset:24576\n"
    "s_add_u32 s65, s65, s62\n"
    "v_mfma_f32_16x16x32_bf16 v[80:83], a[196:199], a[132:135], v[80:83]\n"
    "s_lshl_b32 s65, s65, 18\n"
    "v_mfma_f32_16x16x32_bf16 v[108:111], a[192:195], a[136:139], v[108:111]\n"
    "ds_read_b64_tr_b16 a[190:191], v213 offset:24576\n"
    "s_lshl_b32 s69, s64, 15\n"
    "v_mfma_f32_16x16x32_bf16 v[108:111], a[196:199], a[140:143], v[108:111]\n"
    "s_add_u32 s65, s65, s69\n"
    "v_mfma_f32_16x16x32_bf16 a[32:35], a[200:203], a[112:115], a[32:35]\n"
    "ds_read_b64_tr_b16 a[192:193], v214 offset:16384\n"
    "s_lshl_b32 s69, s61, 14\n"
    "v_mfma_f32_16x16x32_bf16 a[32:35], a[204:207], a[116:119], a[32:35]\n"
    "s_add_u32 s65, s65, s69\n"
    "v_mfma_f32_16x16x32_bf16 a[36:39], a[200:203], a[120:123], a[36:39]\n"
    "ds_read_b64_tr_b16 a[194:195], v215 offset:16384\n"
    "s_mov_b32 s70, 0xbfb8aa3b\n"
    "v_mfma_f32_16x16x32_bf16 a[36:39], a[204:207], a[124:127], a[36:39]\n"
    "s_mov_b32 s71, 0xbfb8aa3b\n"
    "v_mfma_f32_16x16x32_bf16 a[40:43], a[200:203], a[128:131], a[40:43]\n"
    "ds_read_b64_tr_b16 a[196:197], v214 offset:24576\n"
    "v_mfma_f32_16x16x32_bf16 a[40:43], a[204:207], a[132:135], a[40:43]\n"
    "v_mfma_f32_16x16x32_bf16 a[44:47], a[200:203], a[136:139], a[44:47]\n"
    "ds_read_b64_tr_b16 a[198:199], v215 offset:24576\n"
    "v_mfma_f32_16x16x32_bf16 a[44:47], a[204:207], a[140:143], a[44:47]\n"
    "v_mfma_f32_16x16x32_bf16 a[0:3], a[112:115], a[208:211], a[0:3]\n"
    "ds_read_b64_tr_b16 a[200:201], v216 offset:16384\n"
    "v_mfma_f32_16x16x32_bf16 a[0:3], a[116:119], a[212:215], a[0:3]\n"
    "v_mfma_f32_16x16x32_bf16 a[4:7], a[120:123], a[208:211], a[4:7]\n"
    "ds_read_b64_tr_b16 a[202:203], v217 offset:16384\n"
    "v_mfma_f32_16x16x32_bf16 a[4:7], a[124:127], a[212:215], a[4:7]\n"
    "v_mfma_f32_16x16x32_bf16 a[8:11], a[128:131], a[208:211], a[8:11]\n"
    "ds_read_b64_tr_b16 a[204:205], v216 offset:24576\n"
    "v_mfma_f32_16x16x32_bf16 a[8:11], a[132:135], a[212:215], a[8:11]\n"
    "v_mfma_f32_16x16x32_bf16 a[12:15], a[136:139], a[208:211], a[12:15]\n"
    "ds_read_b64_tr_b16 a[206:207], v217 offset:24576\n"
    "v_mfma_f32_16x16x32_bf16 a[12:15], a[140:143], a[212:215], a[12:15]\n"
    "s_waitcnt vmcnt(1)\n"
    "s_barrier\n"
    "v_mfma_f32_16x16x32_bf16 v[112:115], a[144:147], a[80:83], v[112:115]\n"
    "ds_read_b128 a[208:211], v218 offset:0\n"
    "v_mfma_f32_16x16x32_bf16 v[112:115], a[148:151], a[84:87], v[112:115]\n"
    "ds_read_b128 a[212:215], v219 offset:0\n"
    "v_mfma_f32_16x16x32_bf16 v[136:139], a[144:147], a[88:91], v[136:139]\n"
    "ds_read_b128 a[112:115], v208 offset:40960\n"
    "v_mfma_f32_16x16x32_bf16 v[136:139], a[148:151], a[92:95], v[136:139]\n"
    "v_mfma_f32_16x16x32_bf16 v[160:163], a[144:147], a[96:99], v[160:163]\n"
    "ds_read_b128 a[116:119], v209 offset:40960\n"
    "v_mfma_f32_16x16x32_bf16 v[160:163], a[148:151], a[100:103], v[160:163]\n"
    "v_mfma_f32_16x16x32_bf16 v[184:187], a[144:147], a[104:107], v[184:187]\n"
    "v_mfma_f32_16x16x32_bf16 v[184:187], a[148:151], a[108:111], v[184:187]\n"
    "ds_read_b128 a[120:123], v208 offset:43008\n"
    "v_mfma_f32_16x16x32_bf16 v[116:119], a[152:155], a[80:83], v[116:119]\n"
    "ds_read_b64_tr_b16 a[144:145], v210 offset:32768\n"
    "s_waitcnt lgkmcnt(15)\n"
    "v_mfma_f32_16x16x32_bf16 v[116:119], a[156:159], a[84:87], v[116:119]\n"
    "ds_read_b128 a[124:127], v209 offset:43008\n"
    "v_mfma_f32_16x16x32_bf16 v[140:143], a[152:155], a[88:91], v[140:143]\n"
    "ds_read_b64_tr_b16 a[146:147], v211 offset:32768\n"
    "v_accvgpr_read_b32 v244, a0\n"
    "v_accvgpr_read_b32 v245, a1\n"
    "v_accvgpr_read_b32 v246, a2\n"
    "v_accvgpr_read_b32 v247, a3\n"
    "v_mfma_f32_16x16x32_bf16 v[140:143], a[156:159], a[92:95], v[140:143]\n"
    "v_cvt_pk_bf16_f32 v228, v244, v245\n"
    "v_cvt_pk_bf16_f32 v229, v246, v247\n"
    "v_accvgpr_read_b32 v248, a4\n"
    "v_mfma_f32_16x16x32_bf16 v[164:167], a[152:155], a[96:99], v[164:167]\n"
    "ds_read_b128 a[128:131], v208 offset:45056\n"
    "ds_read_b64_tr_b16 a[148:149], v210 offset:40960\n"
    "v_accvgpr_read_b32 v249, a5\n"
    "v_accvgpr_read_b32 v250, a6\n"
    "v_accvgpr_read_b32 v251, a7\n"
    "v_cvt_pk_bf16_f32 v230, v248, v249\n"
    "v_mfma_f32_16x16x32_bf16 v[164:167], a[156:159], a[100:103], v[164:167]\n"
    "v_cvt_pk_bf16_f32 v231, v250, v251\n"
    "s_nop 1\n"
    "v_permlane16_swap_b32 v228, v230\n"
    "v_mfma_f32_16x16x32_bf16 v[188:191], a[152:155], a[104:107], v[188:191]\n"
    "ds_read_b128 a[132:135], v209 offset:45056\n"
    "ds_read_b64_tr_b16 a[150:151], v211 offset:40960\n"
    "v_permlane16_swap_b32 v229, v231\n"
    "s_add_u32 s69, s66, 0\n"
    "buffer_store_dwordx4 v[228:231], v243, s[56:59], s69 offen offset:0 sc1\n"
    "v_accvgpr_read_b32 v244, a8\n"
    "v_mfma_f32_16x16x32_bf16 v[188:191], a[156:159], a[108:111], v[188:191]\n"
    "v_accvgpr_read_b32 v245, a9\n"
    "v_accvgpr_read_b32 v246, a10\n"
    "v_accvgpr_read_b32 v247, a11\n"
    "v_mfma_f32_16x16x32_bf16 v[120:123], a[160:163], a[80:83], v[120:123]\n"
    "ds_read_b64_tr_b16 a[152:153], v212 offset:32768\n"
    "v_cvt_pk_bf16_f32 v232, v244, v245\n"
    "v_cvt_pk_bf16_f32 v233, v246, v247\n"
    "v_accvgpr_read_b32 v248, a12\n"
    "v_mfma_f32_16x16x32_bf16 v[120:123], a[164:167], a[84:87], v[120:123]\n"
    "ds_read_b128 a[136:139], v208 offset:47104\n"
    "v_accvgpr_read_b32 v249, a13\n"
    "v_accvgpr_read_b32 v250, a14\n"
    "v_accvgpr_read_b32 v251, a15\n"
    "v_cvt_pk_bf16_f32 v234, v248, v249\n"
    "v_mfma_f32_16x16x32_bf16 v[144:147], a[160:163], a[88:91], v[144:147]\n"
    "ds_read_b64_tr_b16 a[154:155], v213 offset:32768\n"
    "v_cvt_pk_bf16_f32 v235, v250, v251\n"
    "s_nop 1\n"
    "v_permlane16_swap_b32 v232, v234\n"
    "v_mfma_f32_16x16x32_bf16 v[144:147], a[164:167], a[92:95], v[144:147]\n"
    "ds_read_b128 a[140:143], v209 offset:47104\n"
    "v_permlane16_swap_b32 v233, v235\n"
    "s_add_u32 s69, s66, 0\n"
    "buffer_store_dwordx4 v[232:235], v243, s[56:59], s69 offen offset:1024 sc1\n"
    "v_pk_mul_f32 v[236:237], v[0:1], s[70:71]\n"
    "v_mfma_f32_16x16x32_bf16 v[168:171], a[160:163], a[96:99], v[168:171]\n"
    "ds_read_b64_tr_b16 a[156:157], v212 offset:40960\n"
    "v_pk_mul_f32 v[238:239], v[2:3], s[70:71]\n"
    "v_exp_f32 v236, v236\n"
    "v_exp_f32 v237, v237\n"
    "v_mfma_f32_16x16x32_bf16 v[168:171], a[164:167], a[100:103], v[168:171]\n"
    "v_exp_f32 v238, v238\n"
    "v_exp_f32 v239, v239\n"
    "v_pk_mul_f32 v[0:1], v[0:1], v[16:17]\n"
    "v_mfma_f32_16x16x32_bf16 v[192:195], a[160:163], a[104:107], v[192:195]\n"
    "ds_read_b64_tr_b16 a[158:159], v213 offset:40960\n"
    "v_pk_mul_f32 v[2:3], v[2:3], v[18:19]\n"
    "v_pk_add_f32 v[236:237], v[236:237], 1.0 op_sel_hi:[1,0]\n"
    "v_pk_add_f32 v[238:239], v[238:239], 1.0 op_sel_hi:[1,0]\n"
    "v_rcp_f32 v236, v236\n"
    "v_mfma_f32_16x16x32_bf16 v[192:195], a[164:167], a[108:111], v[192:195]\n"
    "v_rcp_f32 v237, v237\n"
    "v_rcp_f32 v238, v238\n"
    "v_rcp_f32 v239, v239\n"
    "v_mfma_f32_16x16x32_bf16 v[124:127], a[168:171], a[80:83], v[124:127]\n"
    "ds_read_b64_tr_b16 a[160:161], v214 offset:32768\n"
    "s_nop 0\n"
    "v_pk_mul_f32 v[0:1], v[0:1], v[236:237]\n"
    "v_pk_mul_f32 v[2:3], v[2:3], v[238:239]\n"
    "v_cvt_pk_bf16_f32 v220, v0, v1\n"
    "v_mfma_f32_16x16x32_bf16 v[124:127], a[172:175], a[84:87], v[124:127]\n"
    "v_cvt_pk_bf16_f32 v221, v2, v3\n"
    "v_pk_mul_f32 v[236:237], v[4:5], s[70:71]\n"
    "v_pk_mul_f32 v[238:239], v[6:7], s[70:71]\n"
    "v_mfma_f32_16x16x32_bf16 v[148:151], a[168:171], a[88:91], v[148:151]\n"
    "ds_read_b64_tr_b16 a[162:163], v215 offset:32768\n"
    "v_exp_f32 v236, v236\n"
    "v_exp_f32 v237, v237\n"
    "v_exp_f32 v238, v238\n"
    "v_mfma_f32_16x16x32_bf16 v[148:151], a[172:175], a[92:95], v[148:151]\n"
    "v_exp_f32 v239, v239\n"
    "v_pk_mul_f32 v[4:5], v[4:5], v[20:21]\n"
    "v_pk_mul_f32 v[6:7], v[6:7], v[22:23]\n"
    "v_pk_add_f32 v[236:237], v[236:237], 1.0 op_sel_hi:[1,0]\n"
    "v_mfma_f32_16x16x32_bf16 v[172:175], a[168:171], a[96:99], v[172:175]\n"
    "ds_read_b64_tr_b16 a[164:165], v214 offset:40960\n"
    "v_pk_add_f32 v[238:239], v[238:239], 1.0 op_sel_hi:[1,0]\n"
    "v_rcp_f32 v236, v236\n"
    "v_rcp_f32 v237, v237\n"
    "v_mfma_f32_16x16x32_bf16 v[172:175], a[172:175], a[100:103], v[172:175]\n"
    "v_rcp_f32 v238, v238\n"
    "v_rcp_f32 v239, v239\n"
    "s_nop 0\n"
    "v_pk_mul_f32 v[4:5], v[4:5], v[236:237]\n"
    "v_mfma_f32_16x16x32_bf16 v[196:199], a[168:171], a[104:107], v[196:199]\n"
    "ds_read_b64_tr_b16 a[166:167], v215 offset:40960\n"
    "v_pk_mul_f32 v[6:7], v[6:7], v[238:239]\n"
    "v_cvt_pk_bf16_f32 v222, v4, v5\n"
    "v_cvt_pk_bf16_f32 v223, v6, v7\n"
    "v_mfma_f32_16x16x32_bf16 v[196:199], a[172:175], a[108:111], v[196:199]\n"
    "s_nop 1\n"
    "v_permlane16_swap_b32 v220, v222\n"
    "v_permlane16_swap_b32 v221, v223\n"
    "v_mfma_f32_16x16x32_bf16 v[128:131], a[176:179], a[80:83], v[128:131]\n"
    "ds_read_b64_tr_b16 a[168:169], v216 offset:32768\n"
    "buffer_store_dwordx4 v[220:223], v243, s[52:55], s65 offen offset:0 sc1\n"
    "v_pk_mul_f32 v[236:237], v[8:9], s[70:71]\n"
    "v_pk_mul_f32 v[238:239], v[10:11], s[70:71]\n"
    "v_exp_f32 v236, v236\n"
    "v_mfma_f32_16x16x32_bf16 v[128:131], a[180:183], a[84:87], v[128:131]\n"
    "v_exp_f32 v237, v237\n"
    "v_exp_f32 v238, v238\n"
    "v_exp_f32 v239, v239\n"
    "v_mfma_f32_16x16x32_bf16 v[152:155], a[176:179], a[88:91], v[152:155]\n"
    "ds_read_b64_tr_b16 a[170:171], v217 offset:32768\n"
    "v_pk_mul_f32 v[8:9], v[8:9], v[24:25]\n"
    "v_pk_mul_f32 v[10:11], v[10:11], v[26:27]\n"
    "v_pk_add_f32 v[236:237], v[236:237], 1.0 op_sel_hi:[1,0]\n"
    "v_pk_add_f32 v[238:239], v[238:239], 1.0 op_sel_hi:[1,0]\n"
    "v_mfma_f32_16x16x32_bf16 v[152:155], a[180:183], a[92:95], v[152:155]\n"
    "v_rcp_f32 v236, v236\n"
    "v_rcp_f32 v237, v237\n"
    "v_rcp_f32 v238, v238\n"
    "v_mfma_f32_16x16x32_bf16 v[176:179], a[176:179], a[96:99], v[176:179]\n"
    "ds_read_b64_tr_b16 a[172:173], v216 offset:40960\n"
    "v_rcp_f32 v239, v239\n"
    "s_nop 0\n"
    "v_pk_mul_f32 v[8:9], v[8:9], v[236:237]\n"
    "v_mfma_f32_16x16x32_bf16 v[176:179], a[180:183], a[100:103], v[176:179]\n"
    "v_pk_mul_f32 v[10:11], v[10:11], v[238:239]\n"
    "v_cvt_pk_bf16_f32 v224, v8, v9\n"
    "v_cvt_pk_bf16_f32 v225, v10, v11\n"
    "v_accvgpr_read_b32 v244, a32\n"
    "v_mfma_f32_16x16x32_bf16 v[200:203], a[176:179], a[104:107], v[200:203]\n"
    "ds_read_b64_tr_b16 a[174:175], v217 offset:40960\n"
    "v_accvgpr_read_b32 v245, a33\n"
    "v_accvgpr_read_b32 v246, a34\n"
    "v_accvgpr_read_b32 v247, a35\n"
    "v_mfma_f32_16x16x32_bf16 v[200:203], a[180:183], a[108:111], v[200:203]\n"
    "v_pk_mul_f32 v[236:237], v[12:13], s[70:71]\n"
    "v_pk_mul_f32 v[238:239], v[14:15], s[70:71]\n"
    "v_exp_f32 v236, v236\n"
    "v_exp_f32 v237, v237\n"
    "v_mfma_f32_16x16x32_bf16 v[132:135], a[184:187], a[80:83], v[132:135]\n"
    "ds_read_b64_tr_b16 a[176:177], v210 offset:49152\n"
    "v_exp_f32 v238, v238\n"
    "v_exp_f32 v239, v239\n"
    "v_pk_mul_f32 v[12:13], v[12:13], v[244:245]\n"
    "s_waitcnt lgkmcnt(15)\n"
    "v_mfma_f32_16x16x32_bf16 v[132:135], a[188:191], a[84:87], v[132:135]\n"
    "v_pk_mul_f32 v[14:15], v[14:15], v[246:247]\n"
    "v_pk_add_f32 v[236:237], v[236:237], 1.0 op_sel_hi:[1,0]\n"
    "v_pk_add_f32 v[238:239], v[238:239], 1.0 op_sel_hi:[1,0]\n"
    "v_mfma_f32_16x16x32_bf16 v[156:159], a[184:187], a[88:91], v[156:159]\n"
    "ds_read_b64_tr_b16 a[178:179], v211 offset:49152\n"
    "v_rcp_f32 v236, v236\n"
    "v_rcp_f32 v237, v237\n"
    "v_rcp_f32 v238, v238\n"
    "v_rcp_f32 v239, v239\n"
    "v_mfma_f32_16x16x32_bf16 v[156:159], a[188:191], a[92:95], v[156:159]\n"
    "s_nop 0\n"
    "v_pk_mul_f32 v[12:13], v[12:13], v[236:237]\n"
    "v_pk_mul_f32 v[14:15], v[14:15], v[238:239]\n"
    "v_mfma_f32_16x16x32_bf16 v[180:183], a[184:187], a[96:99], v[180:183]\n"
    "ds_read_b64_tr_b16 a[180:181], v210 offset:57344\n"
    "v_cvt_pk_bf16_f32 v226, v12, v13\n"
    "v_cvt_pk_bf16_f32 v227, v14, v15\n"
    "s_nop 1\n"
    "v_permlane16_swap_b32 v224, v226\n"
    "v_mfma_f32_16x16x32_bf16 v[180:183], a[188:191], a[100:103], v[180:183]\n"
    "v_permlane16_swap_b32 v225, v227\n"
    "buffer_store_dwordx4 v[224:227], v243, s[52:55], s65 offen offset:1024 sc1\n"
    "s_add_u32 s65, s65, 2048\n"
    "v_mfma_f32_16x16x32_bf16 v[204:207], a[184:187], a[104:107], v[204:207]\n"
    "ds_read_b64_tr_b16 a[182:183], v211 offset:57344\n"
    "v_pk_mul_f32 v[236:237], v[28:29], s[70:71]\n"
    "v_pk_mul_f32 v[238:239], v[30:31], s[70:71]\n"
    "v_exp_f32 v236, v236\n"
    "v_mfma_f32_16x16x32_bf16 v[204:207], a[188:191], a[108:111], v[204:207]\n"
    "v_exp_f32 v237, v237\n"
    "v_exp_f32 v238, v238\n"
    "v_exp_f32 v239, v239\n"
    "v_pk_mul_f32 v[28:29], v[28:29], v[44:45]\n"
    "v_mfma_f32_16x16x32_bf16 a[64:67], a[192:195], a[80:83], a[64:67]\n"
    "ds_read_b64_tr_b16 a[184:185], v212 offset:49152\n"
    "v_pk_mul_f32 v[30:31], v[30:31], v[46:47]\n"
    "v_pk_add_f32 v[236:237], v[236:237], 1.0 op_sel_hi:[1,0]\n"
    "v_pk_add_f32 v[238:239], v[238:239], 1.0 op_sel_hi:[1,0]\n"
    "v_mfma_f32_16x16x32_bf16 a[64:67], a[196:199], a[84:87], a[64:67]\n"
    "v_rcp_f32 v236, v236\n"
    "v_rcp_f32 v237, v237\n"
    "v_rcp_f32 v238, v238\n"
    "v_rcp_f32 v239, v239\n"
    "v_mfma_f32_16x16x32_bf16 a[68:71], a[192:195], a[88:91], a[68:71]\n"
    "ds_read_b64_tr_b16 a[186:187], v213 offset:49152\n"
    "s_nop 0\n"
    "v_pk_mul_f32 v[28:29], v[28:29], v[236:237]\n"
    "v_pk_mul_f32 v[30:31], v[30:31], v[238:239]\n"
    "v_mfma_f32_16x16x32_bf16 a[68:71], a[196:199], a[92:95], a[68:71]\n"
    "v_cvt_pk_bf16_f32 v220, v28, v29\n"
    "v_cvt_pk_bf16_f32 v221, v30, v31\n"
    "v_pk_mul_f32 v[236:237], v[32:33], s[70:71]\n"
    "v_mfma_f32_16x16x32_bf16 a[72:75], a[192:195], a[96:99], a[72:75]\n"
    "ds_read_b64_tr_b16 a[188:189], v212 offset:57344\n"
    "v_pk_mul_f32 v[238:239], v[34:35], s[70:71]\n"
    "v_exp_f32 v236, v236\n"
    "v_exp_f32 v237, v237\n"
    "v_exp_f32 v238, v238\n"
    "v_mfma_f32_16x16x32_bf16 a[72:75], a[196:199], a[100:103], a[72:75]\n"
    "v_exp_f32 v239, v239\n"
    "v_pk_mul_f32 v[32:33], v[32:33], v[48:49]\n"
    "v_pk_mul_f32 v[34:35], v[34:35], v[50:51]\n"
    "v_mfma_f32_16x16x32_bf16 a[76:79], a[192:195], a[104:107], a[76:79]\n"
    "ds_read_b64_tr_b16 a[190:191], v213 offset:57344\n"
    "v_pk_add_f32 v[236:237], v[236:237], 1.0 op_sel_hi:[1,0]\n"
    "v_pk_add_f32 v[238:239], v[238:239], 1.0 op_sel_hi:[1,0]\n"
    "v_rcp_f32 v236, v236\n"
    "v_rcp_f32 v237, v237\n"
    "v_mfma_f32_16x16x32_bf16 a[76:79], a[196:199], a[108:111], a[76:79]\n"
    "v_rcp_f32 v238, v238\n"
    "v_rcp_f32 v239, v239\n"
    "s_nop 0\n"
    "v_mfma_f32_16x16x32_bf16 a[48:51], a[200:203], a[80:83], a[48:51]\n"
    "ds_read_b64_tr_b16 a[192:193], v214 offset:49152\n"
    "v_pk_mul_f32 v[32:33], v[32:33], v[236:237]\n"
    "v_pk_mul_f32 v[34:35], v[34:35], v[238:239]\n"
    "v_cvt_pk_bf16_f32 v222, v32, v33\n"
    "v_mfma_f32_16x16x32_bf16 a[48:51], a[204:207], a[84:87], a[48:51]\n"
    "v_cvt_pk_bf16_f32 v223, v34, v35\n"
    "s_nop 1\n"
    "v_permlane16_swap_b32 v220, v222\n"
    "v_permlane16_swap_b32 v221, v223\n"
    "v_mfma_f32_16x16x32_bf16 a[52:55], a[200:203], a[88:91], a[52:55]\n"
    "ds_read_b64_tr_b16 a[194:195], v215 offset:49152\n"
    "buffer_store_dwordx4 v[220:223], v243, s[52:55], s65 offen offset:0 sc1\n"
    "v_pk_mul_f32 v[236:237], v[36:37], s[70:71]\n"
    "v_pk_mul_f32 v[238:239], v[38:39], s[70:71]\n"
    "v_mfma_f32_16x16x32_bf16 a[52:55], a[204:207], a[92:95], a[52:55]\n"
    "v_exp_f32 v236, v236\n"
    "v_exp_f32 v237, v237\n"
    "v_exp_f32 v238, v238\n"
    "v_exp_f32 v239, v239\n"
    "v_mfma_f32_16x16x32_bf16 a[56:59], a[200:203], a[96:99], a[56:59]\n"
    "ds_read_b64_tr_b16 a[196:197], v214 offset:57344\n"
    "v_pk_mul_f32 v[36:37], v[36:37], v[52:53]\n"
    "v_pk_mul_f32 v[38:39], v[38:39], v[54:55]\n"
    "v_pk_add_f32 v[236:237], v[236:237], 1.0 op_sel_hi:[1,0]\n"
    "v_mfma_f32_16x16x32_bf16 a[56:59], a[204:207], a[100:103], a[56:59]\n"
    "v_pk_add_f32 v[238:239], v[238:239], 1.0 op_sel_hi:[1,0]\n"
    "v_rcp_f32 v236, v236\n"
    "v_rcp_f32 v237, v237\n"
    "v_mfma_f32_16x16x32_bf16 a[60:63], a[200:203], a[104:107], a[60:63]\n"
    "ds_read_b64_tr_b16 a[198:199], v215 offset:57344\n"
    "v_rcp_f32 v238, v238\n"
    "v_rcp_f32 v239, v239\n"
    "s_nop 0\n"
    "v_pk_mul_f32 v[36:37], v[36:37], v[236:237]\n"
    "v_mfma_f32_16x16x32_bf16 a[60:63], a[204:207], a[108:111], a[60:63]\n"
    "v_pk_mul_f32 v[38:39], v[38:39], v[238:239]\n"
    "v_cvt_pk_bf16_f32 v224, v36, v37\n"
    "v_cvt_pk_bf16_f32 v225, v38, v39\n"
    "v_mfma_f32_16x16x32_bf16 a[16:19], a[80:83], a[208:211], a[16:19]\n"
    "ds_read_b64_tr_b16 a[200:201], v216 offset:49152\n"
    "v_accvgpr_read_b32 v244, a36\n"
    "v_accvgpr_read_b32 v245, a37\n"
    "v_accvgpr_read_b32 v246, a38\n"
    "v_accvgpr_read_b32 v247, a39\n"
    "v_mfma_f32_16x16x32_bf16 a[16:19], a[84:87], a[212:215], a[16:19]\n"
    "v_pk_mul_f32 v[236:237], v[40:41], s[70:71]\n"
    "v_pk_mul_f32 v[238:239], v[42:43], s[70:71]\n"
    "v_exp_f32 v236, v236\n"
    "v_mfma_f32_16x16x32_bf16 a[20:23], a[88:91], a[208:211], a[20:23]\n"
    "ds_read_b64_tr_b16 a[202:203], v217 offset:49152\n"
    "v_exp_f32 v237, v237\n"
    "v_exp_f32 v238, v238\n"
    "v_exp_f32 v239, v239\n"
    "v_mfma_f32_16x16x32_bf16 a[20:23], a[92:95], a[212:215], a[20:23]\n"
    "v_pk_mul_f32 v[40:41], v[40:41], v[244:245]\n"
    "v_pk_mul_f32 v[42:43], v[42:43], v[246:247]\n"
    "v_pk_add_f32 v[236:237], v[236:237], 1.0 op_sel_hi:[1,0]\n"
    "v_pk_add_f32 v[238:239], v[238:239], 1.0 op_sel_hi:[1,0]\n"
    "v_mfma_f32_16x16x32_bf16 a[24:27], a[96:99], a[208:211], a[24:27]\n"
    "ds_read_b64_tr_b16 a[204:205], v216 offset:57344\n"
    "v_rcp_f32 v236, v236\n"
    "v_rcp_f32 v237, v237\n"
    "v_rcp_f32 v238, v238\n"
    "v_mfma_f32_16x16x32_bf16 a[24:27], a[100:103], a[212:215], a[24:27]\n"
    "v_rcp_f32 v239, v239\n"
    "s_nop 0\n"
    "v_pk_mul_f32 v[40:41], v[40:41], v[236:237]\n"
    "v_pk_mul_f32 v[42:43], v[42:43], v[238:239]\n"
    "v_mfma_f32_16x16x32_bf16 a[28:31], a[104:107], a[208:211], a[28:31]\n"
    "ds_read_b64_tr_b16 a[206:207], v217 offset:57344\n"
    "v_cvt_pk_bf16_f32 v226, v40, v41\n"
    "v_cvt_pk_bf16_f32 v227, v42, v43\n"
    "s_nop 1\n"
    "v_mfma_f32_16x16x32_bf16 a[28:31], a[108:111], a[212:215], a[28:31]\n"
    "v_permlane16_swap_b32 v224, v226\n"
    "v_permlane16_swap_b32 v225, v227\n"
    "buffer_store_dwordx4 v[224:227], v243, s[52:55], s65 offen offset:1024 sc1\n"
    "v_mfma_f32_16x16x32_bf16 v[112:115], a[144:147], a[112:115], v[112:115]\n"
    "ds_read_b128 a[208:211], v218 offset:4096\n"
    "v_mfma_f32_16x16x32_bf16 v[112:115], a[148:151], a[116:119], v[112:115]\n"
    "ds_read_b128 a[212:215], v219 offset:4096\n"
    "v_mfma_f32_16x16x32_bf16 v[136:139], a[144:147], a[120:123], v[136:139]\n"
    "v_mfma_f32_16x16x32_bf16 v[136:139], a[148:151], a[124:127], v[136:139]\n"
    "s_add_u32 s65, s65, 2048\n"
    "v_pk_mul_f32 v[236:237], v[56:57], s[70:71]\n"
    "v_pk_mul_f32 v[238:239], v[58:59], s[70:71]\n"
    "v_mfma_f32_16x16x32_bf16 v[160:163], a[144:147], a[128:131], v[160:163]\n"
    "v_exp_f32 v236, v236\n"
    "v_exp_f32 v237, v237\n"
    "v_exp_f32 v238, v238\n"
    "v_mfma_f32_16x16x32_bf16 v[160:163], a[148:151], a[132:135], v[160:163]\n"
    "v_exp_f32 v239, v239\n"
    "v_pk_mul_f32 v[56:57], v[56:57], v[72:73]\n"
    "s_waitcnt lgkmcnt(15)\n"
    "v_mfma_f32_16x16x32_bf16 v[184:187], a[144:147], a[136:139], v[184:187]\n"
    "v_pk_mul_f32 v[58:59], v[58:59], v[74:75]\n"
    "v_pk_add_f32 v[236:237], v[236:237], 1.0 op_sel_hi:[1,0]\n"
    "v_pk_add_f32 v[238:239], v[238:239], 1.0 op_sel_hi:[1,0]\n"
    "v_mfma_f32_16x16x32_bf16 v[184:187], a[148:151], a[140:143], v[184:187]\n"
    "v_rcp_f32 v236, v236\n"
    "v_rcp_f32 v237, v237\n"
    "v_mfma_f32_16x16x32_bf16 v[116:119], a[152:155], a[112:115], v[116:119]\n"
    "v_rcp_f32 v238, v238\n"
    "v_rcp_f32 v239, v239\n"
    "s_nop 0\n"
    "v_mfma_f32_16x16x32_bf16 v[116:119], a[156:159], a[116:119], v[116:119]\n"
    "v_pk_mul_f32 v[56:57], v[56:57], v[236:237]\n"
    "v_pk_mul_f32 v[58:59], v[58:59], v[238:239]\n"
    "v_cvt_pk_bf16_f32 v220, v56, v57\n"
    "v_mfma_f32_16x16x32_bf16 v[140:143], a[152:155], a[120:123], v[140:143]\n"
    "v_cvt_pk_bf16_f32 v221, v58, v59\n"
    "v_pk_mul_f32 v[236:237], v[60:61], s[70:71]\n"
    "v_mfma_f32_16x16x32_bf16 v[140:143], a[156:159], a[124:127], v[140:143]\n"
    "v_pk_mul_f32 v[238:239], v[62:63], s[70:71]\n"
    "v_exp_f32 v236, v236\n"
    "v_exp_f32 v237, v237\n"
    "v_mfma_f32_16x16x32_bf16 v[164:167], a[152:155], a[128:131], v[164:167]\n"
    "v_exp_f32 v238, v238\n"
    "v_exp_f32 v239, v239\n"
    "v_mfma_f32_16x16x32_bf16 v[164:167], a[156:159], a[132:135], v[164:167]\n"
    "v_pk_mul_f32 v[60:61], v[60:61], v[76:77]\n"
    "v_pk_mul_f32 v[62:63], v[62:63], v[78:79]\n"
    "v_pk_add_f32 v[236:237], v[236:237], 1.0 op_sel_hi:[1,0]\n"
    "v_mfma_f32_16x16x32_bf16 v[188:191], a[152:155], a[136:139], v[188:191]\n"
    "v_pk_add_f32 v[238:239], v[238:239], 1.0 op_sel_hi:[1,0]\n"
    "v_rcp_f32 v236, v236\n"
    "v_rcp_f32 v237, v237\n"
    "v_mfma_f32_16x16x32_bf16 v[188:191], a[156:159], a[140:143], v[188:191]\n"
    "v_rcp_f32 v238, v238\n"
    "v_rcp_f32 v239, v239\n"
    "v_mfma_f32_16x16x32_bf16 v[120:123], a[160:163], a[112:115], v[120:123]\n"
    "s_nop 0\n"
    "v_pk_mul_f32 v[60:61], v[60:61], v[236:237]\n"
    "v_pk_mul_f32 v[62:63], v[62:63], v[238:239]\n"
    "v_mfma_f32_16x16x32_bf16 v[120:123], a[164:167], a[116:119], v[120:123]\n"
    "v_cvt_pk_bf16_f32 v222, v60, v61\n"
    "v_cvt_pk_bf16_f32 v223, v62, v63\n"
    "v_mfma_f32_16x16x32_bf16 v[144:147], a[160:163], a[120:123], v[144:147]\n"
    "s_nop 1\n"
    "v_permlane16_swap_b32 v220, v222\n"
    "v_permlane16_swap_b32 v221, v223\n"
    "v_mfma_f32_16x16x32_bf16 v[144:147], a[164:167], a[124:127], v[144:147]\n"
    "buffer_store_dwordx4 v[220:223], v243, s[52:55], s65 offen offset:0 sc1\n"
    "v_pk_mul_f32 v[236:237], v[64:65], s[70:71]\n"
    "v_pk_mul_f32 v[238:239], v[66:67], s[70:71]\n"
    "v_mfma_f32_16x16x32_bf16 v[168:171], a[160:163], a[128:131], v[168:171]\n"
    "v_exp_f32 v236, v236\n"
    "v_exp_f32 v237, v237\n"
    "v_mfma_f32_16x16x32_bf16 v[168:171], a[164:167], a[132:135], v[168:171]\n"
    "v_exp_f32 v238, v238\n"
    "v_exp_f32 v239, v239\n"
    "v_pk_mul_f32 v[64:65], v[64:65], v[80:81]\n"
    "v_mfma_f32_16x16x32_bf16 v[192:195], a[160:163], a[136:139], v[192:195]\n"
    "v_pk_mul_f32 v[66:67], v[66:67], v[82:83]\n"
    "v_pk_add_f32 v[236:237], v[236:237], 1.0 op_sel_hi:[1,0]\n"
    "v_mfma_f32_16x16x32_bf16 v[192:195], a[164:167], a[140:143], v[192:195]\n"
    "v_pk_add_f32 v[238:239], v[238:239], 1.0 op_sel_hi:[1,0]\n"
    "v_rcp_f32 v236, v236\n"
    "v_rcp_f32 v237, v237\n"
    "v_mfma_f32_16x16x32_bf16 v[124:127], a[168:171], a[112:115], v[124:127]\n"
    "v_rcp_f32 v238, v238\n"
    "v_rcp_f32 v239, v239\n"
    "s_nop 0\n"
    "v_mfma_f32_16x16x32_bf16 v[124:127], a[172:175], a[116:119], v[124:127]\n"
    "v_pk_mul_f32 v[64:65], v[64:65], v[236:237]\n"
    "v_pk_mul_f32 v[66:67], v[66:67], v[238:239]\n"
    "v_mfma_f32_16x16x32_bf16 v[148:151], a[168:171], a[120:123], v[148:151]\n"
    "v_cvt_pk_bf16_f32 v224, v64, v65\n"
    "v_cvt_pk_bf16_f32 v225, v66, v67\n"
    "v_accvgpr_read_b32 v244, a40\n"
    "v_mfma_f32_16x16x32_bf16 v[148:151], a[172:175], a[124:127], v[148:151]\n"
    "v_accvgpr_read_b32 v245, a41\n"
    "v_accvgpr_read_b32 v246, a42\n"
    "v_mfma_f32_16x16x32_bf16 v[172:175], a[168:171], a[128:131], v[172:175]\n"
    "v_accvgpr_read_b32 v247, a43\n"
    "v_pk_mul_f32 v[236:237], v[68:69], s[70:71]\n"
    "v_pk_mul_f32 v[238:239], v[70:71], s[70:71]\n"
    "v_mfma_f32_16x16x32_bf16 v[172:175], a[172:175], a[132:135], v[172:175]\n"
    "v_exp_f32 v236, v236\n"
    "v_exp_f32 v237, v237\n"
    "v_exp_f32 v238, v238\n"
    "v_mfma_f32_16x16x32_bf16 v[196:199], a[168:171], a[136:139], v[196:199]\n"
    "v_exp_f32 v239, v239\n"
    "v_pk_mul_f32 v[68:69], v[68:69], v[244:245]\n"
    "v_mfma_f32_16x16x32_bf16 v[196:199], a[172:175], a[140:143], v[196:199]\n"
    "v_pk_mul_f32 v[70:71], v[70:71], v[246:247]\n"
    "v_pk_add_f32 v[236:237], v[236:237], 1.0 op_sel_hi:[1,0]\n"
    "v_pk_add_f32 v[238:239], v[238:239], 1.0 op_sel_hi:[1,0]\n"
    "v_mfma_f32_16x16x32_bf16 v[128:131], a[176:179], a[112:115], v[128:131]\n"
    "v_rcp_f32 v236, v236\n"
    "v_rcp_f32 v237, v237\n"
    "s_waitcnt lgkmcnt(14)\n"
    "v_mfma_f32_16x16x32_bf16 v[128:131], a[180:183], a[116:119], v[128:131]\n"
    "v_rcp_f32 v238, v238\n"
    "v_rcp_f32 v239, v239\n"
    "s_nop 0\n"
    "v_mfma_f32_16x16x32_bf16 v[152:155], a[176:179], a[120:123], v[152:155]\n"
    "v_pk_mul_f32 v[68:69], v[68:69], v[236:237]\n"
    "v_pk_mul_f32 v[70:71], v[70:71], v[238:239]\n"
    "v_cvt_pk_bf16_f32 v226, v68, v69\n"
    "v_mfma_f32_16x16x32_bf16 v[152:155], a[180:183], a[124:127], v[152:155]\n"
    "v_cvt_pk_bf16_f32 v227, v70, v71\n"
    "s_nop 1\n"
    "v_mfma_f32_16x16x32_bf16 v[176:179], a[176:179], a[128:131], v[176:179]\n"
    "v_permlane16_swap_b32 v224, v226\n"
    "v_permlane16_swap_b32 v225, v227\n"
    "buffer_store_dwordx4 v[224:227], v243, s[52:55], s65 offen offset:1024 sc1\n"
    "v_mfma_f32_16x16x32_bf16 v[176:179], a[180:183], a[132:135], v[176:179]\n"
    "s_add_u32 s65, s65, 2048\n"
    "v_pk_mul_f32 v[236:237], v[84:85], s[70:71]\n"
    "v_mfma_f32_16x16x32_bf16 v[200:203], a[176:179], a[136:139], v[200:203]\n"
    "v_pk_mul_f32 v[238:239], v[86:87], s[70:71]\n"
    "v_exp_f32 v236, v236\n"
    "v_exp_f32 v237, v237\n"
    "v_mfma_f32_16x16x32_bf16 v[200:203], a[180:183], a[140:143], v[200:203]\n"
    "v_exp_f32 v238, v238\n"
    "v_exp_f32 v239, v239\n"
    "s_waitcnt lgkmcnt(10)\n"
    "v_mfma_f32_16x16x32_bf16 v[132:135], a[184:187], a[112:115], v[132:135]\n"
    "v_pk_mul_f32 v[84:85], v[84:85], v[100:101]\n"
    "v_pk_mul_f32 v[86:87], v[86:87], v[102:103]\n"
    "v_pk_add_f32 v[236:237], v[236:237], 1.0 op_sel_hi:[1,0]\n"
    "v_mfma_f32_16x16x32_bf16 v[132:135], a[188:191], a[116:119], v[132:135]\n"
    "v_pk_add_f32 v[238:239], v[238:239], 1.0 op_sel_hi:[1,0]\n"
    "v_rcp_f32 v236, v236\n"
    "v_rcp_f32 v237, v237\n"
    "v_mfma_f32_16x16x32_bf16 v[156:159], a[184:187], a[120:123], v[156:159]\n"
    "v_rcp_f32 v238, v238\n"
    "v_rcp_f32 v239, v239\n"
    "v_mfma_f32_16x16x32_bf16 v[156:159], a[188:191], a[124:127], v[156:159]\n"
    "s_nop 0\n"
    "v_pk_mul_f32 v[84:85], v[84:85], v[236:237]\n"
    "v_pk_mul_f32 v[86:87], v[86:87], v[238:239]\n"
    "v_mfma_f32_16x16x32_bf16 v[180:183], a[184:187], a[128:131], v[180:183]\n"
    "v_cvt_pk_bf16_f32 v220, v84, v85\n"
    "v_cvt_pk_bf16_f32 v221, v86, v87\n"
    "v_mfma_f32_16x16x32_bf16 v[180:183], a[188:191], a[132:135], v[180:183]\n"
    "v_pk_mul_f32 v[236:237], v[88:89], s[70:71]\n"
    "v_pk_mul_f32 v[238:239], v[90:91], s[70:71]\n"
    "v_exp_f32 v236, v236\n"
    "v_mfma_f32_16x16x32_bf16 v[204:207], a[184:187], a[136:139], v[204:207]\n"
    "v_exp_f32 v237, v237\n"
    "v_exp_f32 v238, v238\n"
    "v_exp_f32 v239, v239\n"
    "v_mfma_f32_16x16x32_bf16 v[204:207], a[188:191], a[140:143], v[204:207]\n"
    "v_pk_mul_f32 v[88:89], v[88:89], v[104:105]\n"
    "v_pk_mul_f32 v[90:91], v[90:91], v[106:107]\n"
    "s_waitcnt lgkmcnt(6)\n"
    "v_mfma_f32_16x16x32_bf16 a[64:67], a[192:195], a[112:115], a[64:67]\n"
    "v_pk_add_f32 v[236:237], v[236:237], 1.0 op_sel_hi:[1,0]\n"
    "v_pk_add_f32 v[238:239], v[238:239], 1.0 op_sel_hi:[1,0]\n"
    "v_rcp_f32 v236, v236\n"
    "v_mfma_f32_16x16x32_bf16 a[64:67], a[196:199], a[116:119], a[64:67]\n"
    "v_rcp_f32 v237, v237\n"
    "v_rcp_f32 v238, v238\n"
    "v_mfma_f32_16x16x32_bf16 a[68:71], a[192:195], a[120:123], a[68:71]\n"
    "v_rcp_f32 v239, v239\n"
    "s_nop 0\n"
    "v_pk_mul_f32 v[88:89], v[88:89], v[236:237]\n"
    "v_mfma_f32_16x16x32_bf16 a[68:71], a[196:199], a[124:127], a[68:71]\n"
    "v_pk_mul_f32 v[90:91], v[90:91], v[238:239]\n"
    "v_cvt_pk_bf16_f32 v222, v88, v89\n"
    "v_cvt_pk_bf16_f32 v223, v90, v91\n"
    "v_mfma_f32_16x16x32_bf16 a[72:75], a[192:195], a[128:131], a[72:75]\n"
    "s_nop 1\n"
    "v_permlane16_swap_b32 v220, v222\n"
    "v_mfma_f32_16x16x32_bf16 a[72:75], a[196:199], a[132:135], a[72:75]\n"
    "v_permlane16_swap_b32 v221, v223\n"
    "buffer_store_dwordx4 v[220:223], v243, s[52:55], s65 offen offset:0 sc1\n"
    "v_pk_mul_f32 v[236:237], v[92:93], s[70:71]\n"
    "v_mfma_f32_16x16x32_bf16 a[76:79], a[192:195], a[136:139], a[76:79]\n"
    "v_pk_mul_f32 v[238:239], v[94:95], s[70:71]\n"
    "v_exp_f32 v236, v236\n"
    "v_mfma_f32_16x16x32_bf16 a[76:79], a[196:199], a[140:143], a[76:79]\n"
    "v_exp_f32 v237, v237\n"
    "v_exp_f32 v238, v238\n"
    "v_exp_f32 v239, v239\n"
    "s_waitcnt lgkmcnt(2)\n"
    "v_mfma_f32_16x16x32_bf16 a[48:51], a[200:203], a[112:115], a[48:51]\n"
    "v_pk_mul_f32 v[92:93], v[92:93], v[108:109]\n"
    "v_pk_mul_f32 v[94:95], v[94:95], v[110:111]\n"
    "v_pk_add_f32 v[236:237], v[236:237], 1.0 op_sel_hi:[1,0]\n"
    "v_mfma_f32_16x16x32_bf16 a[48:51], a[204:207], a[116:119], a[48:51]\n"
    "v_pk_add_f32 v[238:239], v[238:239], 1.0 op_sel_hi:[1,0]\n"
    "v_rcp_f32 v236, v236\n"
    "v_mfma_f32_16x16x32_bf16 a[52:55], a[200:203], a[120:123], a[52:55]\n"
    "v_rcp_f32 v237, v237\n"
    "v_rcp_f32 v238, v238\n"
    "v_rcp_f32 v239, v239\n"
    "v_mfma_f32_16x16x32_bf16 a[52:55], a[204:207], a[124:127], a[52:55]\n"
    "s_nop 0\n"
    "v_pk_mul_f32 v[92:93], v[92:93], v[236:237]\n"
    "v_mfma_f32_16x16x32_bf16 a[56:59], a[200:203], a[128:131], a[56:59]\n"
    "v_pk_mul_f32 v[94:95], v[94:95], v[238:239]\n"
    "v_cvt_pk_bf16_f32 v224, v92, v93\n"
    "v_cvt_pk_bf16_f32 v225, v94, v95\n"
    "v_mfma_f32_16x16x32_bf16 a[56:59], a[204:207], a[132:135], a[56:59]\n"
    "v_accvgpr_read_b32 v244, a44\n"
    "v_accvgpr_read_b32 v245, a45\n"
    "v_accvgpr_read_b32 v246, a46\n"
    "v_mfma_f32_16x16x32_bf16 a[60:63], a[200:203], a[136:139], a[60:63]\n"
    "v_accvgpr_read_b32 v247, a47\n"
    "v_pk_mul_f32 v[236:237], v[96:97], s[70:71]\n"
    "v_mfma_f32_16x16x32_bf16 a[60:63], a[204:207], a[140:143], a[60:63]\n"
    "v_pk_mul_f32 v[238:239], v[98:99], s[70:71]\n"
    "v_exp_f32 v236, v236\n"
    "v_exp_f32 v237, v237\n"
    "s_waitcnt lgkmcnt(0)\n"
    "v_mfma_f32_16x16x32_bf16 a[16:19], a[112:115], a[208:211], a[16:19]\n"
    "v_exp_f32 v238, v238\n"
    "v_exp_f32 v239, v239\n"
    "v_mfma_f32_16x16x32_bf16 a[16:19], a[116:119], a[212:215], a[16:19]\n"
    "v_pk_mul_f32 v[96:97], v[96:97], v[244:245]\n"
    "v_pk_mul_f32 v[98:99], v[98:99], v[246:247]\n"
    "v_pk_add_f32 v[236:237], v[236:237], 1.0 op_sel_hi:[1,0]\n"
    "v_mfma_f32_16x16x32_bf16 a[20:23], a[120:123], a[208:211], a[20:23]\n"
    "v_pk_add_f32 v[238:239], v[238:239], 1.0 op_sel_hi:[1,0]\n"
    "v_rcp_f32 v236, v236\n"
    "v_rcp_f32 v237, v237\n"
    "v_mfma_f32_16x16x32_bf16 a[20:23], a[124:127], a[212:215], a[20:23]\n"
    "v_rcp_f32 v238, v238\n"
    "v_rcp_f32 v239, v239\n"
    "v_mfma_f32_16x16x32_bf16 a[24:27], a[128:131], a[208:211], a[24:27]\n"
    "s_nop 0\n"
    "v_pk_mul_f32 v[96:97], v[96:97], v[236:237]\n"
    "v_pk_mul_f32 v[98:99], v[98:99], v[238:239]\n"
    "v_mfma_f32_16x16x32_bf16 a[24:27], a[132:135], a[212:215], a[24:27]\n"
    "v_cvt_pk_bf16_f32 v226, v96, v97\n"
    "v_cvt_pk_bf16_f32 v227, v98, v99\n"
    "v_mfma_f32_16x16x32_bf16 a[28:31], a[136:139], a[208:211], a[28:31]\n"
    "s_nop 1\n"
    "v_permlane16_swap_b32 v224, v226\n"
    "v_permlane16_swap_b32 v225, v227\n"
    "v_mfma_f32_16x16x32_bf16 a[28:31], a[140:143], a[212:215], a[28:31]\n"
    "buffer_store_dwordx4 v[224:227], v243, s[52:55], s65 offen offset:1024 sc1\n"
    "s_add_u32 s65, s65, 2048\n"
    "s_nop 7\n"
    "s_lshl_b32 s99, s64, 5\n"
    "s_add_u32 s99, s99, s63\n"
    "s_lshl_b32 s99, s99, 3\n"
    "v_accvgpr_read_b32 v244, a16\n"
    "v_accvgpr_read_b32 v245, a17\n"
    "v_accvgpr_read_b32 v246, a18\n"
    "v_accvgpr_read_b32 v247, a19\n"
    "v_cvt_pk_bf16_f32 v228, v244, v245\n"
    "v_cvt_pk_bf16_f32 v229, v246, v247\n"
    "v_accvgpr_read_b32 v248, a20\n"
    "v_accvgpr_read_b32 v249, a21\n"
    "v_accvgpr_read_b32 v250, a22\n"
    "v_accvgpr_read_b32 v251, a23\n"
    "v_cvt_pk_bf16_f32 v230, v248, v249\n"
    "v_cvt_pk_bf16_f32 v231, v250, v251\n"
    "s_nop 1\n"
    "v_permlane16_swap_b32 v228, v230\n"
    "v_permlane16_swap_b32 v229, v231\n"
    "s_add_u32 s69, s66, 131072\n"
    "buffer_store_dwordx4 v[228:231], v243, s[56:59], s69 offen offset:0 sc1\n"
    "v_accvgpr_read_b32 v244, a24\n"
    "v_accvgpr_read_b32 v245, a25\n"
    "v_accvgpr_read_b32 v246, a26\n"
    "v_accvgpr_read_b32 v247, a27\n"
    "v_cvt_pk_bf16_f32 v232, v244, v245\n"
    "v_cvt_pk_bf16_f32 v233, v246, v247\n"
    "v_accvgpr_read_b32 v248, a28\n"
    "v_accvgpr_read_b32 v249, a29\n"
    "v_accvgpr_read_b32 v250, a30\n"
    "v_accvgpr_read_b32 v251, a31\n"
    "v_cvt_pk_bf16_f32 v234, v248, v249\n"
    "v_cvt_pk_bf16_f32 v235, v250, v251\n"
    "s_nop 1\n"
    "v_permlane16_swap_b32 v232, v234\n"
    "v_permlane16_swap_b32 v233, v235\n"
    "s_add_u32 s69, s66, 131072\n"
    "buffer_store_dwordx4 v[232:235], v243, s[56:59], s69 offen offset:1024 sc1\n"
    "v_pk_mul_f32 v[236:237], v[112:113], s[70:71]\n"
    "v_pk_mul_f32 v[238:239], v[114:115], s[70:71]\n"
    "v_exp_f32 v236, v236\n"
    "v_exp_f32 v237, v237\n"
    "v_exp_f32 v238, v238\n"
    "v_exp_f32 v239, v239\n"
    "v_pk_mul_f32 v[112:113], v[112:113], v[128:129]\n"
    "v_pk_mul_f32 v[114:115], v[114:115], v[130:131]\n"
    "v_pk_add_f32 v[236:237], v[236:237], 1.0 op_sel_hi:[1,0]\n"
    "v_pk_add_f32 v[238:239], v[238:239], 1.0 op_sel_hi:[1,0]\n"
    "v_rcp_f32 v236, v236\n"
    "v_rcp_f32 v237, v237\n"
    "v_rcp_f32 v238, v238\n"
    "v_rcp_f32 v239, v239\n"
    "s_nop 0\n"
    "v_pk_mul_f32 v[112:113], v[112:113], v[236:237]\n"
    "v_pk_mul_f32 v[114:115], v[114:115], v[238:239]\n"
    "v_cvt_pk_bf16_f32 v220, v112, v113\n"
    "v_cvt_pk_bf16_f32 v221, v114, v115\n"
    "v_pk_mul_f32 v[236:237], v[116:117], s[70:71]\n"
    "v_pk_mul_f32 v[238:239], v[118:119], s[70:71]\n"
    "v_exp_f32 v236, v236\n"
    "v_exp_f32 v237, v237\n"
    "v_exp_f32 v238, v238\n"
    "v_exp_f32 v239, v239\n"
    "v_pk_mul_f32 v[116:117], v[116:117], v[132:133]\n"
    "v_pk_mul_f32 v[118:119], v[118:119], v[134:135]\n"
    "v_pk_add_f32 v[236:237], v[236:237], 1.0 op_sel_hi:[1,0]\n"
    "v_pk_add_f32 v[238:239], v[238:239], 1.0 op_sel_hi:[1,0]\n"
    "v_rcp_f32 v236, v236\n"
    "v_rcp_f32 v237, v237\n"
    "v_rcp_f32 v238, v238\n"
    "v_rcp_f32 v239, v239\n"
    "s_nop 0\n"
    "v_pk_mul_f32 v[116:117], v[116:117], v[236:237]\n"
    "v_pk_mul_f32 v[118:119], v[118:119], v[238:239]\n"
    "v_cvt_pk_bf16_f32 v222, v116, v117\n"
    "v_cvt_pk_bf16_f32 v223, v118, v119\n"
    "s_nop 1\n"
    "v_permlane16_swap_b32 v220, v222\n"
    "v_permlane16_swap_b32 v221, v223\n"
    "buffer_store_dwordx4 v[220:223], v243, s[52:55], s65 offen offset:0 sc1\n"
    "v_accvgpr_read_b32 v244, a64\n"
    "v_accvgpr_read_b32 v245, a65\n"
    "v_accvgpr_read_b32 v246, a66\n"
    "v_accvgpr_read_b32 v247, a67\n"
    "v_pk_mul_f32 v[236:237], v[120:121], s[70:71]\n"
    "v_pk_mul_f32 v[238:239], v[122:123], s[70:71]\n"
    "v_exp_f32 v236, v236\n"
    "v_exp_f32 v237, v237\n"
    "v_exp_f32 v238, v238\n"
    "v_exp_f32 v239, v239\n"
    "v_pk_mul_f32 v[120:121], v[120:121], v[244:245]\n"
    "v_pk_mul_f32 v[122:123], v[122:123], v[246:247]\n"
    "v_pk_add_f32 v[236:237], v[236:237], 1.0 op_sel_hi:[1,0]\n"
    "v_pk_add_f32 v[238:239], v[238:239], 1.0 op_sel_hi:[1,0]\n"
    "v_rcp_f32 v236, v236\n"
    "v_rcp_f32 v237, v237\n"
    "v_rcp_f32 v238, v238\n"
    "v_rcp_f32 v239, v239\n"
    "s_nop 0\n"
    "v_pk_mul_f32 v[120:121], v[120:121], v[236:237]\n"
    "v_pk_mul_f32 v[122:123], v[122:123], v[238:239]\n"
    "v_cvt_pk_bf16_f32 v224, v120, v121\n"
    "v_cvt_pk_bf16_f32 v225, v122, v123\n"
    "v_accvgpr_read_b32 v244, a48\n"
    "v_accvgpr_read_b32 v245, a49\n"
    "v_accvgpr_read_b32 v246, a50\n"
    "v_accvgpr_read_b32 v247, a51\n"
    "v_pk_mul_f32 v[236:237], v[124:125], s[70:71]\n"
    "v_pk_mul_f32 v[238:239], v[126:127], s[70:71]\n"
    "v_exp_f32 v236, v236\n"
    "v_exp_f32 v237, v237\n"
    "v_exp_f32 v238, v238\n"
    "v_exp_f32 v239, v239\n"
    "v_pk_mul_f32 v[124:125], v[124:125], v[244:245]\n"
    "v_pk_mul_f32 v[126:127], v[126:127], v[246:247]\n"
    "v_pk_add_f32 v[236:237], v[236:237], 1.0 op_sel_hi:[1,0]\n"
    "v_pk_add_f32 v[238:239], v[238:239], 1.0 op_sel_hi:[1,0]\n"
    "v_rcp_f32 v236, v236\n"
    "v_rcp_f32 v237, v237\n"
    "v_rcp_f32 v238, v238\n"
    "v_rcp_f32 v239, v239\n"
    "s_nop 0\n"
    "v_pk_mul_f32 v[124:125], v[124:125], v[236:237]\n"
    "v_pk_mul_f32 v[126:127], v[126:127], v[238:239]\n"
    "v_cvt_pk_bf16_f32 v226, v124, v125\n"
    "v_cvt_pk_bf16_f32 v227, v126, v127\n"
    "s_nop 1\n"
    "v_permlane16_swap_b32 v224, v226\n"
    "v_permlane16_swap_b32 v225, v227\n"
    "buffer_store_dwordx4 v[224:227], v243, s[52:55], s65 offen offset:1024 sc1\n"
    "s_add_u32 s65, s65, 2048\n"
    "v_pk_mul_f32 v[236:237], v[136:137], s[70:71]\n"
    "v_pk_mul_f32 v[238:239], v[138:139], s[70:71]\n"
    "v_exp_f32 v236, v236\n"
    "v_exp_f32 v237, v237\n"
    "v_exp_f32 v238, v238\n"
    "v_exp_f32 v239, v239\n"
    "v_pk_mul_f32 v[136:137], v[136:137], v[152:153]\n"
    "v_pk_mul_f32 v[138:139], v[138:139], v[154:155]\n"
    "v_pk_add_f32 v[236:237], v[236:237], 1.0 op_sel_hi:[1,0]\n"
    "v_pk_add_f32 v[238:239], v[238:239], 1.0 op_sel_hi:[1,0]\n"
    "v_rcp_f32 v236, v236\n"
    "v_rcp_f32 v237, v237\n"
    "v_rcp_f32 v238, v238\n"
    "v_rcp_f32 v239, v239\n"
    "s_nop 0\n"
    "v_pk_mul_f32 v[136:137], v[136:137], v[236:237]\n"
    "v_pk_mul_f32 v[138:139], v[138:139], v[238:239]\n"
    "v_cvt_pk_bf16_f32 v220, v136, v137\n"
    "v_cvt_pk_bf16_f32 v221, v138, v139\n"
    "v_pk_mul_f32 v[236:237], v[140:141], s[70:71]\n"
    "v_pk_mul_f32 v[238:239], v[142:143], s[70:71]\n"
    "v_exp_f32 v236, v236\n"
    "v_exp_f32 v237, v237\n"
    "v_exp_f32 v238, v238\n"
    "v_exp_f32 v239, v239\n"
    "v_pk_mul_f32 v[140:141], v[140:141], v[156:157]\n"
    "v_pk_mul_f32 v[142:143], v[142:143], v[158:159]\n"
    "v_pk_add_f32 v[236:237], v[236:237], 1.0 op_sel_hi:[1,0]\n"
    "v_pk_add_f32 v[238:239], v[238:239], 1.0 op_sel_hi:[1,0]\n"
    "v_rcp_f32 v236, v236\n"
    "v_rcp_f32 v237, v237\n"
    "v_rcp_f32 v238, v238\n"
    "v_rcp_f32 v239, v239\n"
    "s_nop 0\n"
    "v_pk_mul_f32 v[140:141], v[140:141], v[236:237]\n"
    "v_pk_mul_f32 v[142:143], v[142:143], v[238:239]\n"
    "v_cvt_pk_bf16_f32 v222, v140, v141\n"
    "v_cvt_pk_bf16_f32 v223, v142, v143\n"
    "s_nop 1\n"
    "v_permlane16_swap_b32 v220, v222\n"
    "v_permlane16_swap_b32 v221, v223\n"
    "buffer_store_dwordx4 v[220:223], v243, s[52:55], s65 offen offset:0 sc1\n"
    "v_accvgpr_read_b32 v244, a68\n"
    "v_accvgpr_read_b32 v245, a69\n"
    "v_accvgpr_read_b32 v246, a70\n"
    "v_accvgpr_read_b32 v247, a71\n"
    "v_pk_mul_f32 v[236:237], v[144:145], s[70:71]\n"
    "v_pk_mul_f32 v[238:239], v[146:147], s[70:71]\n"
    "v_exp_f32 v236, v236\n"
    "v_exp_f32 v237, v237\n"
    "v_exp_f32 v238, v238\n"
    "v_exp_f32 v239, v239\n"
    "v_pk_mul_f32 v[144:145], v[144:145], v[244:245]\n"
    "v_pk_mul_f32 v[146:147], v[146:147], v[246:247]\n"
    "v_pk_add_f32 v[236:237], v[236:237], 1.0 op_sel_hi:[1,0]\n"
    "v_pk_add_f32 v[238:239], v[238:239], 1.0 op_sel_hi:[1,0]\n"
    "v_rcp_f32 v236, v236\n"
    "v_rcp_f32 v237, v237\n"
    "v_rcp_f32 v238, v238\n"
    "v_rcp_f32 v239, v239\n"
    "s_nop 0\n"
    "v_pk_mul_f32 v[144:145], v[144:145], v[236:237]\n"
    "v_pk_mul_f32 v[146:147], v[146:147], v[238:239]\n"
    "v_cvt_pk_bf16_f32 v224, v144, v145\n"
    "v_cvt_pk_bf16_f32 v225, v146, v147\n"
    "v_accvgpr_read_b32 v244, a52\n"
    "v_accvgpr_read_b32 v245, a53\n"
    "v_accvgpr_read_b32 v246, a54\n"
    "v_accvgpr_read_b32 v247, a55\n"
    "v_pk_mul_f32 v[236:237], v[148:149], s[70:71]\n"
    "v_pk_mul_f32 v[238:239], v[150:151], s[70:71]\n"
    "v_exp_f32 v236, v236\n"
    "v_exp_f32 v237, v237\n"
    "v_exp_f32 v238, v238\n"
    "v_exp_f32 v239, v239\n"
    "v_pk_mul_f32 v[148:149], v[148:149], v[244:245]\n"
    "v_pk_mul_f32 v[150:151], v[150:151], v[246:247]\n"
    "v_pk_add_f32 v[236:237], v[236:237], 1.0 op_sel_hi:[1,0]\n"
    "v_pk_add_f32 v[238:239], v[238:239], 1.0 op_sel_hi:[1,0]\n"
    "v_rcp_f32 v236, v236\n"
    "v_rcp_f32 v237, v237\n"
    "v_rcp_f32 v238, v238\n"
    "v_rcp_f32 v239, v239\n"
    "s_nop 0\n"
    "v_pk_mul_f32 v[148:149], v[148:149], v[236:237]\n"
    "v_pk_mul_f32 v[150:151], v[150:151], v[238:239]\n"
    "v_cvt_pk_bf16_f32 v226, v148, v149\n"
    "v_cvt_pk_bf16_f32 v227, v150, v151\n"
    "s_nop 1\n"
    "v_permlane16_swap_b32 v224, v226\n"
    "v_permlane16_swap_b32 v225, v227\n"
    "buffer_store_dwordx4 v[224:227], v243, s[52:55], s65 offen offset:1024 sc1\n"
    "s_add_u32 s65, s65, 2048\n"
    "v_pk_mul_f32 v[236:237], v[160:161], s[70:71]\n"
    "v_pk_mul_f32 v[238:239], v[162:163], s[70:71]\n"
    "v_exp_f32 v236, v236\n"
    "v_exp_f32 v237, v237\n"
    "v_exp_f32 v238, v238\n"
    "v_exp_f32 v239, v239\n"
    "v_pk_mul_f32 v[160:161], v[160:161], v[176:177]\n"
    "v_pk_mul_f32 v[162:163], v[162:163], v[178:179]\n"
    "v_pk_add_f32 v[236:237], v[236:237], 1.0 op_sel_hi:[1,0]\n"
    "v_pk_add_f32 v[238:239], v[238:239], 1.0 op_sel_hi:[1,0]\n"
    "v_rcp_f32 v236, v236\n"
    "v_rcp_f32 v237, v237\n"
    "v_rcp_f32 v238, v238\n"
    "v_rcp_f32 v239, v239\n"
    "s_nop 0\n"
    "v_pk_mul_f32 v[160:161], v[160:161], v[236:237]\n"
    "v_pk_mul_f32 v[162:163], v[162:163], v[238:239]\n"
    "v_cvt_pk_bf16_f32 v220, v160, v161\n"
    "v_cvt_pk_bf16_f32 v221, v162, v163\n"
    "v_pk_mul_f32 v[236:237], v[164:165], s[70:71]\n"
    "v_pk_mul_f32 v[238:239], v[166:167], s[70:71]\n"
    "v_exp_f32 v236, v236\n"
    "v_exp_f32 v237, v237\n"
    "v_exp_f32 v238, v238\n"
    "v_exp_f32 v239, v239\n"
    "v_pk_mul_f32 v[164:165], v[164:165], v[180:181]\n"
    "v_pk_mul_f32 v[166:167], v[166:167], v[182:183]\n"
    "v_pk_add_f32 v[236:237], v[236:237], 1.0 op_sel_hi:[1,0]\n"
    "v_pk_add_f32 v[238:239], v[238:239], 1.0 op_sel_hi:[1,0]\n"
    "v_rcp_f32 v236, v236\n"
    "v_rcp_f32 v237, v237\n"
    "v_rcp_f32 v238, v238\n"
    "v_rcp_f32 v239, v239\n"
    "s_nop 0\n"
    "v_pk_mul_f32 v[164:165], v[164:165], v[236:237]\n"
    "v_pk_mul_f32 v[166:167], v[166:167], v[238:239]\n"
    "v_cvt_pk_bf16_f32 v222, v164, v165\n"
    "v_cvt_pk_bf16_f32 v223, v166, v167\n"
    "s_nop 1\n"
    "v_permlane16_swap_b32 v220, v222\n"
    "v_permlane16_swap_b32 v221, v223\n"
    "buffer_store_dwordx4 v[220:223], v243, s[52:55], s65 offen offset:0 sc1\n"
    "v_accvgpr_read_b32 v244, a72\n"
    "v_accvgpr_read_b32 v245, a73\n"
    "v_accvgpr_read_b32 v246, a74\n"
    "v_accvgpr_read_b32 v247, a75\n"
    "v_pk_mul_f32 v[236:237], v[168:169], s[70:71]\n"
    "v_pk_mul_f32 v[238:239], v[170:171], s[70:71]\n"
    "v_exp_f32 v236, v236\n"
    "v_exp_f32 v237, v237\n"
    "v_exp_f32 v238, v238\n"
    "v_exp_f32 v239, v239\n"
    "v_pk_mul_f32 v[168:169], v[168:169], v[244:245]\n"
    "v_pk_mul_f32 v[170:171], v[170:171], v[246:247]\n"
    "v_pk_add_f32 v[236:237], v[236:237], 1.0 op_sel_hi:[1,0]\n"
    "v_pk_add_f32 v[238:239], v[238:239], 1.0 op_sel_hi:[1,0]\n"
    "v_rcp_f32 v236, v236\n"
    "v_rcp_f32 v237, v237\n"
    "v_rcp_f32 v238, v238\n"
    "v_rcp_f32 v239, v239\n"
    "s_nop 0\n"
    "v_pk_mul_f32 v[168:169], v[168:169], v[236:237]\n"
    "v_pk_mul_f32 v[170:171], v[170:171], v[238:239]\n"
    "v_cvt_pk_bf16_f32 v224, v168, v169\n"
    "v_cvt_pk_bf16_f32 v225, v170, v171\n"
    "v_accvgpr_read_b32 v244, a56\n"
    "v_accvgpr_read_b32 v245, a57\n"
    "v_accvgpr_read_b32 v246, a58\n"
    "v_accvgpr_read_b32 v247, a59\n"
    "v_pk_mul_f32 v[236:237], v[172:173], s[70:71]\n"
    "v_pk_mul_f32 v[238:239], v[174:175], s[70:71]\n"
    "v_exp_f32 v236, v236\n"
    "v_exp_f32 v237, v237\n"
    "v_exp_f32 v238, v238\n"
    "v_exp_f32 v239, v239\n"
    "v_pk_mul_f32 v[172:173], v[172:173], v[244:245]\n"
    "v_pk_mul_f32 v[174:175], v[174:175], v[246:247]\n"
    "v_pk_add_f32 v[236:237], v[236:237], 1.0 op_sel_hi:[1,0]\n"
    "v_pk_add_f32 v[238:239], v[238:239], 1.0 op_sel_hi:[1,0]\n"
    "v_rcp_f32 v236, v236\n"
    "v_rcp_f32 v237, v237\n"
    "v_rcp_f32 v238, v238\n"
    "v_rcp_f32 v239, v239\n"
    "s_nop 0\n"
    "v_pk_mul_f32 v[172:173], v[172:173], v[236:237]\n"
    "v_pk_mul_f32 v[174:175], v[174:175], v[238:239]\n"
    "v_cvt_pk_bf16_f32 v226, v172, v173\n"
    "v_cvt_pk_bf16_f32 v227, v174, v175\n"
    "s_nop 1\n"
    "v_permlane16_swap_b32 v224, v226\n"
    "v_permlane16_swap_b32 v225, v227\n"
    "buffer_store_dwordx4 v[224:227], v243, s[52:55], s65 offen offset:1024 sc1\n"
    "s_add_u32 s65, s65, 2048\n"
    "v_pk_mul_f32 v[236:237], v[184:185], s[70:71]\n"
    "v_pk_mul_f32 v[238:239], v[186:187], s[70:71]\n"
    "v_exp_f32 v236, v236\n"
    "v_exp_f32 v237, v237\n"
    "v_exp_f32 v238, v238\n"
    "v_exp_f32 v239, v239\n"
    "v_pk_mul_f32 v[184:185], v[184:185], v[200:201]\n"
    "v_pk_mul_f32 v[186:187], v[186:187], v[202:203]\n"
    "v_pk_add_f32 v[236:237], v[236:237], 1.0 op_sel_hi:[1,0]\n"
    "v_pk_add_f32 v[238:239], v[238:239], 1.0 op_sel_hi:[1,0]\n"
    "v_rcp_f32 v236, v236\n"
    "v_rcp_f32 v237, v237\n"
    "v_rcp_f32 v238, v238\n"
    "v_rcp_f32 v239, v239\n"
    "s_nop 0\n"
    "v_pk_mul_f32 v[184:185], v[184:185], v[236:237]\n"
    "v_pk_mul_f32 v[186:187], v[186:187], v[238:239]\n"
    "v_cvt_pk_bf16_f32 v220, v184, v185\n"
    "v_cvt_pk_bf16_f32 v221, v186, v187\n"
    "v_pk_mul_f32 v[236:237], v[188:189], s[70:71]\n"
    "v_pk_mul_f32 v[238:239], v[190:191], s[70:71]\n"
    "v_exp_f32 v236, v236\n"
    "v_exp_f32 v237, v237\n"
    "v_exp_f32 v238, v238\n"
    "v_exp_f32 v239, v239\n"
    "v_pk_mul_f32 v[188:189], v[188:189], v[204:205]\n"
    "v_pk_mul_f32 v[190:191], v[190:191], v[206:207]\n"
    "v_pk_add_f32 v[236:237], v[236:237], 1.0 op_sel_hi:[1,0]\n"
    "v_pk_add_f32 v[238:239], v[238:239], 1.0 op_sel_hi:[1,0]\n"
    "v_rcp_f32 v236, v236\n"
    "v_rcp_f32 v237, v237\n"
    "v_rcp_f32 v238, v238\n"
    "v_rcp_f32 v239, v239\n"
    "s_nop 0\n"
    "v_pk_mul_f32 v[188:189], v[188:189], v[236:237]\n"
    "v_pk_mul_f32 v[190:191], v[190:191], v[238:239]\n"
    "v_cvt_pk_bf16_f32 v222, v188, v189\n"
    "v_cvt_pk_bf16_f32 v223, v190, v191\n"
    "s_nop 1\n"
    "v_permlane16_swap_b32 v220, v222\n"
    "v_permlane16_swap_b32 v221, v223\n"
    "buffer_store_dwordx4 v[220:223], v243, s[52:55], s65 offen offset:0 sc1\n"
    "v_accvgpr_read_b32 v244, a76\n"
    "v_accvgpr_read_b32 v245, a77\n"
    "v_accvgpr_read_b32 v246, a78\n"
    "v_accvgpr_read_b32 v247, a79\n"
    "v_pk_mul_f32 v[236:237], v[192:193], s[70:71]\n"
    "v_pk_mul_f32 v[238:239], v[194:195], s[70:71]\n"
    "v_exp_f32 v236, v236\n"
    "v_exp_f32 v237, v237\n"
    "v_exp_f32 v238, v238\n"
    "v_exp_f32 v239, v239\n"
    "v_pk_mul_f32 v[192:193], v[192:193], v[244:245]\n"
    "v_pk_mul_f32 v[194:195], v[194:195], v[246:247]\n"
    "v_pk_add_f32 v[236:237], v[236:237], 1.0 op_sel_hi:[1,0]\n"
    "v_pk_add_f32 v[238:239], v[238:239], 1.0 op_sel_hi:[1,0]\n"
    "v_rcp_f32 v236, v236\n"
    "v_rcp_f32 v237, v237\n"
    "v_rcp_f32 v238, v238\n"
    "v_rcp_f32 v239, v239\n"
    "s_nop 0\n"
    "v_pk_mul_f32 v[192:193], v[192:193], v[236:237]\n"
    "v_pk_mul_f32 v[194:195], v[194:195], v[238:239]\n"
    "v_cvt_pk_bf16_f32 v224, v192, v193\n"
    "v_cvt_pk_bf16_f32 v225, v194, v195\n"
    "v_accvgpr_read_b32 v244, a60\n"
    "v_accvgpr_read_b32 v245, a61\n"
    "v_accvgpr_read_b32 v246, a62\n"
    "v_accvgpr_read_b32 v247, a63\n"
    "v_pk_mul_f32 v[236:237], v[196:197], s[70:71]\n"
    "v_pk_mul_f32 v[238:239], v[198:199], s[70:71]\n"
    "v_exp_f32 v236, v236\n"
    "v_exp_f32 v237, v237\n"
    "v_exp_f32 v238, v238\n"
    "v_exp_f32 v239, v239\n"
    "v_pk_mul_f32 v[196:197], v[196:197], v[244:245]\n"
    "v_pk_mul_f32 v[198:199], v[198:199], v[246:247]\n"
    "v_pk_add_f32 v[236:237], v[236:237], 1.0 op_sel_hi:[1,0]\n"
    "v_pk_add_f32 v[238:239], v[238:239], 1.0 op_sel_hi:[1,0]\n"
    "v_rcp_f32 v236, v236\n"
    "v_rcp_f32 v237, v237\n"
    "v_rcp_f32 v238, v238\n"
    "v_rcp_f32 v239, v239\n"
    "s_nop 0\n"
    "v_pk_mul_f32 v[196:197], v[196:197], v[236:237]\n"
    "v_pk_mul_f32 v[198:199], v[198:199], v[238:239]\n"
    "v_cvt_pk_bf16_f32 v226, v196, v197\n"
    "v_cvt_pk_bf16_f32 v227, v198, v199\n"
    "s_nop 1\n"
    "v_permlane16_swap_b32 v224, v226\n"
    "v_permlane16_swap_b32 v225, v227\n"
    "buffer_store_dwordx4 v[224:227], v243, s[52:55], s65 offen offset:1024 sc1\n"
    "s_add_u32 s65, s65, 2048\n"
    "s_waitcnt vmcnt(8)\n"
    "s_barrier\n"
    "s_cmp_lg_u32 s60, 0\n"
    "s_cbranch_scc1 LABF_%=_5\n"
    "s_add_u32 s98, s99, 2048\n"
    "v_mov_b32 v249, s98\n"
    "v_mov_b32 v252, s4\n"
    "v_mov_b32 v253, s5\n"
    "s_mov_b64 s[10:11], exec\n"
    "s_mov_b64 exec, 1\n"
    "global_store_dwordx2 v249, v[252:253], s[6:7] sc1\n"
    "s_mov_b64 exec, s[10:11]\n"
    "LABF_%=_5:\n"
    "s_add_u32 s99, s99, 4096\n"
    "s_branch LP1E_%=\n"
    "LP1D_%=:\n"
    "v_lshrrev_b32 v243, 3, v240\n"
    "v_and_b32 v244, 7, v240\n"
    "v_xor_b32 v244, v244, v243\n"
    "v_lshlrev_b32 v244, 4, v244\n"
    "v_lshl_add_u32 v245, v243, 11, v244\n"
    "s_lshl_b32 s69, s61, 18\n"
    "s_lshl_b32 s70, s62, 16\n"
    "s_add_u32 s69, s69, s70\n"
    "s_add_u32 s70, s69, 0\n"
    "v_add_u32 v220, s70, v245\n"
    "s_add_u32 s70, s69, 15360\n"
    "v_add_u32 v221, s70, v245\n"
    "s_add_u32 s70, s69, 30720\n"
    "v_add_u32 v222, s70, v245\n"
    "s_add_u32 s70, s69, 46080\n"
    "v_add_u32 v223, s70, v245\n"
    "s_add_u32 s70, s69, 131072\n"
    "v_add_u32 v224, s70, v245\n"
    "s_add_u32 s70, s69, 146432\n"
    "v_add_u32 v225, s70, v245\n"
    "s_add_u32 s70, s69, 161792\n"
    "v_add_u32 v226, s70, v245\n"
    "s_add_u32 s70, s69, 177152\n"
    "v_add_u32 v227, s70, v245\n"
    "s_sub_u32 s69, s63, 28\n"
    "s_mul_i32 s69, s69, 144\n"
    "s_and_b32 s70, s60, 1\n"
    "s_mul_i32 s70, s70, 72\n"
    "s_add_u32 s69, s69, s70\n"
    "s_add_u32 s69, s69, 448\n"
    "s_lshl_b32 s69, s69, 11\n"
    "s_add_u32 s70, s69, 0\n"
    "v_add_u32 v228, s70, v245\n"
    "s_add_u32 s70, s69, 15360\n"
    "v_add_u32 v229, s70, v245\n"
    "s_add_u32 s70, s69, 30720\n"
    "v_add_u32 v230, s70, v245\n"
    "s_add_u32 s70, s69, 46080\n"
    "v_add_u32 v231, s70, v245\n"
    "s_add_u32 s70, s69, 65536\n"
    "v_add_u32 v232, s70, v245\n"
    "s_add_u32 s70, s69, 80896\n"
    "v_add_u32 v233, s70, v245\n"
    "s_add_u32 s70, s69, 96256\n"
    "v_add_u32 v234, s70, v245\n"
    "s_add_u32 s70, s69, 111616\n"
    "v_add_u32 v235, s70, v245\n"
    "s_add_u32 s70, s69, 131072\n"
    "v_add_u32 v236, s70, v245\n"
    "s_lshl_b32 s69, s61, 14\n"
    "s_lshl_b32 s70, s62, 12\n"
    "s_add_u32 s69, s69, s70\n"
    "s_add_u32 s72, s69, 0\n"
    "s_add_u32 s73, s69, 8192\n"
    "s_add_u32 s74, s69, 32768\n"
    "s_add_u32 s75, s69, 40960\n"
    "s_mul_i32 s69, s60, 9216\n"
    "s_add_u32 s76, s69, 65536\n"
    "s_add_u32 s77, s69, 69632\n"
    "s_add_u32 s78, s69, 73728\n"
    "s_add_u32 s79, s69, 102400\n"
    "s_add_u32 s80, s69, 106496\n"
    "s_add_u32 s81, s69, 110592\n"
    "s_mov_b32 s65, 0\n"
    "s_mov_b32 s66, 0\n"
    "s_waitcnt lgkmcnt(0)\n"
    "s_lshl_b32 s69, s64, 19\n"
    "s_add_u32 s36, s16, s69\n"
    "s_addc_u32 s37, s17, 0\n"
    "s_and_b32 s37, s37, 0xffff\n"
    "s_sub_u32 s38, 4194304, s69\n"
    "s_mov_b32 s39, 0x20000\n"
    "s_cmp_lt_u32 s60, 2\n"
    "s_cselect_b32 s48, s22, s24\n"
    "s_cselect_b32 s49, s23, s25\n"
    "s_and_b32 s49, s49, 0xffff\n"
    "s_mov_b32 s50, 2097152\n"
    "s_mov_b32 s51, 0x20000\n"
    "s_mul_hi_u32 s10, s4, 1284865837\n"
    "s_mul_i32 s11, s4, 1481765933\n"
    "s_add_u32 s10, s10, s11\n"
    "s_mul_i32 s11, s5, 1284865837\n"
    "s_add_u32 s10, s10, s11\n"
    "s_mul_i32 s11, s4, 1284865837\n"
    "s_add_u32 s4, s11, 4150755663\n"
    "s_addc_u32 s5, s10, 335903614\n"
    "s_mov_b32 s56, s16\n"
    "s_and_b32 s57, s17, 0xffff\n"
    "s_mov_b32 s58, 4194304\n"
    "s_mov_b32 s59, 0x20000\n"
    "s_waitcnt vmcnt(0)\n"
    "s_mov_b32 s98, 0xffff0000\n"
    "v_lshlrev_b32 v32, 16, v8\n"
    "v_and_b32 v33, s98, v8\n"
    "v_lshlrev_b32 v34, 16, v9\n"
    "v_and_b32 v35, s98, v9\n"
    "v_lshlrev_b32 v36, 16, v10\n"
    "v_and_b32 v37, s98, v10\n"
    "v_lshlrev_b32 v38, 16, v11\n"
    "v_and_b32 v39, s98, v11\n"
    "v_lshlrev_b32 v40, 16, v12\n"
    "v_and_b32 v41, s98, v12\n"
    "v_lshlrev_b32 v42, 16, v13\n"
    "v_and_b32 v43, s98, v13\n"
    "v_lshlrev_b32 v44, 16, v14\n"
    "v_and_b32 v45, s98, v14\n"
    "v_lshlrev_b32 v46, 16, v15\n"
    "v_and_b32 v47, s98, v15\n"
    "v_lshlrev_b32 v48, 16, v16\n"
    "v_and_b32 v49, s98, v16\n"
    "v_lshlrev_b32 v50, 16, v17\n"
    "v_and_b32 v51, s98, v17\n"
    "v_lshlrev_b32 v52, 16, v18\n"
    "v_and_b32 v53, s98, v18\n"
    "v_lshlrev_b32 v54, 16, v19\n"
    "v_and_b32 v55, s98, v19\n"
    "v_lshlrev_b32 v56, 16, v20\n"
    "v_and_b32 v57, s98, v20\n"
    "v_lshlrev_b32 v58, 16, v21\n"
    "v_and_b32 v59, s98, v21\n"
    "v_lshlrev_b32 v60, 16, v22\n"
    "v_and_b32 v61, s98, v22\n"
    "v_lshlrev_b32 v62, 16, v23\n"
    "v_and_b32 v63, s98, v23\n"
    "v_pk_add_f32 v[64:65], v[32:33], v[34:35]\n"
    "v_pk_add_f32 v[66:67], v[36:37], v[38:39]\n"
    "v_pk_add_f32 v[68:69], v[48:49], v[50:51]\n"
    "v_pk_add_f32 v[70:71], v[52:53], v[54:55]\n"
    "v_pk_add_f32 v[64:65], v[64:65], v[40:41]\n"
    "v_pk_add_f32 v[66:67], v[66:67], v[42:43]\n"
    "v_pk_add_f32 v[68:69], v[68:69], v[56:57]\n"
    "v_pk_add_f32 v[70:71], v[70:71], v[58:59]\n"
    "v_pk_add_f32 v[64:65], v[64:65], v[44:45]\n"
    "v_pk_add_f32 v[66:67], v[66:67], v[46:47]\n"
    "v_pk_add_f32 v[68:69], v[68:69], v[60:61]\n"
    "v_pk_add_f32 v[70:71], v[70:71], v[62:63]\n"
    "v_pk_add_f32 v[64:65], v[64:65], v[66:67]\n"
    "v_pk_add_f32 v[68:69], v[68:69], v[70:71]\n"
    "v_add_f32 v64, v64, v65\n"
    "v_add_f32 v68, v68, v69\n"
    "s_nop 1\n"
    "v_add_f32_dpp v64, v64, v64 row_ror:8 row_mask:0xf bank_mask:0xf\n"
    "v_add_f32_dpp v68, v68, v68 row_ror:8 row_mask:0xf bank_mask:0xf\n"
    "s_nop 1\n"
    "v_add_f32_dpp v64, v64, v64 row_ror:4 row_mask:0xf bank_mask:0xf\n"
    "v_add_f32_dpp v68, v68, v68 row_ror:4 row_mask:0xf bank_mask:0xf\n"
    "s_nop 1\n"
    "v_add_f32_dpp v64, v64, v64 row_ror:2 row_mask:0xf bank_mask:0xf\n"
    "v_add_f32_dpp v68, v68, v68 row_ror:2 row_mask:0xf bank_mask:0xf\n"
    "s_nop 1\n"
    "v_add_f32_dpp v64, v64, v64 row_ror:1 row_mask:0xf bank_mask:0xf\n"
    "v_add_f32_dpp v68, v68, v68 row_ror:1 row_mask:0xf bank_mask:0xf\n"
    "v_mov_b32 v65, v64\n"
    "v_mov_b32 v69, v68\n"
    "s_nop 1\n"
    "v_permlane16_swap_b32 v64, v65\n"
    "v_permlane16_swap_b32 v68, v69\n"
    "s_nop 1\n"
    "v_add_f32 v64, v64, v65\n"
    "v_add_f32 v68, v68, v69\n"
    "v_mov_b32 v65, v64\n"
    "v_mov_b32 v69, v68\n"
    "s_nop 1\n"
    "v_permlane32_swap_b32 v64, v65\n"
    "v_permlane32_swap_b32 v68, v69\n"
    "s_nop 1\n"
    "v_add_f32 v64, v64, v65\n"
    "v_add_f32 v68, v68, v69\n"
    "v_mul_f32 v64, 0xba800000, v64\n"
    "v_mul_f32 v68, 0xba800000, v68\n"
    "v_pk_add_f32 v[32:33], v[32:33], v[64:65] op_sel_hi:[1,0]\n"
    "v_pk_add_f32 v[34:35], v[34:35], v[64:65] op_sel_hi:[1,0]\n"
    "v_pk_add_f32 v[36:37], v[36:37], v[64:65] op_sel_hi:[1,0]\n"
    "v_pk_add_f32 v[38:39], v[38:39], v[64:65] op_sel_hi:[1,0]\n"
    "v_pk_add_f32 v[40:41], v[40:41], v[64:65] op_sel_hi:[1,0]\n"
    "v_pk_add_f32 v[42:43], v[42:43], v[64:65] op_sel_hi:[1,0]\n"
    "v_pk_add_f32 v[44:45], v[44:45], v[64:65] op_sel_hi:[1,0]\n"
    "v_pk_add_f32 v[46:47], v[46:47], v[64:65] op_sel_hi:[1,0]\n"
    "v_pk_add_f32 v[48:49], v[48:49], v[68:69] op_sel_hi:[1,0]\n"
    "v_pk_add_f32 v[50:51], v[50:51], v[68:69] op_sel_hi:[1,0]\n"
    "v_pk_add_f32 v[52:53], v[52:53], v[68:69] op_sel_hi:[1,0]\n"
    "v_pk_add_f32 v[54:55], v[54:55], v[68:69] op_sel_hi:[1,0]\n"
    "v_pk_add_f32 v[56:57], v[56:57], v[68:69] op_sel_hi:[1,0]\n"
    "v_pk_add_f32 v[58:59], v[58:59], v[68:69] op_sel_hi:[1,0]\n"
    "v_pk_add_f32 v[60:61], v[60:61], v[68:69] op_sel_hi:[1,0]\n"
    "v_pk_add_f32 v[62:63], v[62:63], v[68:69] op_sel_hi:[1,0]\n"
    "v_pk_mul_f32 v[72:73], v[32:33], v[32:33]\n"
    "v_pk_mul_f32 v[74:75], v[34:35], v[34:35]\n"
    "v_pk_mul_f32 v[76:77], v[48:49], v[48:49]\n"
    "v_pk_mul_f32 v[78:79], v[50:51], v[50:51]\n"
    "v_pk_fma_f32 v[72:73], v[36:37], v[36:37], v[72:73]\n"
    "v_pk_fma_f32 v[74:75], v[38:39], v[38:39], v[74:75]\n"
    "v_pk_fma_f32 v[76:77], v[52:53], v[52:53], v[76:77]\n"
    "v_pk_fma_f32 v[78:79], v[54:55], v[54:55], v[78:79]\n"
    "v_pk_fma_f32 v[72:73], v[40:41], v[40:41], v[72:73]\n"
    "v_pk_fma_f32 v[74:75], v[42:43], v[42:43], v[74:75]\n"
    "v_pk_fma_f32 v[76:77], v[56:57], v[56:57], v[76:77]\n"
    "v_pk_fma_f32 v[78:79], v[58:59], v[58:59], v[78:79]\n"
    "v_pk_fma_f32 v[72:73], v[44:45], v[44:45], v[72:73]\n"
    "v_pk_fma_f32 v[74:75], v[46:47], v[46:47], v[74:75]\n"
    "v_pk_fma_f32 v[76:77], v[60:61], v[60:61], v[76:77]\n"
    "v_pk_fma_f32 v[78:79], v[62:63], v[62:63], v[78:79]\n"
    "v_pk_add_f32 v[72:73], v[72:73], v[74:75]\n"
    "v_pk_add_f32 v[76:77], v[76:77], v[78:79]\n"
    "v_add_f32 v72, v72, v73\n"
    "v_add_f32 v76, v76, v77\n"
    "s_nop 1\n"
    "v_add_f32_dpp v72, v72, v72 row_ror:8 row_mask:0xf bank_mask:0xf\n"
    "v_add_f32_dpp v76, v76, v76 row_ror:8 row_mask:0xf bank_mask:0xf\n"
    "s_nop 1\n"
    "v_add_f32_dpp v72, v72, v72 row_ror:4 row_mask:0xf bank_mask:0xf\n"
    "v_add_f32_dpp v76, v76, v76 row_ror:4 row_mask:0xf bank_mask:0xf\n"
    "s_nop 1\n"
    "v_add_f32_dpp v72, v72, v72 row_ror:2 row_mask:0xf bank_mask:0xf\n"
    "v_add_f32_dpp v76, v76, v76 row_ror:2 row_mask:0xf bank_mask:0xf\n"
    "s_nop 1\n"
    "v_add_f32_dpp v72, v72, v72 row_ror:1 row_mask:0xf bank_mask:0xf\n"
    "v_add_f32_dpp v76, v76, v76 row_ror:1 row_mask:0xf bank_mask:0xf\n"
    "v_mov_b32 v73, v72\n"
    "v_mov_b32 v77, v76\n"
    "s_nop 1\n"
    "v_permlane16_swap_b32 v72, v73\n"
    "v_permlane16_swap_b32 v76, v77\n"
    "s_nop 1\n"
    "v_add_f32 v72, v72, v73\n"
    "v_add_f32 v76, v76, v77\n"
    "v_mov_b32 v73, v72\n"
    "v_mov_b32 v77, v76\n"
    "s_nop 1\n"
    "v_permlane32_swap_b32 v72, v73\n"
    "v_permlane32_swap_b32 v76, v77\n"
    "s_nop 1\n"
    "v_add_f32 v72, v72, v73\n"
    "v_add_f32 v76, v76, v77\n"
    "v_mul_f32 v72, 0x3a800000, v72\n"
    "v_mul_f32 v76, 0x3a800000, v76\n"
    "v_cvt_pk_bf16_f32 v72, v72, v72\n"
    "v_lshlrev_b32 v72, 16, v72\n"
    "v_cvt_pk_bf16_f32 v76, v76, v76\n"
    "v_lshlrev_b32 v76, 16, v76\n"
    "v_add_f32 v72, 0x3727c5ac, v72\n"
    "v_add_f32 v76, 0x3727c5ac, v76\n"
    "v_cvt_pk_bf16_f32 v72, v72, v72\n"
    "v_lshlrev_b32 v72, 16, v72\n"
    "v_cvt_pk_bf16_f32 v76, v76, v76\n"
    "v_lshlrev_b32 v76, 16, v76\n"
    "v_sqrt_f32 v72, v72\n"
    "v_sqrt_f32 v76, v76\n"
    "s_nop 0\n"
    "v_cvt_pk_bf16_f32 v72, v72, v72\n"
    "v_lshlrev_b32 v72, 16, v72\n"
    "v_cvt_pk_bf16_f32 v76, v76, v76\n"
    "v_lshlrev_b32 v76, 16, v76\n"
    "v_rcp_f32 v72, v72\n"
    "v_rcp_f32 v76, v76\n"
    "s_nop 0\n"
    "v_pk_mul_f32 v[32:33], v[32:33], v[72:73] op_sel_hi:[1,0]\n"
    "v_pk_mul_f32 v[34:35], v[34:35], v[72:73] op_sel_hi:[1,0]\n"
    "v_pk_mul_f32 v[36:37], v[36:37], v[72:73] op_sel_hi:[1,0]\n"
    "v_pk_mul_f32 v[38:39], v[38:39], v[72:73] op_sel_hi:[1,0]\n"
    "v_pk_mul_f32 v[40:41], v[40:41], v[72:73] op_sel_hi:[1,0]\n"
    "v_pk_mul_f32 v[42:43], v[42:43], v[72:73] op_sel_hi:[1,0]\n"
    "v_pk_mul_f32 v[44:45], v[44:45], v[72:73] op_sel_hi:[1,0]\n"
    "v_pk_mul_f32 v[46:47], v[46:47], v[72:73] op_sel_hi:[1,0]\n"
    "v_cvt_pk_bf16_f32 v8, v32, v33\n"
    "v_cvt_pk_bf16_f32 v9, v34, v35\n"
    "v_cvt_pk_bf16_f32 v10, v36, v37\n"
    "v_cvt_pk_bf16_f32 v11, v38, v39\n"
    "v_cvt_pk_bf16_f32 v12, v40, v41\n"
    "v_cvt_pk_bf16_f32 v13, v42, v43\n"
    "v_cvt_pk_bf16_f32 v14, v44, v45\n"
    "v_cvt_pk_bf16_f32 v15, v46, v47\n"
    "v_pk_mul_f32 v[48:49], v[48:49], v[76:77] op_sel_hi:[1,0]\n"
    "v_pk_mul_f32 v[50:51], v[50:51], v[76:77] op_sel_hi:[1,0]\n"
    "v_pk_mul_f32 v[52:53], v[52:53], v[76:77] op_sel_hi:[1,0]\n"
    "v_pk_mul_f32 v[54:55], v[54:55], v[76:77] op_sel_hi:[1,0]\n"
    "v_pk_mul_f32 v[56:57], v[56:57], v[76:77] op_sel_hi:[1,0]\n"
    "v_pk_mul_f32 v[58:59], v[58:59], v[76:77] op_sel_hi:[1,0]\n"
    "v_pk_mul_f32 v[60:61], v[60:61], v[76:77] op_sel_hi:[1,0]\n"
    "v_pk_mul_f32 v[62:63], v[62:63], v[76:77] op_sel_hi:[1,0]\n"
    "v_cvt_pk_bf16_f32 v16, v48, v49\n"
    "v_cvt_pk_bf16_f32 v17, v50, v51\n"
    "v_cvt_pk_bf16_f32 v18, v52, v53\n"
    "v_cvt_pk_bf16_f32 v19, v54, v55\n"
    "v_cvt_pk_bf16_f32 v20, v56, v57\n"
    "v_cvt_pk_bf16_f32 v21, v58, v59\n"
    "v_cvt_pk_bf16_f32 v22, v60, v61\n"
    "v_cvt_pk_bf16_f32 v23, v62, v63\n"
    "buffer_store_dwordx4 v[8:11], v24, s[56:59], s99 offen offset:0 sc1\n"
    "buffer_store_dwordx4 v[12:15], v24, s[56:59], s99 offen offset:1024 sc1\n"
    "buffer_store_dwordx4 v[16:19], v24, s[56:59], s99 offen offset:2048 sc1\n"
    "buffer_store_dwordx4 v[20:23], v24, s[56:59], s99 offen offset:3072 sc1\n"
    "s_mov_b32 s65, 0\n"
    "s_mov_b32 s66, 0\n"
    "s_mov_b32 m0, s76\n"
    "s_nop 0\n"
    "buffer_load_dwordx4 v228, s[48:51], s65 offen offset:0 nt lds\n"
    "buffer_load_dwordx4 v229, s[48:51], s65 offen offset:1024 nt lds\n"
    "buffer_load_dwordx4 v230, s[48:51], s65 offen offset:2048 nt lds\n"
    "buffer_load_dwordx4 v231, s[48:51], s65 offen offset:3072 nt lds\n"
    "s_mov_b32 m0, s77\n"
    "s_nop 0\n"
    "buffer_load_dwordx4 v232, s[48:51], s65 offen offset:0 nt lds\n"
    "buffer_load_dwordx4 v233, s[48:51], s65 offen offset:1024 nt lds\n"
    "buffer_load_dwordx4 v234, s[48:51], s65 offen offset:2048 nt lds\n"
    "buffer_load_dwordx4 v235, s[48:51], s65 offen offset:3072 nt lds\n"
    "s_mov_b32 m0, s78\n"
    "s_nop 0\n"
    "buffer_load_dwordx4 v236, s[48:51], s65 offen offset:0 nt lds\n"
    "s_mov_b32 s65, 128\n"
    "s_mov_b32 s66, 458752\n"
    "s_mov_b32 m0, s79\n"
    "s_nop 0\n"
    "buffer_load_dwordx4 v228, s[48:51], s65 offen offset:0 nt lds\n"
    "buffer_load_dwordx4 v229, s[48:51], s65 offen offset:1024 nt lds\n"
    "buffer_load_dwordx4 v230, s[48:51], s65 offen offset:2048 nt lds\n"
    "buffer_load_dwordx4 v231, s[48:51], s65 offen offset:3072 nt lds\n"
    "s_mov_b32 m0, s80\n"
    "s_nop 0\n"
    "buffer_load_dwordx4 v232, s[48:51], s65 offen offset:0 nt lds\n"
    "buffer_load_dwordx4 v233, s[48:51], s65 offen offset:1024 nt lds\n"
    "buffer_load_dwordx4 v234, s[48:51], s65 offen offset:2048 nt lds\n"
    "buffer_load_dwordx4 v235, s[48:51], s65 offen offset:3072 nt lds\n"
    "s_mov_b32 m0, s81\n"
    "s_nop 0\n"
    "buffer_load_dwordx4 v236, s[48:51], s65 offen offset:0 nt lds\n"
    "s_load_dwordx2 s[16:17], %1, 64\n"
    "s_load_dwordx2 s[18:19], %1, 72\n"
    "s_load_dwordx2 s[20:21], %1, 104\n"
    "v_and_b32 v243, 7, v241\n"
    "v_xor_b32 v243, v242, v243\n"
    "v_xor_b32 v244, 4, v243\n"
    "s_lshl_b32 s69, s61, 14\n"
    "v_lshlrev_b32 v245, 7, v241\n"
    "v_add_u32 v246, s69, v245\n"
    "v_lshl_add_u32 v208, v243, 4, v246\n"
    "v_lshl_add_u32 v209, v244, 4, v246\n"
    "s_mul_i32 s69, s62, 18432\n"
    "s_add_u32 s69, s69, 65536\n"
    "v_add_u32 v246, s69, v245\n"
    "v_lshl_add_u32 v218, v243, 4, v246\n"
    "v_lshl_add_u32 v219, v244, 4, v246\n"
    "s_cmp_eq_u32 s60, 0\n"
    "s_cbranch_scc0 LTS0_%=_6\n"
    "s_lshl_b32 s69, s8, 1\n"
    "s_add_u32 s69, s69, 0\n"
    "s_lshl_b32 s69, s69, 6\n"
    "v_add_u32 v243, s69, v240\n"
    "v_lshrrev_b32 v244, 3, v243\n"
    "v_and_b32 v244, 2047, v244\n"
    "v_lshlrev_b32 v244, 12, v244\n"
    "v_and_b32 v243, 7, v243\n"
    "v_add_u32 v243, 16, v243\n"
    "v_lshl_add_u32 v237, v243, 7, v244\n"
    "v_mov_b32 v254, 0\n"
    "v_mov_b32 v246, 0\n"
    "s_branch LTSE_%=_6\n"
    "LTS0_%=_6:\n"
    "s_cmp_eq_u32 s60, 1\n"
    "s_cbranch_scc0 LTS1_%=_6\n"
    "s_lshl_b32 s69, s8, 1\n"
    "s_add_u32 s69, s69, 0\n"
    "s_lshl_b32 s69, s69, 6\n"
    "v_add_u32 v243, s69, v240\n"
    "v_lshrrev_b32 v244, 3, v243\n"
    "v_and_b32 v244, 2047, v244\n"
    "v_lshlrev_b32 v244, 12, v244\n"
    "v_and_b32 v243, 7, v243\n"
    "v_add_u32 v243, 0, v243\n"
    "v_lshl_add_u32 v254, v243, 7, v244\n"
    "s_lshl_b32 s69, s8, 1\n"
    "s_add_u32 s69, s69, 1\n"
    "s_lshl_b32 s69, s69, 6\n"
    "v_add_u32 v243, s69, v240\n"
    "v_lshrrev_b32 v244, 3, v243\n"
    "v_and_b32 v244, 2047, v244\n"
    "v_lshlrev_b32 v244, 12, v244\n"
    "v_and_b32 v243, 7, v243\n"
    "v_add_u32 v243, 16, v243\n"
    "v_lshl_add_u32 v237, v243, 7, v244\n"
    "v_mov_b32 v246, 0\n"
    "s_branch LTSE_%=_6\n"
    "LTS1_%=_6:\n"
    "s_cmp_eq_u32 s60, 2\n"
    "s_cbranch_scc0 LTS2_%=_6\n"
    "s_lshl_b32 s69, s8, 1\n"
    "s_add_u32 s69, s69, 1\n"
    "s_lshl_b32 s69, s69, 6\n"
    "v_add_u32 v243, s69, v240\n"
    "v_lshrrev_b32 v244, 3, v243\n"
    "v_and_b32 v244, 2047, v244\n"
    "v_lshlrev_b32 v244, 12, v244\n"
    "v_and_b32 v243, 7, v243\n"
    "v_add_u32 v243, 0, v243\n"
    "v_lshl_add_u32 v254, v243, 7, v244\n"
    "s_lshl_b32 s69, s8, 1\n"
    "s_add_u32 s69, s69, 0\n"
    "s_lshl_b32 s69, s69, 6\n"
    "v_add_u32 v243, s69, v240\n"
    "v_lshrrev_b32 v244, 3, v243\n"
    "v_and_b32 v244, 2047, v244\n"
    "v_lshlrev_b32 v244, 12, v244\n"
    "v_and_b32 v243, 7, v243\n"
    "v_add_u32 v243, 8, v243\n"
    "v_lshl_add_u32 v246, v243, 7, v244\n"
    "s_lshl_b32 s69, s8, 6\n"
    "v_add_u32 v243, s69, v240\n"
    "v_lshrrev_b32 v244, 13, v243\n"
    "v_mul_u32_u24 v244, 1792, v244\n"
    "v_lshrrev_b32 v237, 4, v243\n"
    "v_and_b32 v237, 511, v237\n"
    "v_add_u32 v244, v244, v237\n"
    "v_add_u32 v244, 512, v244\n"
    "v_lshlrev_b32 v244, 11, v244\n"
    "v_and_b32 v237, 15, v243\n"
    "v_lshl_add_u32 v237, v237, 7, v244\n"
    "s_branch LTSE_%=_6\n"
    "LTS2_%=_6:\n"
    "s_cmp_eq_u32 s60, 3\n"
    "s_cbranch_scc0 LTS3_%=_6\n"
    "s_lshl_b32 s69, s8, 6\n"
    "v_add_u32 v243, s69, v240\n"
    "v_lshrrev_b32 v244, 13, v243\n"
    "v_mul_u32_u24 v244, 1792, v244\n"
    "v_lshrrev_b32 v254, 4, v243\n"
    "v_and_b32 v254, 511, v254\n"
    "v_add_u32 v244, v244, v254\n"
    "v_add_u32 v244, 0, v244\n"
    "v_lshlrev_b32 v244, 11, v244\n"
    "v_and_b32 v254, 15, v243\n"
    "v_lshl_add_u32 v254, v254, 7, v244\n"
    "s_lshl_b32 s69, s8, 1\n"
    "s_add_u32 s69, s69, 1\n"
    "s_lshl_b32 s69, s69, 6\n"
    "v_add_u32 v243, s69, v240\n"
    "v_lshrrev_b32 v244, 3, v243\n"
    "v_and_b32 v244, 2047, v244\n"
    "v_lshlrev_b32 v244, 12, v244\n"
    "v_and_b32 v243, 7, v243\n"
    "v_add_u32 v243, 8, v243\n"
    "v_lshl_add_u32 v246, v243, 7, v244\n"
    "v_mov_b32 v237, 0\n"
    "s_branch LTSE_%=_6\n"
    "LTS3_%=_6:\n"
    "LTSE_%=_6:\n"
    "s_waitcnt lgkmcnt(0)\n"
    "s_cmp_lt_u32 s8, 128\n"
    "s_cselect_b32 s96, s16, s18\n"
    "s_cselect_b32 s97, s17, s19\n"
    "s_mov_b32 s15, 0x20000\n"
    "s_mov_b32 s9, 0x40000000\n"
    "s_cmp_eq_u32 s60, 2\n"
    "s_cselect_b32 s9, 4294967292, s9\n"
    "s_cmp_eq_u32 s60, 3\n"
    "s_cselect_b32 s9, 4294967287, s9\n"
    "s_lshl_b32 s69, s64, 5\n"
    "s_add_u32 s69, s69, s63\n"
    "s_lshl_b32 s69, s69, 3\n"
    "s_add_u32 s69, s69, 0\n"
    "s_waitcnt vmcnt(18)\n"
    "s_barrier\n"
    "s_cmp_lg_u32 s60, 0\n"
    "s_cbranch_scc1 LLNP_%=_7\n"
    "v_mov_b32 v249, s69\n"
    "v_mov_b32 v252, s4\n"
    "v_mov_b32 v253, s5\n"
    "s_mov_b64 s[10:11], exec\n"
    "s_mov_b64 exec, 1\n"
    "global_store_dwordx2 v249, v[252:253], s[6:7] sc1\n"
    "s_mov_b64 exec, s[10:11]\n"
    "LLNP_%=_7:\n"
    "s_cmp_eq_u32 s60, 0\n"
    "s_cbranch_scc1 LTL_%=_8\n"
    "s_cmp_eq_u32 s60, 3\n"
    "s_cselect_b32 s12, s20, s96\n"
    "s_cselect_b32 s13, s21, s97\n"
    "s_mov_b32 s14, 8388608\n"
    "s_cselect_b32 s14, 7340032, s14\n"
    "buffer_load_dword v253, v254, s[12:15], 0 offen\n"
    "LTL_%=_8:\n"
    "s_mov_b32 s12, s96\n"
    "s_mov_b32 s13, s97\n"
    "s_mov_b32 s14, 0\n"
    "v_mov_b32 v254, v246\n"
    "s_cmp_lg_u32 s60, 0\n"
    "s_cbranch_scc1 LLNW_%=_9\n"
    "s_lshl_b32 s69, s64, 8\n"
    "s_add_u32 s69, s69, 0\n"
    "v_lshlrev_b32 v249, 3, v240\n"
    "v_add_u32 v249, s69, v249\n"
    "s_mov_b64 s[10:11], exec\n"
    "s_mov_b32 exec_lo, -1\n"
    "s_mov_b32 exec_hi, 0\n"
    "s_mov_b32 s3, 0\n"
    "global_load_dwordx2 v[252:253], v249, s[6:7] sc1\n"
    "LWP_%=_10:\n"
    "global_load_dwordx2 v[206:207], v249, s[6:7] sc1\n"
    "s_waitcnt vmcnt(1)\n"
    "v_cmp_ne_u64 vcc, s[4:5], v[252:253]\n"
    "s_nop 4\n"
    "s_cbranch_vccz LWD_%=_10\n"
    "global_load_dwordx2 v[252:253], v249, s[6:7] sc1\n"
    "s_waitcnt vmcnt(1)\n"
    "v_cmp_ne_u64 vcc, s[4:5], v[206:207]\n"
    "s_nop 4\n"
    "s_cbranch_vccz LWD_%=_10\n"
    "s_add_u32 s3, s3, 1\n"
    "s_cmp_lt_u32 s3, 0x10000\n"
    "s_cbranch_scc1 LWP_%=_10\n"
    "LWD_%=_10:\n"
    "s_mov_b64 exec, s[10:11]\n"
    "LLNW_%=_9:\n"
    "s_barrier\n"
    "s_mov_b32 s65, 0\n"
    "s_mov_b32 m0, s72\n"
    "s_nop 0\n"
    "buffer_load_dwordx4 v220, s[36:39], s65 offen offset:0 sc1 lds\n"
    "buffer_load_dwordx4 v221, s[36:39], s65 offen offset:1024 sc1 lds\n"
    "buffer_load_dwordx4 v222, s[36:39], s65 offen offset:2048 sc1 lds\n"
    "buffer_load_dwordx4 v223, s[36:39], s65 offen offset:3072 sc1 lds\n"
    "s_mov_b32 m0, s73\n"
    "s_nop 0\n"
    "buffer_load_dwordx4 v224, s[36:39], s65 offen offset:0 sc1 lds\n"
    "buffer_load_dwordx4 v225, s[36:39], s65 offen offset:1024 sc1 lds\n"
    "buffer_load_dwordx4 v226, s[36:39], s65 offen offset:2048 sc1 lds\n"
    "buffer_load_dwordx4 v227, s[36:39], s65 offen offset:3072 sc1 lds\n"
    "s_mov_b32 s65, 128\n"
    "s_mov_b32 m0, s74\n"
    "s_nop 0\n"
    "buffer_load_dwordx4 v220, s[36:39], s65 offen offset:0 sc1 lds\n"
    "buffer_load_dwordx4 v221, s[36:39], s65 offen offset:1024 sc1 lds\n"
    "buffer_load_dwordx4 v222, s[36:39], s65 offen offset:2048 sc1 lds\n"
    "buffer_load_dwordx4 v223, s[36:39], s65 offen offset:3072 sc1 lds\n"
    "s_mov_b32 m0, s75\n"
    "s_nop 0\n"
    "buffer_load_dwordx4 v224, s[36:39], s65 offen offset:0 sc1 lds\n"
    "buffer_load_dwordx4 v225, s[36:39], s65 offen offset:1024 sc1 lds\n"
    "buffer_load_dwordx4 v226, s[36:39], s65 offen offset:2048 sc1 lds\n"
    "buffer_load_dwordx4 v227, s[36:39], s65 offen offset:3072 sc1 lds\n"
    "s_mov_b32 s65, 256\n"
    "s_mov_b32 s66, 917504\n"
    "s_waitcnt vmcnt(12)\n"
    "s_barrier\n"
    "ds_read_b128 a[80:83], v208 offset:0\n"
    "ds_read_b128 a[84:87], v209 offset:0\n"
    "ds_read_b128 a[88:91], v208 offset:2048\n"
    "ds_read_b128 a[92:95], v209 offset:2048\n"
    "ds_read_b128 a[96:99], v208 offset:4096\n"
    "ds_read_b128 a[100:103], v209 offset:4096\n"
    "ds_read_b128 a[104:107], v208 offset:6144\n"
    "ds_read_b128 a[108:111], v209 offset:6144\n"
    "ds_read_b128 a[144:147], v218 offset:0\n"
    "ds_read_b128 a[148:151], v219 offset:0\n"
    "ds_read_b128 a[152:155], v218 offset:2048\n"
    "ds_read_b128 a[156:159], v219 offset:2048\n"
    "ds_read_b128 a[160:163], v218 offset:4096\n"
    "ds_read_b128 a[164:167], v219 offset:4096\n"
    "ds_read_b128 a[168:171], v218 offset:6144\n"
    "ds_read_b128 a[172:175], v219 offset:6144\n"
    "ds_read_b128 a[176:179], v218 offset:8192\n"
    "ds_read_b128 a[180:183], v219 offset:8192\n"
    "ds_read_b128 a[184:187], v218 offset:10240\n"
    "ds_read_b128 a[188:191], v219 offset:10240\n"
    "ds_read_b128 a[192:195], v218 offset:12288\n"
    "ds_read_b128 a[196:199], v219 offset:12288\n"
    "ds_read_b128 a[200:203], v218 offset:14336\n"
    "ds_read_b128 a[204:207], v219 offset:14336\n"
    "ds_read_b128 a[208:211], v218 offset:16384\n"
    "ds_read_b128 a[212:215], v219 offset:16384\n"
    "s_waitcnt lgkmcnt(15)\n"
    "v_mfma_f32_16x16x32_bf16 v[0:3], a[80:83], a[144:147], 0\n"
    "v_mov_b32 v112, 0\n"
    "v_mov_b32 v113, 0\n"
    "v_mov_b32 v114, 0\n"
    "v_mfma_f32_16x16x32_bf16 v[0:3], a[84:87], a[148:151], v[0:3]\n"
    "v_mov_b32 v115, 0\n"
    "v_mov_b32 v116, 0\n"
    "v_mfma_f32_16x16x32_bf16 v[28:31], a[88:91], a[144:147], 0\n"
    "v_mov_b32 v117, 0\n"
    "v_mov_b32 v118, 0\n"
    "v_mfma_f32_16x16x32_bf16 v[28:31], a[92:95], a[148:151], v[28:31]\n"
    "v_mov_b32 v119, 0\n"
    "v_mov_b32 v120, 0\n"
    "v_mfma_f32_16x16x32_bf16 v[56:59], a[96:99], a[144:147], 0\n"
    "v_mov_b32 v121, 0\n"
    "v_mov_b32 v122, 0\n"
    "v_mfma_f32_16x16x32_bf16 v[56:59], a[100:103], a[148:151], v[56:59]\n"
    "v_mov_b32 v123, 0\n"
    "v_mov_b32 v124, 0\n"
    "v_mov_b32 v125, 0\n"
    "v_mfma_f32_16x16x32_bf16 v[84:87], a[104:107], a[144:147], 0\n"
    "v_mov_b32 v126, 0\n"
    "v_mov_b32 v127, 0\n"
    "v_mfma_f32_16x16x32_bf16 v[84:87], a[108:111], a[148:151], v[84:87]\n"
    "v_mov_b32 v128, 0\n"
    "v_mov_b32 v129, 0\n"
    "v_mfma_f32_16x16x32_bf16 v[4:7], a[80:83], a[152:155], 0\n"
    "v_mov_b32 v130, 0\n"
    "v_mov_b32 v131, 0\n"
    "s_waitcnt lgkmcnt(14)\n"
    "v_mfma_f32_16x16x32_bf16 v[4:7], a[84:87], a[156:159], v[4:7]\n"
    "v_mov_b32 v132, 0\n"
    "v_mov_b32 v133, 0\n"
    "v_mfma_f32_16x16x32_bf16 v[32:35], a[88:91], a[152:155], 0\n"
    "v_mov_b32 v134, 0\n"
    "v_mov_b32 v135, 0\n"
    "v_mfma_f32_16x16x32_bf16 v[32:35], a[92:95], a[156:159], v[32:35]\n"
    "s_waitcnt vmcnt(8) lgkmcnt(0)\n"
    "s_barrier\n"
    "buffer_load_dword v253, v254, s[12:15], 0 offen\n"
    "v_accvgpr_write_b32 a64, 0\n"
    "v_accvgpr_write_b32 a65, 0\n"
    "v_accvgpr_write_b32 a66, 0\n"
    "v_mfma_f32_16x16x32_bf16 v[60:63], a[96:99], a[152:155], 0\n"
    "ds_read_b128 a[112:115], v208 offset:8192\n"
    "v_accvgpr_write_b32 a67, 0\n"
    "v_accvgpr_write_b32 a48, 0\n"
    "v_mfma_f32_16x16x32_bf16 v[60:63], a[100:103], a[156:159], v[60:63]\n"
    "s_mov_b32 m0, s76\n"
    "s_nop 0\n"
    "buffer_load_dwordx4 v228, s[48:51], s65 offen offset:0 nt lds\n"
    "v_accvgpr_write_b32 a49, 0\n"
    "v_accvgpr_write_b32 a50, 0\n"
    "v_mfma_f32_16x16x32_bf16 v[88:91], a[104:107], a[152:155], 0\n"
    "ds_read_b128 a[116:119], v209 offset:8192\n"
    "v_accvgpr_write_b32 a51, 0\n"
    "v_accvgpr_write_b32 a16, 0\n"
    "v_mfma_f32_16x16x32_bf16 v[88:91], a[108:111], a[156:159], v[88:91]\n"
    "v_accvgpr_write_b32 a17, 0\n"
    "v_accvgpr_write_b32 a18, 0\n"
    "v_mfma_f32_16x16x32_bf16 v[8:11], a[80:83], a[160:163], 0\n"
    "ds_read_b128 a[120:123], v208 offset:10240\n"
    "v_accvgpr_write_b32 a19, 0\n"
    "v_mov_b32 v136, 0\n"
    "v_mov_b32 v137, 0\n"
    "v_mfma_f32_16x16x32_bf16 v[8:11], a[84:87], a[164:167], v[8:11]\n"
    "v_mov_b32 v138, 0\n"
    "v_mov_b32 v139, 0\n"
    "v_mfma_f32_16x16x32_bf16 v[36:39], a[88:91], a[160:163], 0\n"
    "ds_read_b128 a[124:127], v209 offset:10240\n"
    "v_mov_b32 v140, 0\n"
    "v_mov_b32 v141, 0\n"
    "v_mfma_f32_16x16x32_bf16 v[36:39], a[92:95], a[164:167], v[36:39]\n"
    "buffer_load_dwordx4 v229, s[48:51], s65 offen offset:1024 nt lds\n"
    "v_mov_b32 v142, 0\n"
    "v_mov_b32 v143, 0\n"
    "v_mfma_f32_16x16x32_bf16 v[64:67], a[96:99], a[160:163], 0\n"
    "ds_read_b128 a[128:131], v208 offset:12288\n"
    "v_mov_b32 v144, 0\n"
    "v_mov_b32 v145, 0\n"
    "v_mfma_f32_16x16x32_bf16 v[64:67], a[100:103], a[164:167], v[64:67]\n"
    "v_mov_b32 v146, 0\n"
    "v_mov_b32 v147, 0\n"
    "v_mfma_f32_16x16x32_bf16 v[92:95], a[104:107], a[160:163], 0\n"
    "ds_read_b128 a[132:135], v209 offset:12288\n"
    "v_mov_b32 v148, 0\n"
    "v_mov_b32 v149, 0\n"
    "v_mov_b32 v150, 0\n"
    "v_mfma_f32_16x16x32_bf16 v[92:95], a[108:111], a[164:167], v[92:95]\n"
    "v_mov_b32 v151, 0\n"
    "v_mov_b32 v152, 0\n"
    "v_mfma_f32_16x16x32_bf16 v[12:15], a[80:83], a[168:171], 0\n"
    "ds_read_b128 a[136:139], v208 offset:14336\n"
    "v_mov_b32 v153, 0\n"
    "v_mov_b32 v154, 0\n"
    "v_mfma_f32_16x16x32_bf16 v[12:15], a[84:87], a[172:175], v[12:15]\n"
    "v_mov_b32 v155, 0\n"
    "v_mov_b32 v156, 0\n"
    "v_mfma_f32_16x16x32_bf16 v[40:43], a[88:91], a[168:171], 0\n"
    "ds_read_b128 a[140:143], v209 offset:14336\n"
    "buffer_load_dwordx4 v230, s[48:51], s65 offen offset:2048 nt lds\n"
    "v_mov_b32 v157, 0\n"
    "v_mov_b32 v158, 0\n"
    "v_mfma_f32_16x16x32_bf16 v[40:43], a[92:95], a[172:175], v[40:43]\n"
    "v_mov_b32 v159, 0\n"
    "v_accvgpr_write_b32 a68, 0\n"
    "v_accvgpr_write_b32 a69, 0\n"
    "v_mfma_f32_16x16x32_bf16 v[68:71], a[96:99], a[168:171], 0\n"
    "v_accvgpr_write_b32 a70, 0\n"
    "v_accvgpr_write_b32 a71, 0\n"
    "v_mfma_f32_16x16x32_bf16 v[68:71], a[100:103], a[172:175], v[68:71]\n"
    "v_accvgpr_write_b32 a52, 0\n"
    "v_accvgpr_write_b32 a53, 0\n"
    "v_mfma_f32_16x16x32_bf16 v[96:99], a[104:107], a[168:171], 0\n"
    "v_accvgpr_write_b32 a54, 0\n"
    "v_accvgpr_write_b32 a55, 0\n"
    "v_mfma_f32_16x16x32_bf16 v[96:99], a[108:111], a[172:175], v[96:99]\n"
    "v_accvgpr_write_b32 a20, 0\n"
    "v_accvgpr_write_b32 a21, 0\n"
    "v_mfma_f32_16x16x32_bf16 v[16:19], a[80:83], a[176:179], 0\n"
    "buffer_load_dwordx4 v231, s[48:51], s65 offen offset:3072 nt lds\n"
    "v_accvgpr_write_b32 a22, 0\n"
    "v_accvgpr_write_b32 a23, 0\n"
    "v_mfma_f32_16x16x32_bf16 v[16:19], a[84:87], a[180:183], v[16:19]\n"
    "v_mov_b32 v160, 0\n"
    "v_mov_b32 v161, 0\n"
    "v_mov_b32 v162, 0\n"
    "v_mfma_f32_16x16x32_bf16 v[44:47], a[88:91], a[176:179], 0\n"
    "v_mov_b32 v163, 0\n"
    "v_mov_b32 v164, 0\n"
    "v_mfma_f32_16x16x32_bf16 v[44:47], a[92:95], a[180:183], v[44:47]\n"
    "v_mov_b32 v165, 0\n"
    "v_mov_b32 v166, 0\n"
    "v_mfma_f32_16x16x32_bf16 v[72:75], a[96:99], a[176:179], 0\n"
    "v_mov_b32 v167, 0\n"
    "v_mov_b32 v168, 0\n"
    "v_mfma_f32_16x16x32_bf16 v[72:75], a[100:103], a[180:183], v[72:75]\n"
    "v_mov_b32 v169, 0\n"
    "v_mov_b32 v170, 0\n"
    "v_mfma_f32_16x16x32_bf16 v[100:103], a[104:107], a[176:179], 0\n"
    "v_mov_b32 v171, 0\n"
    "v_mov_b32 v172, 0\n"
    "v_mov_b32 v173, 0\n"
    "v_mfma_f32_16x16x32_bf16 v[100:103], a[108:111], a[180:183], v[100:103]\n"
    "s_mov_b32 m0, s77\n"
    "s_nop 0\n"
    "buffer_load_dwordx4 v232, s[48:51], s65 offen offset:0 nt lds\n"
    "v_mov_b32 v174, 0\n"
    "v_mov_b32 v175, 0\n"
    "v_mfma_f32_16x16x32_bf16 v[20:23], a[80:83], a[184:187], 0\n"
    "s_add_u32 s9, s9, 1\n"
    "s_cmp_eq_u32 s9, 0\n"
    "s_cselect_b32 s14, 8388608, 0\n"
    "v_mov_b32 v176, 0\n"
    "v_mov_b32 v177, 0\n"
    "v_mfma_f32_16x16x32_bf16 v[20:23], a[84:87], a[188:191], v[20:23]\n"
    "v_mov_b32 v178, 0\n"
    "v_mov_b32 v179, 0\n"
    "v_mfma_f32_16x16x32_bf16 v[48:51], a[88:91], a[184:187], 0\n"
    "v_mov_b32 v180, 0\n"
    "v_mov_b32 v181, 0\n"
    "v_mfma_f32_16x16x32_bf16 v[48:51], a[92:95], a[188:191], v[48:51]\n"
    "v_mov_b32 v182, 0\n"
    "v_mov_b32 v183, 0\n"
    "v_mfma_f32_16x16x32_bf16 v[76:79], a[96:99], a[184:187], 0\n"
    "v_accvgpr_write_b32 a72, 0\n"
    "v_accvgpr_write_b32 a73, 0\n"
    "v_accvgpr_write_b32 a74, 0\n"
    "v_mfma_f32_16x16x32_bf16 v[76:79], a[100:103], a[188:191], v[76:79]\n"
    "buffer_load_dwordx4 v233, s[48:51], s65 offen offset:1024 nt lds\n"
    "v_accvgpr_write_b32 a75, 0\n"
    "v_accvgpr_write_b32 a56, 0\n"
    "v_mfma_f32_16x16x32_bf16 v[104:107], a[104:107], a[184:187], 0\n"
    "v_accvgpr_write_b32 a57, 0\n"
    "v_accvgpr_write_b32 a58, 0\n"
    "v_mfma_f32_16x16x32_bf16 v[104:107], a[108:111], a[188:191], v[104:107]\n"
    "v_accvgpr_write_b32 a59, 0\n"
    "v_accvgpr_write_b32 a24, 0\n"
    "v_mfma_f32_16x16x32_bf16 v[24:27], a[80:83], a[192:195], 0\n"
    "v_accvgpr_write_b32 a25, 0\n"
    "v_accvgpr_write_b32 a26, 0\n"
    "v_mfma_f32_16x16x32_bf16 v[24:27], a[84:87], a[196:199], v[24:27]\n"
    "v_accvgpr_write_b32 a27, 0\n"
    "v_mov_b32 v184, 0\n"
    "v_mov_b32 v185, 0\n"
    "v_mfma_f32_16x16x32_bf16 v[52:55], a[88:91], a[192:195], 0\n"
    "v_mov_b32 v186, 0\n"
    "v_mov_b32 v187, 0\n"
    "v_mfma_f32_16x16x32_bf16 v[52:55], a[92:95], a[196:199], v[52:55]\n"
    "v_mov_b32 v188, 0\n"
    "v_mov_b32 v189, 0\n"
    "v_mfma_f32_16x16x32_bf16 v[80:83], a[96:99], a[192:195], 0\n"
    "buffer_load_dwordx4 v234, s[48:51], s65 offen offset:2048 nt lds\n"
    "v_mov_b32 v190, 0\n"
    "v_mov_b32 v191, 0\n"
    "v_mfma_f32_16x16x32_bf16 v[80:83], a[100:103], a[196:199], v[80:83]\n"
    "v_mov_b32 v192, 0\n"
    "v_mov_b32 v193, 0\n"
    "v_mfma_f32_16x16x32_bf16 v[108:111], a[104:107], a[192:195], 0\n"
    "v_mov_b32 v194, 0\n"
    "v_mov_b32 v195, 0\n"
    "v_mfma_f32_16x16x32_bf16 v[108:111], a[108:111], a[196:199], v[108:111]\n"
    "v_mov_b32 v196, 0\n"
    "v_mov_b32 v197, 0\n"
    "v_mov_b32 v198, 0\n"
    "v_mfma_f32_16x16x32_bf16 a[32:35], a[80:83], a[200:203], 0\n"
    "v_mov_b32 v199, 0\n"
    "v_mov_b32 v200, 0\n"
    "v_mfma_f32_16x16x32_bf16 a[32:35], a[84:87], a[204:207], a[32:35]\n"
    "v_mov_b32 v201, 0\n"
    "v_mov_b32 v202, 0\n"
    "v_mfma_f32_16x16x32_bf16 a[36:39], a[88:91], a[200:203], 0\n"
    "buffer_load_dwordx4 v235, s[48:51], s65 offen offset:3072 nt lds\n"
    "v_mov_b32 v203, 0\n"
    "v_mov_b32 v204, 0\n"
    "v_mfma_f32_16x16x32_bf16 a[36:39], a[92:95], a[204:207], a[36:39]\n"
    "v_mov_b32 v205, 0\n"
    "v_mov_b32 v206, 0\n"
    "v_mfma_f32_16x16x32_bf16 a[40:43], a[96:99], a[200:203], 0\n"
    "v_mov_b32 v207, 0\n"
    "v_accvgpr_write_b32 a76, 0\n"
    "v_accvgpr_write_b32 a77, 0\n"
    "v_mfma_f32_16x16x32_bf16 a[40:43], a[100:103], a[204:207], a[40:43]\n"
    "v_accvgpr_write_b32 a78, 0\n"
    "v_accvgpr_write_b32 a79, 0\n"
    "v_mfma_f32_16x16x32_bf16 a[44:47], a[104:107], a[200:203], 0\n"
    "v_accvgpr_write_b32 a60, 0\n"
    "v_accvgpr_write_b32 a61, 0\n"
    "v_mfma_f32_16x16x32_bf16 a[44:47], a[108:111], a[204:207], a[44:47]\n"
    "v_accvgpr_write_b32 a62, 0\n"
    "v_accvgpr_write_b32 a63, 0\n"
    "v_mfma_f32_16x16x32_bf16 a[0:3], a[80:83], a[208:211], 0\n"
    "v_accvgpr_write_b32 a28, 0\n"
    "v_accvgpr_write_b32 a29, 0\n"
    "v_mfma_f32_16x16x32_bf16 a[0:3], a[84:87], a[212:215], a[0:3]\n"
    "s_mov_b32 m0, s78\n"
    "s_nop 0\n"
    "buffer_load_dwordx4 v236, s[48:51], s65 offen offset:0 nt lds\n"
    "v_accvgpr_write_b32 a30, 0\n"
    "v_accvgpr_write_b32 a31, 0\n"
    "v_mfma_f32_16x16x32_bf16 a[4:7], a[88:91], a[208:211], 0\n"
    "v_mfma_f32_16x16x32_bf16 a[4:7], a[92:95], a[212:215], a[4:7]\n"
    "v_mfma_f32_16x16x32_bf16 a[8:11], a[96:99], a[208:211], 0\n"
    "v_mfma_f32_16x16x32_bf16 a[8:11], a[100:103], a[212:215], a[8:11]\n"
    "v_mfma_f32_16x16x32_bf16 a[12:15], a[104:107], a[208:211], 0\n"
    "v_mfma_f32_16x16x32_bf16 a[12:15], a[108:111], a[212:215], a[12:15]\n"
    "s_mov_b32 s67, 7\n"
    "LD1_loop_%=:\n"
    "s_waitcnt vmcnt(14) lgkmcnt(0)\n"
    "s_barrier\n"
    "v_mfma_f32_16x16x32_bf16 v[112:115], a[112:115], a[144:147], v[112:115]\n"
    "ds_read_b128 a[80:83], v208 offset:32768\n"
    "v_mfma_f32_16x16x32_bf16 v[112:115], a[116:119], a[148:151], v[112:115]\n"
    "ds_read_b128 a[84:87], v209 offset:32768\n"
    "v_mfma_f32_16x16x32_bf16 v[136:139], a[120:123], a[144:147], v[136:139]\n"
    "ds_read_b128 a[88:91], v208 offset:34816\n"
    "s_mov_b32 m0, s72\n"
    "s_nop 0\n"
    "buffer_load_dwordx4 v220, s[36:39], s65 offen offset:0 sc1 lds\n"
    "v_mfma_f32_16x16x32_bf16 v[136:139], a[124:127], a[148:151], v[136:139]\n"
    "ds_read_b128 a[92:95], v209 offset:34816\n"
    "v_mfma_f32_16x16x32_bf16 v[160:163], a[128:131], a[144:147], v[160:163]\n"
    "ds_read_b128 a[96:99], v208 offset:36864\n"
    "v_mfma_f32_16x16x32_bf16 v[160:163], a[132:135], a[148:151], v[160:163]\n"
    "ds_read_b128 a[100:103], v209 offset:36864\n"
    "v_mfma_f32_16x16x32_bf16 v[184:187], a[136:139], a[144:147], v[184:187]\n"
    "ds_read_b128 a[104:107], v208 offset:38912\n"
    "v_mfma_f32_16x16x32_bf16 v[184:187], a[140:143], a[148:151], v[184:187]\n"
    "ds_read_b128 a[108:111], v209 offset:38912\n"
    "v_mfma_f32_16x16x32_bf16 v[116:119], a[112:115], a[152:155], v[116:119]\n"
    "ds_read_b128 a[144:147], v218 offset:36864\n"
    "v_mfma_f32_16x16x32_bf16 v[116:119], a[116:119], a[156:159], v[116:119]\n"
    "v_mfma_f32_16x16x32_bf16 v[140:143], a[120:123], a[152:155], v[140:143]\n"
    "buffer_load_dwordx4 v221, s[36:39], s65 offen offset:1024 sc1 lds\n"
    "v_mfma_f32_16x16x32_bf16 v[140:143], a[124:127], a[156:159], v[140:143]\n"
    "v_mfma_f32_16x16x32_bf16 v[164:167], a[128:131], a[152:155], v[164:167]\n"
    "ds_read_b128 a[148:151], v219 offset:36864\n"
    "v_mfma_f32_16x16x32_bf16 v[164:167], a[132:135], a[156:159], v[164:167]\n"
    "v_mfma_f32_16x16x32_bf16 v[188:191], a[136:139], a[152:155], v[188:191]\n"
    "v_mfma_f32_16x16x32_bf16 v[188:191], a[140:143], a[156:159], v[188:191]\n"
    "v_mfma_f32_16x16x32_bf16 v[120:123], a[112:115], a[160:163], v[120:123]\n"
    "ds_read_b128 a[152:155], v218 offset:38912\n"
    "v_mfma_f32_16x16x32_bf16 v[120:123], a[116:119], a[164:167], v[120:123]\n"
    "v_mfma_f32_16x16x32_bf16 v[144:147], a[120:123], a[160:163], v[144:147]\n"
    "v_mfma_f32_16x16x32_bf16 v[144:147], a[124:127], a[164:167], v[144:147]\n"
    "buffer_load_dwordx4 v222, s[36:39], s65 offen offset:2048 sc1 lds\n"
    "v_mfma_f32_16x16x32_bf16 v[168:171], a[128:131], a[160:163], v[168:171]\n"
    "ds_read_b128 a[156:159], v219 offset:38912\n"
    "v_mfma_f32_16x16x32_bf16 v[168:171], a[132:135], a[164:167], v[168:171]\n"
    "v_mfma_f32_16x16x32_bf16 v[192:195], a[136:139], a[160:163], v[192:195]\n"
    "v_mfma_f32_16x16x32_bf16 v[192:195], a[140:143], a[164:167], v[192:195]\n"
    "v_mfma_f32_16x16x32_bf16 v[124:127], a[112:115], a[168:171], v[124:127]\n"
    "ds_read_b128 a[160:163], v218 offset:40960\n"
    "v_mfma_f32_16x16x32_bf16 v[124:127], a[116:119], a[172:175], v[124:127]\n"
    "v_mfma_f32_16x16x32_bf16 v[148:151], a[120:123], a[168:171], v[148:151]\n"
    "v_mfma_f32_16x16x32_bf16 v[148:151], a[124:127], a[172:175], v[148:151]\n"
    "buffer_load_dwordx4 v223, s[36:39], s65 offen offset:3072 sc1 lds\n"
    "v_mfma_f32_16x16x32_bf16 v[172:175], a[128:131], a[168:171], v[172:175]\n"
    "ds_read_b128 a[164:167], v219 offset:40960\n"
    "v_mfma_f32_16x16x32_bf16 v[172:175], a[132:135], a[172:175], v[172:175]\n"
    "v_mfma_f32_16x16x32_bf16 v[196:199], a[136:139], a[168:171], v[196:199]\n"
    "v_mfma_f32_16x16x32_bf16 v[196:199], a[140:143], a[172:175], v[196:199]\n"
    "v_mfma_f32_16x16x32_bf16 v[128:131], a[112:115], a[176:179], v[128:131]\n"
    "ds_read_b128 a[168:171], v218 offset:43008\n"
    "v_mfma_f32_16x16x32_bf16 v[128:131], a[116:119], a[180:183], v[128:131]\n"
    "v_mfma_f32_16x16x32_bf16 v[152:155], a[120:123], a[176:179], v[152:155]\n"
    "v_mfma_f32_16x16x32_bf16 v[152:155], a[124:127], a[180:183], v[152:155]\n"
    "v_mfma_f32_16x16x32_bf16 v[176:179], a[128:131], a[176:179], v[176:179]\n"
    "ds_read_b128 a[172:175], v219 offset:43008\n"
    "s_mov_b32 m0, s73\n"
    "s_nop 0\n"
    "buffer_load_dwordx4 v224, s[36:39], s65 offen offset:0 sc1 lds\n"
    "v_mfma_f32_16x16x32_bf16 v[176:179], a[132:135], a[180:183], v[176:179]\n"
    "v_mfma_f32_16x16x32_bf16 v[200:203], a[136:139], a[176:179], v[200:203]\n"
    "v_mfma_f32_16x16x32_bf16 v[200:203], a[140:143], a[180:183], v[200:203]\n"
    "v_mfma_f32_16x16x32_bf16 v[132:135], a[112:115], a[184:187], v[132:135]\n"
    "ds_read_b128 a[176:179], v218 offset:45056\n"
    "v_mfma_f32_16x16x32_bf16 v[132:135], a[116:119], a[188:191], v[132:135]\n"
    "v_mfma_f32_16x16x32_bf16 v[156:159], a[120:123], a[184:187], v[156:159]\n"
    "v_mfma_f32_16x16x32_bf16 v[156:159], a[124:127], a[188:191], v[156:159]\n"
    "v_mfma_f32_16x16x32_bf16 v[180:183], a[128:131], a[184:187], v[180:183]\n"
    "ds_read_b128 a[180:183], v219 offset:45056\n"
    "buffer_load_dwordx4 v225, s[36:39], s65 offen offset:1024 sc1 lds\n"
    "v_mfma_f32_16x16x32_bf16 v[180:183], a[132:135], a[188:191], v[180:183]\n"
    "v_mfma_f32_16x16x32_bf16 v[204:207], a[136:139], a[184:187], v[204:207]\n"
    "v_mfma_f32_16x16x32_bf16 v[204:207], a[140:143], a[188:191], v[204:207]\n"
    "v_mfma_f32_16x16x32_bf16 a[64:67], a[112:115], a[192:195], a[64:67]\n"
    "ds_read_b128 a[184:187], v218 offset:47104\n"
    "v_mfma_f32_16x16x32_bf16 a[64:67], a[116:119], a[196:199], a[64:67]\n"
    "v_mfma_f32_16x16x32_bf16 a[68:71], a[120:123], a[192:195], a[68:71]\n"
    "v_mfma_f32_16x16x32_bf16 a[68:71], a[124:127], a[196:199], a[68:71]\n"
    "v_mfma_f32_16x16x32_bf16 a[72:75], a[128:131], a[192:195], a[72:75]\n"
    "ds_read_b128 a[188:191], v219 offset:47104\n"
    "v_mfma_f32_16x16x32_bf16 a[72:75], a[132:135], a[196:199], a[72:75]\n"
    "buffer_load_dwordx4 v226, s[36:39], s65 offen offset:2048 sc1 lds\n"
    "v_mfma_f32_16x16x32_bf16 a[76:79], a[136:139], a[192:195], a[76:79]\n"
    "v_mfma_f32_16x16x32_bf16 a[76:79], a[140:143], a[196:199], a[76:79]\n"
    "v_mfma_f32_16x16x32_bf16 a[48:51], a[112:115], a[200:203], a[48:51]\n"
    "ds_read_b128 a[192:195], v218 offset:49152\n"
    "v_mfma_f32_16x16x32_bf16 a[48:51], a[116:119], a[204:207], a[48:51]\n"
    "v_mfma_f32_16x16x32_bf16 a[52:55], a[120:123], a[200:203], a[52:55]\n"
    "v_mfma_f32_16x16x32_bf16 a[52:55], a[124:127], a[204:207], a[52:55]\n"
    "v_mfma_f32_16x16x32_bf16 a[56:59], a[128:131], a[200:203], a[56:59]\n"
    "ds_read_b128 a[196:199], v219 offset:49152\n"
    "v_mfma_f32_16x16x32_bf16 a[56:59], a[132:135], a[204:207], a[56:59]\n"
    "buffer_load_dwordx4 v227, s[36:39], s65 offen offset:3072 sc1 lds\n"
    "v_mfma_f32_16x16x32_bf16 a[60:63], a[136:139], a[200:203], a[60:63]\n"
    "v_mfma_f32_16x16x32_bf16 a[60:63], a[140:143], a[204:207], a[60:63]\n"
    "v_mfma_f32_16x16x32_bf16 a[16:19], a[112:115], a[208:211], a[16:19]\n"
    "ds_read_b128 a[200:203], v218 offset:51200\n"
    "v_mfma_f32_16x16x32_bf16 a[16:19], a[116:119], a[212:215], a[16:19]\n"
    "v_mfma_f32_16x16x32_bf16 a[20:23], a[120:123], a[208:211], a[20:23]\n"
    "v_mfma_f32_16x16x32_bf16 a[20:23], a[124:127], a[212:215], a[20:23]\n"
    "v_mfma_f32_16x16x32_bf16 a[24:27], a[128:131], a[208:211], a[24:27]\n"
    "ds_read_b128 a[204:207], v219 offset:51200\n"
    "v_mfma_f32_16x16x32_bf16 a[24:27], a[132:135], a[212:215], a[24:27]\n"
    "v_mfma_f32_16x16x32_bf16 a[28:31], a[136:139], a[208:211], a[28:31]\n"
    "s_add_u32 s65, s65, 128\n"
    "s_add_u32 s66, s66, 458752\n"
    "s_cmp_ge_u32 s65, 2048\n"
    "s_cselect_b32 s38, 0, s38\n"
    "s_cselect_b32 s42, 0, s42\n"
    "s_cselect_b32 s46, 0, s46\n"
    "s_cselect_b32 s50, 0, s50\n"
    "v_mfma_f32_16x16x32_bf16 a[28:31], a[140:143], a[212:215], a[28:31]\n"
    "s_waitcnt lgkmcnt(13)\n"
    "v_mfma_f32_16x16x32_bf16 v[0:3], a[80:83], a[144:147], v[0:3]\n"
    "ds_read_b128 a[208:211], v218 offset:53248\n"
    "v_mfma_f32_16x16x32_bf16 v[0:3], a[84:87], a[148:151], v[0:3]\n"
    "ds_read_b128 a[212:215], v219 offset:53248\n"
    "v_mfma_f32_16x16x32_bf16 v[28:31], a[88:91], a[144:147], v[28:31]\n"
    "v_mfma_f32_16x16x32_bf16 v[28:31], a[92:95], a[148:151], v[28:31]\n"
    "v_mfma_f32_16x16x32_bf16 v[56:59], a[96:99], a[144:147], v[56:59]\n"
    "v_mfma_f32_16x16x32_bf16 v[56:59], a[100:103], a[148:151], v[56:59]\n"
    "v_mfma_f32_16x16x32_bf16 v[84:87], a[104:107], a[144:147], v[84:87]\n"
    "v_mfma_f32_16x16x32_bf16 v[84:87], a[108:111], a[148:151], v[84:87]\n"
    "v_mfma_f32_16x16x32_bf16 v[4:7], a[80:83], a[152:155], v[4:7]\n"
    "s_waitcnt lgkmcnt(14)\n"
    "v_mfma_f32_16x16x32_bf16 v[4:7], a[84:87], a[156:159], v[4:7]\n"
    "v_mfma_f32_16x16x32_bf16 v[32:35], a[88:91], a[152:155], v[32:35]\n"
    "v_mfma_f32_16x16x32_bf16 v[32:35], a[92:95], a[156:159], v[32:35]\n"
    "s_waitcnt vmcnt(18) lgkmcnt(0)\n"
    "s_barrier\n"
    "buffer_load_dword v253, v254, s[12:15], 0 offen\n"
    "v_mfma_f32_16x16x32_bf16 v[60:63], a[96:99], a[152:155], v[60:63]\n"
    "ds_read_b128 a[112:115], v208 offset:40960\n"
    "v_mfma_f32_16x16x32_bf16 v[60:63], a[100:103], a[156:159], v[60:63]\n"
    "s_mov_b32 m0, s79\n"
    "s_nop 0\n"
    "buffer_load_dwordx4 v228, s[48:51], s65 offen offset:0 nt lds\n"
    "v_mfma_f32_16x16x32_bf16 v[88:91], a[104:107], a[152:155], v[88:91]\n"
    "ds_read_b128 a[116:119], v209 offset:40960\n"
    "v_mfma_f32_16x16x32_bf16 v[88:91], a[108:111], a[156:159], v[88:91]\n"
    "v_mfma_f32_16x16x32_bf16 v[8:11], a[80:83], a[160:163], v[8:11]\n"
    "ds_read_b128 a[120:123], v208 offset:43008\n"
    "v_mfma_f32_16x16x32_bf16 v[8:11], a[84:87], a[164:167], v[8:11]\n"
    "v_mfma_f32_16x16x32_bf16 v[36:39], a[88:91], a[160:163], v[36:39]\n"
    "ds_read_b128 a[124:127], v209 offset:43008\n"
    "v_mfma_f32_16x16x32_bf16 v[36:39], a[92:95], a[164:167], v[36:39]\n"
    "buffer_load_dwordx4 v229, s[48:51], s65 offen offset:1024 nt lds\n"
    "v_mfma_f32_16x16x32_bf16 v[64:67], a[96:99], a[160:163], v[64:67]\n"
    "ds_read_b128 a[128:131], v208 offset:45056\n"
    "v_mfma_f32_16x16x32_bf16 v[64:67], a[100:103], a[164:167], v[64:67]\n"
    "v_mfma_f32_16x16x32_bf16 v[92:95], a[104:107], a[160:163], v[92:95]\n"
    "ds_read_b128 a[132:135], v209 offset:45056\n"
    "v_mfma_f32_16x16x32_bf16 v[92:95], a[108:111], a[164:167], v[92:95]\n"
    "v_mfma_f32_16x16x32_bf16 v[12:15], a[80:83], a[168:171], v[12:15]\n"
    "ds_read_b128 a[136:139], v208 offset:47104\n"
    "v_mfma_f32_16x16x32_bf16 v[12:15], a[84:87], a[172:175], v[12:15]\n"
    "v_mfma_f32_16x16x32_bf16 v[40:43], a[88:91], a[168:171], v[40:43]\n"
    "ds_read_b128 a[140:143], v209 offset:47104\n"
    "buffer_load_dwordx4 v230, s[48:51], s65 offen offset:2048 nt lds\n"
    "v_mfma_f32_16x16x32_bf16 v[40:43], a[92:95], a[172:175], v[40:43]\n"
    "v_mfma_f32_16x16x32_bf16 v[68:71], a[96:99], a[168:171], v[68:71]\n"
    "v_mfma_f32_16x16x32_bf16 v[68:71], a[100:103], a[172:175], v[68:71]\n"
    "v_mfma_f32_16x16x32_bf16 v[96:99], a[104:107], a[168:171], v[96:99]\n"
    "v_mfma_f32_16x16x32_bf16 v[96:99], a[108:111], a[172:175], v[96:99]\n"
    "v_mfma_f32_16x16x32_bf16 v[16:19], a[80:83], a[176:179], v[16:19]\n"
    "buffer_load_dwordx4 v231, s[48:51], s65 offen offset:3072 nt lds\n"
    "v_mfma_f32_16x16x32_bf16 v[16:19], a[84:87], a[180:183], v[16:19]\n"
    "v_mfma_f32_16x16x32_bf16 v[44:47], a[88:91], a[176:179], v[44:47]\n"
    "v_mfma_f32_16x16x32_bf16 v[44:47], a[92:95], a[180:183], v[44:47]\n"
    "v_mfma_f32_16x16x32_bf16 v[72:75], a[96:99], a[176:179], v[72:75]\n"
    "v_mfma_f32_16x16x32_bf16 v[72:75], a[100:103], a[180:183], v[72:75]\n"
    "v_mfma_f32_16x16x32_bf16 v[100:103], a[104:107], a[176:179], v[100:103]\n"
    "v_mfma_f32_16x16x32_bf16 v[100:103], a[108:111], a[180:183], v[100:103]\n"
    "s_mov_b32 m0, s80\n"
    "s_nop 0\n"
    "buffer_load_dwordx4 v232, s[48:51], s65 offen offset:0 nt lds\n"
    "v_mfma_f32_16x16x32_bf16 v[20:23], a[80:83], a[184:187], v[20:23]\n"
    "s_add_u32 s9, s9, 1\n"
    "s_cmp_eq_u32 s9, 0\n"
    "s_cselect_b32 s14, 8388608, 0\n"
    "v_mfma_f32_16x16x32_bf16 v[20:23], a[84:87], a[188:191], v[20:23]\n"
    "v_mfma_f32_16x16x32_bf16 v[48:51], a[88:91], a[184:187], v[48:51]\n"
    "v_mfma_f32_16x16x32_bf16 v[48:51], a[92:95], a[188:191], v[48:51]\n"
    "v_mfma_f32_16x16x32_bf16 v[76:79], a[96:99], a[184:187], v[76:79]\n"
    "v_mfma_f32_16x16x32_bf16 v[76:79], a[100:103], a[188:191], v[76:79]\n"
    "buffer_load_dwordx4 v233, s[48:51], s65 offen offset:1024 nt lds\n"
    "v_mfma_f32_16x16x32_bf16 v[104:107], a[104:107], a[184:187], v[104:107]\n"
    "v_mfma_f32_16x16x32_bf16 v[104:107], a[108:111], a[188:191], v[104:107]\n"
    "v_mfma_f32_16x16x32_bf16 v[24:27], a[80:83], a[192:195], v[24:27]\n"
    "v_mfma_f32_16x16x32_bf16 v[24:27], a[84:87], a[196:199], v[24:27]\n"
    "v_mfma_f32_16x16x32_bf16 v[52:55], a[88:91], a[192:195], v[52:55]\n"
    "v_mfma_f32_16x16x32_bf16 v[52:55], a[92:95], a[196:199], v[52:55]\n"
    "v_mfma_f32_16x16x32_bf16 v[80:83], a[96:99], a[192:195], v[80:83]\n"
    "buffer_load_dwordx4 v234, s[48:51], s65 offen offset:2048 nt lds\n"
    "v_mfma_f32_16x16x32_bf16 v[80:83], a[100:103], a[196:199], v[80:83]\n"
    "v_mfma_f32_16x16x32_bf16 v[108:111], a[104:107], a[192:195], v[108:111]\n"
    "v_mfma_f32_16x16x32_bf16 v[108:111], a[108:111], a[196:199], v[108:111]\n"
    "v_mfma_f32_16x16x32_bf16 a[32:35], a[80:83], a[200:203], a[32:35]\n"
    "v_mfma_f32_16x16x32_bf16 a[32:35], a[84:87], a[204:207], a[32:35]\n"
    "v_mfma_f32_16x16x32_bf16 a[36:39], a[88:91], a[200:203], a[36:39]\n"
    "buffer_load_dwordx4 v235, s[48:51], s65 offen offset:3072 nt lds\n"
    "v_mfma_f32_16x16x32_bf16 a[36:39], a[92:95], a[204:207], a[36:39]\n"
    "v_mfma_f32_16x16x32_bf16 a[40:43], a[96:99], a[200:203], a[40:43]\n"
    "v_mfma_f32_16x16x32_bf16 a[40:43], a[100:103], a[204:207], a[40:43]\n"
    "v_mfma_f32_16x16x32_bf16 a[44:47], a[104:107], a[200:203], a[44:47]\n"
    "v_mfma_f32_16x16x32_bf16 a[44:47], a[108:111], a[204:207], a[44:47]\n"
    "v_mfma_f32_16x16x32_bf16 a[0:3], a[80:83], a[208:211], a[0:3]\n"
    "v_mfma_f32_16x16x32_bf16 a[0:3], a[84:87], a[212:215], a[0:3]\n"
    "s_mov_b32 m0, s81\n"
    "s_nop 0\n"
    "buffer_load_dwordx4 v236, s[48:51], s65 offen offset:0 nt lds\n"
    "v_mfma_f32_16x16x32_bf16 a[4:7], a[88:91], a[208:211], a[4:7]\n"
    "v_mfma_f32_16x16x32_bf16 a[4:7], a[92:95], a[212:215], a[4:7]\n"
    "v_mfma_f32_16x16x32_bf16 a[8:11], a[96:99], a[208:211], a[8:11]\n"
    "v_mfma_f32_16x16x32_bf16 a[8:11], a[100:103], a[212:215], a[8:11]\n"
    "v_mfma_f32_16x16x32_bf16 a[12:15], a[104:107], a[208:211], a[12:15]\n"
    "v_mfma_f32_16x16x32_bf16 a[12:15], a[108:111], a[212:215], a[12:15]\n"
    "s_waitcnt vmcnt(14) lgkmcnt(0)\n"
    "s_barrier\n"
    "v_mfma_f32_16x16x32_bf16 v[112:115], a[112:115], a[144:147], v[112:115]\n"
    "ds_read_b128 a[80:83], v208 offset:0\n"
    "v_mfma_f32_16x16x32_bf16 v[112:115], a[116:119], a[148:151], v[112:115]\n"
    "ds_read_b128 a[84:87], v209 offset:0\n"
    "v_mfma_f32_16x16x32_bf16 v[136:139], a[120:123], a[144:147], v[136:139]\n"
    "ds_read_b128 a[88:91], v208 offset:2048\n"
    "s_mov_b32 m0, s74\n"
    "s_nop 0\n"
    "buffer_load_dwordx4 v220, s[36:39], s65 offen offset:0 sc1 lds\n"
    "v_mfma_f32_16x16x32_bf16 v[136:139], a[124:127], a[148:151], v[136:139]\n"
    "ds_read_b128 a[92:95], v209 offset:2048\n"
    "v_mfma_f32_16x16x32_bf16 v[160:163], a[128:131], a[144:147], v[160:163]\n"
    "ds_read_b128 a[96:99], v208 offset:4096\n"
    "v_mfma_f32_16x16x32_bf16 v[160:163], a[132:135], a[148:151], v[160:163]\n"
    "ds_read_b128 a[100:103], v209 offset:4096\n"
    "v_mfma_f32_16x16x32_bf16 v[184:187], a[136:139], a[144:147], v[184:187]\n"
    "ds_read_b128 a[104:107], v208 offset:6144\n"
    "v_mfma_f32_16x16x32_bf16 v[184:187], a[140:143], a[148:151], v[184:187]\n"
    "ds_read_b128 a[108:111], v209 offset:6144\n"
    "v_mfma_f32_16x16x32_bf16 v[116:119], a[112:115], a[152:155], v[116:119]\n"
    "ds_read_b128 a[144:147], v218 offset:0\n"
    "v_mfma_f32_16x16x32_bf16 v[116:119], a[116:119], a[156:159], v[116:119]\n"
    "v_mfma_f32_16x16x32_bf16 v[140:143], a[120:123], a[152:155], v[140:143]\n"
    "buffer_load_dwordx4 v221, s[36:39], s65 offen offset:1024 sc1 lds\n"
    "v_mfma_f32_16x16x32_bf16 v[140:143], a[124:127], a[156:159], v[140:143]\n"
    "v_mfma_f32_16x16x32_bf16 v[164:167], a[128:131], a[152:155], v[164:167]\n"
    "ds_read_b128 a[148:151], v219 offset:0\n"
    "v_mfma_f32_16x16x32_bf16 v[164:167], a[132:135], a[156:159], v[164:167]\n"
    "v_mfma_f32_16x16x32_bf16 v[188:191], a[136:139], a[152:155], v[188:191]\n"
    "v_mfma_f32_16x16x32_bf16 v[188:191], a[140:143], a[156:159], v[188:191]\n"
    "v_mfma_f32_16x16x32_bf16 v[120:123], a[112:115], a[160:163], v[120:123]\n"
    "ds_read_b128 a[152:155], v218 offset:2048\n"
    "v_mfma_f32_16x16x32_bf16 v[120:123], a[116:119], a[164:167], v[120:123]\n"
    "v_mfma_f32_16x16x32_bf16 v[144:147], a[120:123], a[160:163], v[144:147]\n"
    "v_mfma_f32_16x16x32_bf16 v[144:147], a[124:127], a[164:167], v[144:147]\n"
    "buffer_load_dwordx4 v222, s[36:39], s65 offen offset:2048 sc1 lds\n"
    "v_mfma_f32_16x16x32_bf16 v[168:171], a[128:131], a[160:163], v[168:171]\n"
    "ds_read_b128 a[156:159], v219 offset:2048\n"
    "v_mfma_f32_16x16x32_bf16 v[168:171], a[132:135], a[164:167], v[168:171]\n"
    "v_mfma_f32_16x16x32_bf16 v[192:195], a[136:139], a[160:163], v[192:195]\n"
    "v_mfma_f32_16x16x32_bf16 v[192:195], a[140:143], a[164:167], v[192:195]\n"
    "v_mfma_f32_16x16x32_bf16 v[124:127], a[112:115], a[168:171], v[124:127]\n"
    "ds_read_b128 a[160:163], v218 offset:4096\n"
    "v_mfma_f32_16x16x32_bf16 v[124:127], a[116:119], a[172:175], v[124:127]\n"
    "v_mfma_f32_16x16x32_bf16 v[148:151], a[120:123], a[168:171], v[148:151]\n"
    "v_mfma_f32_16x16x32_bf16 v[148:151], a[124:127], a[172:175], v[148:151]\n"
    "buffer_load_dwordx4 v223, s[36:39], s65 offen offset:3072 sc1 lds\n"
    "v_mfma_f32_16x16x32_bf16 v[172:175], a[128:131], a[168:171], v[172:175]\n"
    "ds_read_b128 a[164:167], v219 offset:4096\n"
    "v_mfma_f32_16x16x32_bf16 v[172:175], a[132:135], a[172:175], v[172:175]\n"
    "v_mfma_f32_16x16x32_bf16 v[196:199], a[136:139], a[168:171], v[196:199]\n"
    "v_mfma_f32_16x16x32_bf16 v[196:199], a[140:143], a[172:175], v[196:199]\n"
    "v_mfma_f32_16x16x32_bf16 v[128:131], a[112:115], a[176:179], v[128:131]\n"
    "ds_read_b128 a[168:171], v218 offset:6144\n"
    "v_mfma_f32_16x16x32_bf16 v[128:131], a[116:119], a[180:183], v[128:131]\n"
    "v_mfma_f32_16x16x32_bf16 v[152:155], a[120:123], a[176:179], v[152:155]\n"
    "v_mfma_f32_16x16x32_bf16 v[152:155], a[124:127], a[180:183], v[152:155]\n"
    "v_mfma_f32_16x16x32_bf16 v[176:179], a[128:131], a[176:179], v[176:179]\n"
    "ds_read_b128 a[172:175], v219 offset:6144\n"
    "s_mov_b32 m0, s75\n"
    "s_nop 0\n"
    "buffer_load_dwordx4 v224, s[36:39], s65 offen offset:0 sc1 lds\n"
    "v_mfma_f32_16x16x32_bf16 v[176:179], a[132:135], a[180:183], v[176:179]\n"
    "v_mfma_f32_16x16x32_bf16 v[200:203], a[136:139], a[176:179], v[200:203]\n"
    "v_mfma_f32_16x16x32_bf16 v[200:203], a[140:143], a[180:183], v[200:203]\n"
    "v_mfma_f32_16x16x32_bf16 v[132:135], a[112:115], a[184:187], v[132:135]\n"
    "ds_read_b128 a[176:179], v218 offset:8192\n"
    "v_mfma_f32_16x16x32_bf16 v[132:135], a[116:119], a[188:191], v[132:135]\n"
    "v_mfma_f32_16x16x32_bf16 v[156:159], a[120:123], a[184:187], v[156:159]\n"
    "v_mfma_f32_16x16x32_bf16 v[156:159], a[124:127], a[188:191], v[156:159]\n"
    "v_mfma_f32_16x16x32_bf16 v[180:183], a[128:131], a[184:187], v[180:183]\n"
    "ds_read_b128 a[180:183], v219 offset:8192\n"
    "buffer_load_dwordx4 v225, s[36:39], s65 offen offset:1024 sc1 lds\n"
    "v_mfma_f32_16x16x32_bf16 v[180:183], a[132:135], a[188:191], v[180:183]\n"
    "v_mfma_f32_16x16x32_bf16 v[204:207], a[136:139], a[184:187], v[204:207]\n"
    "v_mfma_f32_16x16x32_bf16 v[204:207], a[140:143], a[188:191], v[204:207]\n"
    "v_mfma_f32_16x16x32_bf16 a[64:67], a[112:115], a[192:195], a[64:67]\n"
    "ds_read_b128 a[184:187], v218 offset:10240\n"
    "v_mfma_f32_16x16x32_bf16 a[64:67], a[116:119], a[196:199], a[64:67]\n"
    "v_mfma_f32_16x16x32_bf16 a[68:71], a[120:123], a[192:195], a[68:71]\n"
    "v_mfma_f32_16x16x32_bf16 a[68:71], a[124:127], a[196:199], a[68:71]\n"
    "v_mfma_f32_16x16x32_bf16 a[72:75], a[128:131], a[192:195], a[72:75]\n"
    "ds_read_b128 a[188:191], v219 offset:10240\n"
    "v_mfma_f32_16x16x32_bf16 a[72:75], a[132:135], a[196:199], a[72:75]\n"
    "buffer_load_dwordx4 v226, s[36:39], s65 offen offset:2048 sc1 lds\n"
    "v_mfma_f32_16x16x32_bf16 a[76:79], a[136:139], a[192:195], a[76:79]\n"
    "v_mfma_f32_16x16x32_bf16 a[76:79], a[140:143], a[196:199], a[76:79]\n"
    "v_mfma_f32_16x16x32_bf16 a[48:51], a[112:115], a[200:203], a[48:51]\n"
    "ds_read_b128 a[192:195], v218 offset:12288\n"
    "v_mfma_f32_16x16x32_bf16 a[48:51], a[116:119], a[204:207], a[48:51]\n"
    "v_mfma_f32_16x16x32_bf16 a[52:55], a[120:123], a[200:203], a[52:55]\n"
    "v_mfma_f32_16x16x32_bf16 a[52:55], a[124:127], a[204:207], a[52:55]\n"
    "v_mfma_f32_16x16x32_bf16 a[56:59], a[128:131], a[200:203], a[56:59]\n"
    "ds_read_b128 a[196:199], v219 offset:12288\n"
    "v_mfma_f32_16x16x32_bf16 a[56:59], a[132:135], a[204:207], a[56:59]\n"
    "buffer_load_dwordx4 v227, s[36:39], s65 offen offset:3072 sc1 lds\n"
    "v_mfma_f32_16x16x32_bf16 a[60:63], a[136:139], a[200:203], a[60:63]\n"
    "v_mfma_f32_16x16x32_bf16 a[60:63], a[140:143], a[204:207], a[60:63]\n"
    "v_mfma_f32_16x16x32_bf16 a[16:19], a[112:115], a[208:211], a[16:19]\n"
    "ds_read_b128 a[200:203], v218 offset:14336\n"
    "v_mfma_f32_16x16x32_bf16 a[16:19], a[116:119], a[212:215], a[16:19]\n"
    "v_mfma_f32_16x16x32_bf16 a[20:23], a[120:123], a[208:211], a[20:23]\n"
    "v_mfma_f32_16x16x32_bf16 a[20:23], a[124:127], a[212:215], a[20:23]\n"
    "v_mfma_f32_16x16x32_bf16 a[24:27], a[128:131], a[208:211], a[24:27]\n"
    "ds_read_b128 a[204:207], v219 offset:14336\n"
    "v_mfma_f32_16x16x32_bf16 a[24:27], a[132:135], a[212:215], a[24:27]\n"
    "v_mfma_f32_16x16x32_bf16 a[28:31], a[136:139], a[208:211], a[28:31]\n"
    "s_add_u32 s65, s65, 128\n"
    "s_add_u32 s66, s66, 458752\n"
    "s_cmp_ge_u32 s65, 2048\n"
    "s_cselect_b32 s38, 0, s38\n"
    "s_cselect_b32 s42, 0, s42\n"
    "s_cselect_b32 s46, 0, s46\n"
    "s_cselect_b32 s50, 0, s50\n"
    "v_mfma_f32_16x16x32_bf16 a[28:31], a[140:143], a[212:215], a[28:31]\n"
    "s_sub_u32 s67, s67, 1\n"
    "s_cmp_eq_u32 s67, 0\n"
    "s_cbranch_scc1 LD1_exit_%=\n"
    "s_waitcnt lgkmcnt(13)\n"
    "v_mfma_f32_16x16x32_bf16 v[0:3], a[80:83], a[144:147], v[0:3]\n"
    "ds_read_b128 a[208:211], v218 offset:16384\n"
    "v_mfma_f32_16x16x32_bf16 v[0:3], a[84:87], a[148:151], v[0:3]\n"
    "ds_read_b128 a[212:215], v219 offset:16384\n"
    "v_mfma_f32_16x16x32_bf16 v[28:31], a[88:91], a[144:147], v[28:31]\n"
    "v_mfma_f32_16x16x32_bf16 v[28:31], a[92:95], a[148:151], v[28:31]\n"
    "v_mfma_f32_16x16x32_bf16 v[56:59], a[96:99], a[144:147], v[56:59]\n"
    "v_mfma_f32_16x16x32_bf16 v[56:59], a[100:103], a[148:151], v[56:59]\n"
    "v_mfma_f32_16x16x32_bf16 v[84:87], a[104:107], a[144:147], v[84:87]\n"
    "v_mfma_f32_16x16x32_bf16 v[84:87], a[108:111], a[148:151], v[84:87]\n"
    "v_mfma_f32_16x16x32_bf16 v[4:7], a[80:83], a[152:155], v[4:7]\n"
    "s_waitcnt lgkmcnt(14)\n"
    "v_mfma_f32_16x16x32_bf16 v[4:7], a[84:87], a[156:159], v[4:7]\n"
    "v_mfma_f32_16x16x32_bf16 v[32:35], a[88:91], a[152:155], v[32:35]\n"
    "v_mfma_f32_16x16x32_bf16 v[32:35], a[92:95], a[156:159], v[32:35]\n"
    "s_waitcnt vmcnt(18) lgkmcnt(0)\n"
    "s_barrier\n"
    "buffer_load_dword v253, v254, s[12:15], 0 offen\n"
    "v_mfma_f32_16x16x32_bf16 v[60:63], a[96:99], a[152:155], v[60:63]\n"
    "ds_read_b128 a[112:115], v208 offset:8192\n"
    "v_mfma_f32_16x16x32_bf16 v[60:63], a[100:103], a[156:159], v[60:63]\n"
    "s_mov_b32 m0, s76\n"
    "s_nop 0\n"
    "buffer_load_dwordx4 v228, s[48:51], s65 offen offset:0 nt lds\n"
    "v_mfma_f32_16x16x32_bf16 v[88:91], a[104:107], a[152:155], v[88:91]\n"
    "ds_read_b128 a[116:119], v209 offset:8192\n"
    "v_mfma_f32_16x16x32_bf16 v[88:91], a[108:111], a[156:159], v[88:91]\n"
    "v_mfma_f32_16x16x32_bf16 v[8:11], a[80:83], a[160:163], v[8:11]\n"
    "ds_read_b128 a[120:123], v208 offset:10240\n"
    "v_mfma_f32_16x16x32_bf16 v[8:11], a[84:87], a[164:167], v[8:11]\n"
    "v_mfma_f32_16x16x32_bf16 v[36:39], a[88:91], a[160:163], v[36:39]\n"
    "ds_read_b128 a[124:127], v209 offset:10240\n"
    "v_mfma_f32_16x16x32_bf16 v[36:39], a[92:95], a[164:167], v[36:39]\n"
    "buffer_load_dwordx4 v229, s[48:51], s65 offen offset:1024 nt lds\n"
    "v_mfma_f32_16x16x32_bf16 v[64:67], a[96:99], a[160:163], v[64:67]\n"
    "ds_read_b128 a[128:131], v208 offset:12288\n"
    "v_mfma_f32_16x16x32_bf16 v[64:67], a[100:103], a[164:167], v[64:67]\n"
    "v_mfma_f32_16x16x32_bf16 v[92:95], a[104:107], a[160:163], v[92:95]\n"
    "ds_read_b128 a[132:135], v209 offset:12288\n"
    "v_mfma_f32_16x16x32_bf16 v[92:95], a[108:111], a[164:167], v[92:95]\n"
    "v_mfma_f32_16x16x32_bf16 v[12:15], a[80:83], a[168:171], v[12:15]\n"
    "ds_read_b128 a[136:139], v208 offset:14336\n"
    "v_mfma_f32_16x16x32_bf16 v[12:15], a[84:87], a[172:175], v[12:15]\n"
    "v_mfma_f32_16x16x32_bf16 v[40:43], a[88:91], a[168:171], v[40:43]\n"
    "ds_read_b128 a[140:143], v209 offset:14336\n"
    "buffer_load_dwordx4 v230, s[48:51], s65 offen offset:2048 nt lds\n"
    "v_mfma_f32_16x16x32_bf16 v[40:43], a[92:95], a[172:175], v[40:43]\n"
    "v_mfma_f32_16x16x32_bf16 v[68:71], a[96:99], a[168:171], v[68:71]\n"
    "v_mfma_f32_16x16x32_bf16 v[68:71], a[100:103], a[172:175], v[68:71]\n"
    "v_mfma_f32_16x16x32_bf16 v[96:99], a[104:107], a[168:171], v[96:99]\n"
    "v_mfma_f32_16x16x32_bf16 v[96:99], a[108:111], a[172:175], v[96:99]\n"
    "v_mfma_f32_16x16x32_bf16 v[16:19], a[80:83], a[176:179], v[16:19]\n"
    "buffer_load_dwordx4 v231, s[48:51], s65 offen offset:3072 nt lds\n"
    "v_mfma_f32_16x16x32_bf16 v[16:19], a[84:87], a[180:183], v[16:19]\n"
    "v_mfma_f32_16x16x32_bf16 v[44:47], a[88:91], a[176:179], v[44:47]\n"
    "v_mfma_f32_16x16x32_bf16 v[44:47], a[92:95], a[180:183], v[44:47]\n"
    "v_mfma_f32_16x16x32_bf16 v[72:75], a[96:99], a[176:179], v[72:75]\n"
    "v_mfma_f32_16x16x32_bf16 v[72:75], a[100:103], a[180:183], v[72:75]\n"
    "v_mfma_f32_16x16x32_bf16 v[100:103], a[104:107], a[176:179], v[100:103]\n"
    "v_mfma_f32_16x16x32_bf16 v[100:103], a[108:111], a[180:183], v[100:103]\n"
    "s_mov_b32 m0, s77\n"
    "s_nop 0\n"
    "buffer_load_dwordx4 v232, s[48:51], s65 offen offset:0 nt lds\n"
    "v_mfma_f32_16x16x32_bf16 v[20:23], a[80:83], a[184:187], v[20:23]\n"
    "s_add_u32 s9, s9, 1\n"
    "s_cmp_eq_u32 s9, 0\n"
    "s_cselect_b32 s14, 8388608, 0\n"
    "v_mfma_f32_16x16x32_bf16 v[20:23], a[84:87], a[188:191], v[20:23]\n"
    "v_mfma_f32_16x16x32_bf16 v[48:51], a[88:91], a[184:187], v[48:51]\n"
    "v_mfma_f32_16x16x32_bf16 v[48:51], a[92:95], a[188:191], v[48:51]\n"
    "v_mfma_f32_16x16x32_bf16 v[76:79], a[96:99], a[184:187], v[76:79]\n"
    "v_mfma_f32_16x16x32_bf16 v[76:79], a[100:103], a[188:191], v[76:79]\n"
    "buffer_load_dwordx4 v233, s[48:51], s65 offen offset:1024 nt lds\n"
    "v_mfma_f32_16x16x32_bf16 v[104:107], a[104:107], a[184:187], v[104:107]\n"
    "v_mfma_f32_16x16x32_bf16 v[104:107], a[108:111], a[188:191], v[104:107]\n"
    "v_mfma_f32_16x16x32_bf16 v[24:27], a[80:83], a[192:195], v[24:27]\n"
    "v_mfma_f32_16x16x32_bf16 v[24:27], a[84:87], a[196:199], v[24:27]\n"
    "v_mfma_f32_16x16x32_bf16 v[52:55], a[88:91], a[192:195], v[52:55]\n"
    "v_mfma_f32_16x16x32_bf16 v[52:55], a[92:95], a[196:199], v[52:55]\n"
    "v_mfma_f32_16x16x32_bf16 v[80:83], a[96:99], a[192:195], v[80:83]\n"
    "buffer_load_dwordx4 v234, s[48:51], s65 offen offset:2048 nt lds\n"
    "v_mfma_f32_16x16x32_bf16 v[80:83], a[100:103], a[196:199], v[80:83]\n"
    "v_mfma_f32_16x16x32_bf16 v[108:111], a[104:107], a[192:195], v[108:111]\n"
    "v_mfma_f32_16x16x32_bf16 v[108:111], a[108:111], a[196:199], v[108:111]\n"
    "v_mfma_f32_16x16x32_bf16 a[32:35], a[80:83], a[200:203], a[32:35]\n"
    "v_mfma_f32_16x16x32_bf16 a[32:35], a[84:87], a[204:207], a[32:35]\n"
    "v_mfma_f32_16x16x32_bf16 a[36:39], a[88:91], a[200:203], a[36:39]\n"
    "buffer_load_dwordx4 v235, s[48:51], s65 offen offset:3072 nt lds\n"
    "v_mfma_f32_16x16x32_bf16 a[36:39], a[92:95], a[204:207], a[36:39]\n"
    "v_mfma_f32_16x16x32_bf16 a[40:43], a[96:99], a[200:203], a[40:43]\n"
    "v_mfma_f32_16x16x32_bf16 a[40:43], a[100:103], a[204:207], a[40:43]\n"
    "v_mfma_f32_16x16x32_bf16 a[44:47], a[104:107], a[200:203], a[44:47]\n"
    "v_mfma_f32_16x16x32_bf16 a[44:47], a[108:111], a[204:207], a[44:47]\n"
    "v_mfma_f32_16x16x32_bf16 a[0:3], a[80:83], a[208:211], a[0:3]\n"
    "v_mfma_f32_16x16x32_bf16 a[0:3], a[84:87], a[212:215], a[0:3]\n"
    "s_mov_b32 m0, s78\n"
    "s_nop 0\n"
    "buffer_load_dwordx4 v236, s[48:51], s65 offen offset:0 nt lds\n"
    "v_mfma_f32_16x16x32_bf16 a[4:7], a[88:91], a[208:211], a[4:7]\n"
    "v_mfma_f32_16x16x32_bf16 a[4:7], a[92:95], a[212:215], a[4:7]\n"
    "v_mfma_f32_16x16x32_bf16 a[8:11], a[96:99], a[208:211], a[8:11]\n"
    "v_mfma_f32_16x16x32_bf16 a[8:11], a[100:103], a[212:215], a[8:11]\n"
    "v_mfma_f32_16x16x32_bf16 a[12:15], a[104:107], a[208:211], a[12:15]\n"
    "v_mfma_f32_16x16x32_bf16 a[12:15], a[108:111], a[212:215], a[12:15]\n"
    "s_branch LD1_loop_%=\n"
    "LD1_exit_%=:\n"
    "s_waitcnt lgkmcnt(14)\n"
    "v_mfma_f32_16x16x32_bf16 v[0:3], a[80:83], a[144:147], v[0:3]\n"
    "ds_read_b128 a[208:211], v218 offset:16384\n"
    "v_mfma_f32_16x16x32_bf16 v[0:3], a[84:87], a[148:151], v[0:3]\n"
    "ds_read_b128 a[212:215], v219 offset:16384\n"
    "v_mfma_f32_16x16x32_bf16 v[28:31], a[88:91], a[144:147], v[28:31]\n"
    "v_mfma_f32_16x16x32_bf16 v[28:31], a[92:95], a[148:151], v[28:31]\n"
    "v_mfma_f32_16x16x32_bf16 v[56:59], a[96:99], a[144:147], v[56:59]\n"
    "v_mfma_f32_16x16x32_bf16 v[56:59], a[100:103], a[148:151], v[56:59]\n"
    "v_mfma_f32_16x16x32_bf16 v[84:87], a[104:107], a[144:147], v[84:87]\n"
    "s_waitcnt vmcnt(8)\n"
    "s_barrier\n"
    "v_mfma_f32_16x16x32_bf16 v[84:87], a[108:111], a[148:151], v[84:87]\n"
    "v_mov_b32 v254, v237\n"
    "s_cmp_eq_u32 s60, 2\n"
    "s_cselect_b32 s12, s20, s96\n"
    "s_cselect_b32 s13, s21, s97\n"
    "s_mov_b32 s14, 8388608\n"
    "s_cselect_b32 s14, 7340032, s14\n"
    "s_cmp_eq_u32 s60, 3\n"
    "s_cselect_b32 s14, 0, s14\n"
    "buffer_load_dword v253, v254, s[12:15], 0 offen\n"
    "s_waitcnt lgkmcnt(13)\n"
    "v_mfma_f32_16x16x32_bf16 v[4:7], a[80:83], a[152:155], v[4:7]\n"
    "ds_read_b128 a[144:147], v218 offset:36864\n"
    "v_mfma_f32_16x16x32_bf16 v[4:7], a[84:87], a[156:159], v[4:7]\n"
    "v_mfma_f32_16x16x32_bf16 v[32:35], a[88:91], a[152:155], v[32:35]\n"
    "v_mfma_f32_16x16x32_bf16 v[32:35], a[92:95], a[156:159], v[32:35]\n"
    "v_mfma_f32_16x16x32_bf16 v[60:63], a[96:99], a[152:155], v[60:63]\n"
    "ds_read_b128 a[148:151], v219 offset:36864\n"
    "v_mfma_f32_16x16x32_bf16 v[60:63], a[100:103], a[156:159], v[60:63]\n"
    "v_mfma_f32_16x16x32_bf16 v[88:91], a[104:107], a[152:155], v[88:91]\n"
    "v_mfma_f32_16x16x32_bf16 v[88:91], a[108:111], a[156:159], v[88:91]\n"
    "v_mfma_f32_16x16x32_bf16 v[8:11], a[80:83], a[160:163], v[8:11]\n"
    "ds_read_b128 a[152:155], v218 offset:38912\n"
    "s_waitcnt lgkmcnt(13)\n"
    "v_mfma_f32_16x16x32_bf16 v[8:11], a[84:87], a[164:167], v[8:11]\n"
    "v_mfma_f32_16x16x32_bf16 v[36:39], a[88:91], a[160:163], v[36:39]\n"
    "v_mfma_f32_16x16x32_bf16 v[36:39], a[92:95], a[164:167], v[36:39]\n"
    "v_mfma_f32_16x16x32_bf16 v[64:67], a[96:99], a[160:163], v[64:67]\n"
    "ds_read_b128 a[156:159], v219 offset:38912\n"
    "v_mfma_f32_16x16x32_bf16 v[64:67], a[100:103], a[164:167], v[64:67]\n"
    "v_mfma_f32_16x16x32_bf16 v[92:95], a[104:107], a[160:163], v[92:95]\n"
    "v_mfma_f32_16x16x32_bf16 v[92:95], a[108:111], a[164:167], v[92:95]\n"
    "v_mfma_f32_16x16x32_bf16 v[12:15], a[80:83], a[168:171], v[12:15]\n"
    "ds_read_b128 a[160:163], v218 offset:40960\n"
    "v_mfma_f32_16x16x32_bf16 v[12:15], a[84:87], a[172:175], v[12:15]\n"
    "v_mfma_f32_16x16x32_bf16 v[40:43], a[88:91], a[168:171], v[40:43]\n"
    "v_mfma_f32_16x16x32_bf16 v[40:43], a[92:95], a[172:175], v[40:43]\n"
    "v_mfma_f32_16x16x32_bf16 v[68:71], a[96:99], a[168:171], v[68:71]\n"
    "ds_read_b128 a[164:167], v219 offset:40960\n"
    "v_mfma_f32_16x16x32_bf16 v[68:71], a[100:103], a[172:175], v[68:71]\n"
    "v_mfma_f32_16x16x32_bf16 v[96:99], a[104:107], a[168:171], v[96:99]\n"
    "v_mfma_f32_16x16x32_bf16 v[96:99], a[108:111], a[172:175], v[96:99]\n"
    "s_waitcnt lgkmcnt(13)\n"
    "v_mfma_f32_16x16x32_bf16 v[16:19], a[80:83], a[176:179], v[16:19]\n"
    "ds_read_b128 a[168:171], v218 offset:43008\n"
    "v_mfma_f32_16x16x32_bf16 v[16:19], a[84:87], a[180:183], v[16:19]\n"
    "v_mfma_f32_16x16x32_bf16 v[44:47], a[88:91], a[176:179], v[44:47]\n"
    "v_mfma_f32_16x16x32_bf16 v[44:47], a[92:95], a[180:183], v[44:47]\n"
    "v_mfma_f32_16x16x32_bf16 v[72:75], a[96:99], a[176:179], v[72:75]\n"
    "ds_read_b128 a[172:175], v219 offset:43008\n"
    "v_mfma_f32_16x16x32_bf16 v[72:75], a[100:103], a[180:183], v[72:75]\n"
    "v_mfma_f32_16x16x32_bf16 v[100:103], a[104:107], a[176:179], v[100:103]\n"
    "v_mfma_f32_16x16x32_bf16 v[100:103], a[108:111], a[180:183], v[100:103]\n"
    "v_mfma_f32_16x16x32_bf16 v[20:23], a[80:83], a[184:187], v[20:23]\n"
    "ds_read_b128 a[176:179], v218 offset:45056\n"
    "s_waitcnt vmcnt(5)\n"
    "s_barrier\n"
    "s_waitcnt lgkmcnt(15)\n"
    "v_mfma_f32_16x16x32_bf16 v[20:23], a[84:87], a[188:191], v[20:23]\n"
    "v_mfma_f32_16x16x32_bf16 v[48:51], a[88:91], a[184:187], v[48:51]\n"
    "ds_read_b128 a[112:115], v208 offset:32768\n"
    "v_mfma_f32_16x16x32_bf16 v[48:51], a[92:95], a[188:191], v[48:51]\n"
    "v_mfma_f32_16x16x32_bf16 v[76:79], a[96:99], a[184:187], v[76:79]\n"
    "ds_read_b128 a[180:183], v219 offset:45056\n"
    "ds_read_b128 a[116:119], v209 offset:32768\n"
    "v_mfma_f32_16x16x32_bf16 v[76:79], a[100:103], a[188:191], v[76:79]\n"
    "v_mfma_f32_16x16x32_bf16 v[104:107], a[104:107], a[184:187], v[104:107]\n"
    "v_mfma_f32_16x16x32_bf16 v[104:107], a[108:111], a[188:191], v[104:107]\n"
    "ds_read_b128 a[120:123], v208 offset:34816\n"
    "s_waitcnt lgkmcnt(15)\n"
    "v_mfma_f32_16x16x32_bf16 v[24:27], a[80:83], a[192:195], v[24:27]\n"
    "ds_read_b128 a[184:187], v218 offset:47104\n"
    "v_mfma_f32_16x16x32_bf16 v[24:27], a[84:87], a[196:199], v[24:27]\n"
    "ds_read_b128 a[124:127], v209 offset:34816\n"
    "v_mfma_f32_16x16x32_bf16 v[52:55], a[88:91], a[192:195], v[52:55]\n"
    "v_mfma_f32_16x16x32_bf16 v[52:55], a[92:95], a[196:199], v[52:55]\n"
    "v_mfma_f32_16x16x32_bf16 v[80:83], a[96:99], a[192:195], v[80:83]\n"
    "ds_read_b128 a[188:191], v219 offset:47104\n"
    "ds_read_b128 a[128:131], v208 offset:36864\n"
    "v_mfma_f32_16x16x32_bf16 v[80:83], a[100:103], a[196:199], v[80:83]\n"
    "v_mfma_f32_16x16x32_bf16 v[108:111], a[104:107], a[192:195], v[108:111]\n"
    "ds_read_b128 a[132:135], v209 offset:36864\n"
    "v_mfma_f32_16x16x32_bf16 v[108:111], a[108:111], a[196:199], v[108:111]\n"
    "v_mfma_f32_16x16x32_bf16 a[32:35], a[80:83], a[200:203], a[32:35]\n"
    "ds_read_b128 a[192:195], v218 offset:49152\n"
    "v_mfma_f32_16x16x32_bf16 a[32:35], a[84:87], a[204:207], a[32:35]\n"
    "ds_read_b128 a[136:139], v208 offset:38912\n"
    "v_mfma_f32_16x16x32_bf16 a[36:39], a[88:91], a[200:203], a[36:39]\n"
    "v_mfma_f32_16x16x32_bf16 a[36:39], a[92:95], a[204:207], a[36:39]\n"
    "ds_read_b128 a[140:143], v209 offset:38912\n"
    "v_mfma_f32_16x16x32_bf16 a[40:43], a[96:99], a[200:203], a[40:43]\n"
    "ds_read_b128 a[196:199], v219 offset:49152\n"
    "v_mfma_f32_16x16x32_bf16 a[40:43], a[100:103], a[204:207], a[40:43]\n"
    "v_mfma_f32_16x16x32_bf16 a[44:47], a[104:107], a[200:203], a[44:47]\n"
    "v_mfma_f32_16x16x32_bf16 a[44:47], a[108:111], a[204:207], a[44:47]\n"
    "s_waitcnt lgkmcnt(12)\n"
    "v_mfma_f32_16x16x32_bf16 a[0:3], a[80:83], a[208:211], a[0:3]\n"
    "ds_read_b128 a[200:203], v218 offset:51200\n"
    "v_mfma_f32_16x16x32_bf16 a[0:3], a[84:87], a[212:215], a[0:3]\n"
    "v_mfma_f32_16x16x32_bf16 a[4:7], a[88:91], a[208:211], a[4:7]\n"
    "v_mfma_f32_16x16x32_bf16 a[4:7], a[92:95], a[212:215], a[4:7]\n"
    "v_mfma_f32_16x16x32_bf16 a[8:11], a[96:99], a[208:211], a[8:11]\n"
    "ds_read_b128 a[204:207], v219 offset:51200\n"
    "v_mfma_f32_16x16x32_bf16 a[8:11], a[100:103], a[212:215], a[8:11]\n"
    "v_mfma_f32_16x16x32_bf16 a[12:15], a[104:107], a[208:211], a[12:15]\n"
    "v_mfma_f32_16x16x32_bf16 a[12:15], a[108:111], a[212:215], a[12:15]\n"
    "v_mfma_f32_16x16x32_bf16 v[0:3], a[112:115], a[144:147], v[0:3]\n"
    "ds_read_b128 a[208:211], v218 offset:53248\n"
    "s_waitcnt lgkmcnt(4)\n"
    "v_mfma_f32_16x16x32_bf16 v[0:3], a[116:119], a[148:151], v[0:3]\n"
    "ds_read_b128 a[212:215], v219 offset:53248\n"
    "v_mfma_f32_16x16x32_bf16 v[28:31], a[120:123], a[144:147], v[28:31]\n"
    "ds_read_b128 a[80:83], v208 offset:8192\n"
    "v_mfma_f32_16x16x32_bf16 v[28:31], a[124:127], a[148:151], v[28:31]\n"
    "v_mfma_f32_16x16x32_bf16 v[56:59], a[128:131], a[144:147], v[56:59]\n"
    "ds_read_b128 a[84:87], v209 offset:8192\n"
    "v_mfma_f32_16x16x32_bf16 v[56:59], a[132:135], a[148:151], v[56:59]\n"
    "v_mfma_f32_16x16x32_bf16 v[84:87], a[136:139], a[144:147], v[84:87]\n"
    "v_mfma_f32_16x16x32_bf16 v[84:87], a[140:143], a[148:151], v[84:87]\n"
    "ds_read_b128 a[88:91], v208 offset:10240\n"
    "v_mfma_f32_16x16x32_bf16 v[4:7], a[112:115], a[152:155], v[4:7]\n"
    "ds_read_b128 a[144:147], v218 offset:0\n"
    "v_mfma_f32_16x16x32_bf16 v[4:7], a[116:119], a[156:159], v[4:7]\n"
    "ds_read_b128 a[92:95], v209 offset:10240\n"
    "v_mfma_f32_16x16x32_bf16 v[32:35], a[120:123], a[152:155], v[32:35]\n"
    "v_mfma_f32_16x16x32_bf16 v[32:35], a[124:127], a[156:159], v[32:35]\n"
    "v_mfma_f32_16x16x32_bf16 v[60:63], a[128:131], a[152:155], v[60:63]\n"
    "ds_read_b128 a[96:99], v208 offset:12288\n"
    "ds_read_b128 a[148:151], v219 offset:0\n"
    "v_mfma_f32_16x16x32_bf16 v[60:63], a[132:135], a[156:159], v[60:63]\n"
    "v_mfma_f32_16x16x32_bf16 v[88:91], a[136:139], a[152:155], v[88:91]\n"
    "ds_read_b128 a[100:103], v209 offset:12288\n"
    "v_mfma_f32_16x16x32_bf16 v[88:91], a[140:143], a[156:159], v[88:91]\n"
    "v_mfma_f32_16x16x32_bf16 v[8:11], a[112:115], a[160:163], v[8:11]\n"
    "ds_read_b128 a[152:155], v218 offset:2048\n"
    "v_mfma_f32_16x16x32_bf16 v[8:11], a[116:119], a[164:167], v[8:11]\n"
    "ds_read_b128 a[104:107], v208 offset:14336\n"
    "v_mfma_f32_16x16x32_bf16 v[36:39], a[120:123], a[160:163], v[36:39]\n"
    "v_mfma_f32_16x16x32_bf16 v[36:39], a[124:127], a[164:167], v[36:39]\n"
    "ds_read_b128 a[108:111], v209 offset:14336\n"
    "v_mfma_f32_16x16x32_bf16 v[64:67], a[128:131], a[160:163], v[64:67]\n"
    "ds_read_b128 a[156:159], v219 offset:2048\n"
    "v_mfma_f32_16x16x32_bf16 v[64:67], a[132:135], a[164:167], v[64:67]\n"
    "v_mfma_f32_16x16x32_bf16 v[92:95], a[136:139], a[160:163], v[92:95]\n"
    "v_mfma_f32_16x16x32_bf16 v[92:95], a[140:143], a[164:167], v[92:95]\n"
    "v_mfma_f32_16x16x32_bf16 v[12:15], a[112:115], a[168:171], v[12:15]\n"
    "ds_read_b128 a[160:163], v218 offset:4096\n"
    "v_and_b32 v236, 1, v242\n"
    "v_mfma_f32_16x16x32_bf16 v[12:15], a[116:119], a[172:175], v[12:15]\n"
    "v_lshlrev_b32 v236, 1, v236\n"
    "v_mfma_f32_16x16x32_bf16 v[40:43], a[120:123], a[168:171], v[40:43]\n"
    "v_lshrrev_b32 v237, 1, v242\n"
    "v_mfma_f32_16x16x32_bf16 v[40:43], a[124:127], a[172:175], v[40:43]\n"
    "v_mfma_f32_16x16x32_bf16 v[68:71], a[128:131], a[168:171], v[68:71]\n"
    "ds_read_b128 a[164:167], v219 offset:4096\n"
    "v_or_b32 v236, v236, v237\n"
    "v_mfma_f32_16x16x32_bf16 v[68:71], a[132:135], a[172:175], v[68:71]\n"
    "v_lshlrev_b32 v236, 8, v236\n"
    "v_mfma_f32_16x16x32_bf16 v[96:99], a[136:139], a[168:171], v[96:99]\n"
    "v_lshl_add_u32 v243, v241, 4, v236\n"
    "v_mfma_f32_16x16x32_bf16 v[96:99], a[140:143], a[172:175], v[96:99]\n"
    "v_mfma_f32_16x16x32_bf16 v[16:19], a[112:115], a[176:179], v[16:19]\n"
    "ds_read_b128 a[168:171], v218 offset:6144\n"
    "v_and_b32 v236, 1, v242\n"
    "v_mfma_f32_16x16x32_bf16 v[16:19], a[116:119], a[180:183], v[16:19]\n"
    "v_lshl_add_u32 v236, v236, 4, v241\n"
    "v_mfma_f32_16x16x32_bf16 v[44:47], a[120:123], a[176:179], v[44:47]\n"
    "v_mfma_f32_16x16x32_bf16 v[44:47], a[124:127], a[180:183], v[44:47]\n"
    "v_lshlrev_b32 v238, 2, v237\n"
    "v_mfma_f32_16x16x32_bf16 v[72:75], a[128:131], a[176:179], v[72:75]\n"
    "ds_read_b128 a[172:175], v219 offset:6144\n"
    "v_xor_b32 v236, v236, v238\n"
    "v_mfma_f32_16x16x32_bf16 v[72:75], a[132:135], a[180:183], v[72:75]\n"
    "v_lshl_add_u32 v236, v237, 5, v236\n"
    "v_mfma_f32_16x16x32_bf16 v[100:103], a[136:139], a[176:179], v[100:103]\n"
    "v_mfma_f32_16x16x32_bf16 v[100:103], a[140:143], a[180:183], v[100:103]\n"
    "v_lshlrev_b32 v239, 4, v236\n"
    "v_mfma_f32_16x16x32_bf16 v[20:23], a[112:115], a[184:187], v[20:23]\n"
    "ds_read_b128 a[176:179], v218 offset:8192\n"
    "s_cmp_eq_u32 s62, 0\n"
    "v_mfma_f32_16x16x32_bf16 v[20:23], a[116:119], a[188:191], v[20:23]\n"
    "v_mfma_f32_16x16x32_bf16 v[48:51], a[120:123], a[184:187], v[48:51]\n"
    "s_cselect_b32 s56, s28, s30\n"
    "v_mfma_f32_16x16x32_bf16 v[48:51], a[124:127], a[188:191], v[48:51]\n"
    "s_cselect_b32 s57, s29, s31\n"
    "v_mfma_f32_16x16x32_bf16 v[76:79], a[128:131], a[184:187], v[76:79]\n"
    "ds_read_b128 a[180:183], v219 offset:8192\n"
    "s_and_b32 s57, s57, 0xffff\n"
    "v_mfma_f32_16x16x32_bf16 v[76:79], a[132:135], a[188:191], v[76:79]\n"
    "v_mfma_f32_16x16x32_bf16 v[104:107], a[136:139], a[184:187], v[104:107]\n"
    "s_mov_b32 s58, 4194304\n"
    "v_mfma_f32_16x16x32_bf16 v[104:107], a[140:143], a[188:191], v[104:107]\n"
    "s_mov_b32 s59, 0x20000\n"
    "v_mfma_f32_16x16x32_bf16 v[24:27], a[112:115], a[192:195], v[24:27]\n"
    "ds_read_b128 a[184:187], v218 offset:10240\n"
    "s_lshl_b32 s66, s64, 19\n"
    "s_waitcnt lgkmcnt(15)\n"
    "v_mfma_f32_16x16x32_bf16 v[24:27], a[116:119], a[196:199], v[24:27]\n"
    "v_mfma_f32_16x16x32_bf16 v[52:55], a[120:123], a[192:195], v[52:55]\n"
    "s_lshl_b32 s69, s61, 18\n"
    "v_mfma_f32_16x16x32_bf16 v[52:55], a[124:127], a[196:199], v[52:55]\n"
    "s_add_u32 s66, s66, s69\n"
    "v_mfma_f32_16x16x32_bf16 v[80:83], a[128:131], a[192:195], v[80:83]\n"
    "ds_read_b128 a[188:191], v219 offset:10240\n"
    "v_mfma_f32_16x16x32_bf16 v[80:83], a[132:135], a[196:199], v[80:83]\n"
    "s_sub_u32 s69, s63, 28\n"
    "v_mfma_f32_16x16x32_bf16 v[108:111], a[136:139], a[192:195], v[108:111]\n"
    "s_mul_i32 s69, s69, 9\n"
    "v_mfma_f32_16x16x32_bf16 v[108:111], a[140:143], a[196:199], v[108:111]\n"
    "s_add_u32 s69, s69, 28\n"
    "v_mfma_f32_16x16x32_bf16 a[32:35], a[112:115], a[200:203], a[32:35]\n"
    "ds_read_b128 a[192:195], v218 offset:12288\n"
    "v_mfma_f32_16x16x32_bf16 a[32:35], a[116:119], a[204:207], a[32:35]\n"
    "s_lshl_b32 s69, s69, 11\n"
    "v_mfma_f32_16x16x32_bf16 a[36:39], a[120:123], a[200:203], a[36:39]\n"
    "s_add_u32 s66, s66, s69\n"
    "v_mfma_f32_16x16x32_bf16 a[36:39], a[124:127], a[204:207], a[36:39]\n"
    "v_mfma_f32_16x16x32_bf16 a[40:43], a[128:131], a[200:203], a[40:43]\n"
    "ds_read_b128 a[196:199], v219 offset:12288\n"
    "v_mfma_f32_16x16x32_bf16 a[40:43], a[132:135], a[204:207], a[40:43]\n"
    "v_mfma_f32_16x16x32_bf16 a[44:47], a[136:139], a[200:203], a[44:47]\n"
    "v_mfma_f32_16x16x32_bf16 a[44:47], a[140:143], a[204:207], a[44:47]\n"
    "v_mfma_f32_16x16x32_bf16 a[0:3], a[112:115], a[208:211], a[0:3]\n"
    "ds_read_b128 a[200:203], v218 offset:14336\n"
    "v_mfma_f32_16x16x32_bf16 a[0:3], a[116:119], a[212:215], a[0:3]\n"
    "v_mfma_f32_16x16x32_bf16 a[4:7], a[120:123], a[208:211], a[4:7]\n"
    "v_mfma_f32_16x16x32_bf16 a[4:7], a[124:127], a[212:215], a[4:7]\n"
    "v_mfma_f32_16x16x32_bf16 a[8:11], a[128:131], a[208:211], a[8:11]\n"
    "ds_read_b128 a[204:207], v219 offset:14336\n"
    "v_mfma_f32_16x16x32_bf16 a[8:11], a[132:135], a[212:215], a[8:11]\n"
    "v_mfma_f32_16x16x32_bf16 a[12:15], a[136:139], a[208:211], a[12:15]\n"
    "v_mfma_f32_16x16x32_bf16 a[12:15], a[140:143], a[212:215], a[12:15]\n"
    "s_waitcnt vmcnt(1)\n"
    "s_barrier\n"
    "v_mfma_f32_16x16x32_bf16 v[112:115], a[80:83], a[144:147], v[112:115]\n"
    "ds_read_b128 a[208:211], v218 offset:16384\n"
    "s_waitcnt lgkmcnt(14)\n"
    "v_mfma_f32_16x16x32_bf16 v[112:115], a[84:87], a[148:151], v[112:115]\n"
    "ds_read_b128 a[212:215], v219 offset:16384\n"
    "v_mfma_f32_16x16x32_bf16 v[136:139], a[88:91], a[144:147], v[136:139]\n"
    "ds_read_b128 a[112:115], v208 offset:40960\n"
    "v_mfma_f32_16x16x32_bf16 v[136:139], a[92:95], a[148:151], v[136:139]\n"
    "v_mfma_f32_16x16x32_bf16 v[160:163], a[96:99], a[144:147], v[160:163]\n"
    "ds_read_b128 a[116:119], v209 offset:40960\n"
    "v_mfma_f32_16x16x32_bf16 v[160:163], a[100:103], a[148:151], v[160:163]\n"
    "v_mfma_f32_16x16x32_bf16 v[184:187], a[104:107], a[144:147], v[184:187]\n"
    "v_mfma_f32_16x16x32_bf16 v[184:187], a[108:111], a[148:151], v[184:187]\n"
    "ds_read_b128 a[120:123], v208 offset:43008\n"
    "v_mfma_f32_16x16x32_bf16 v[116:119], a[80:83], a[152:155], v[116:119]\n"
    "ds_read_b128 a[144:147], v218 offset:36864\n"
    "s_waitcnt lgkmcnt(15)\n"
    "v_mfma_f32_16x16x32_bf16 v[116:119], a[84:87], a[156:159], v[116:119]\n"
    "ds_read_b128 a[124:127], v209 offset:43008\n"
    "v_mfma_f32_16x16x32_bf16 v[140:143], a[88:91], a[152:155], v[140:143]\n"
    "v_cvt_pk_bf16_f32 v228, v0, v1\n"
    "v_cvt_pk_bf16_f32 v229, v2, v3\n"
    "v_mfma_f32_16x16x32_bf16 v[140:143], a[92:95], a[156:159], v[140:143]\n"
    "v_cvt_pk_bf16_f32 v230, v28, v29\n"
    "v_cvt_pk_bf16_f32 v231, v30, v31\n"
    "v_mfma_f32_16x16x32_bf16 v[164:167], a[96:99], a[152:155], v[164:167]\n"
    "ds_read_b128 a[128:131], v208 offset:45056\n"
    "ds_read_b128 a[148:151], v219 offset:36864\n"
    "s_nop 1\n"
    "v_mfma_f32_16x16x32_bf16 v[164:167], a[100:103], a[156:159], v[164:167]\n"
    "v_permlane16_swap_b32 v228, v230\n"
    "v_permlane16_swap_b32 v229, v231\n"
    "v_mfma_f32_16x16x32_bf16 v[188:191], a[104:107], a[152:155], v[188:191]\n"
    "ds_read_b128 a[132:135], v209 offset:45056\n"
    "s_add_u32 s69, s66, 0\n"
    "v_mfma_f32_16x16x32_bf16 v[188:191], a[108:111], a[156:159], v[188:191]\n"
    "buffer_store_dwordx4 v[228:231], v243, s[56:59], s69 offen offset:0 sc1\n"
    "v_cvt_pk_bf16_f32 v232, v4, v5\n"
    "v_mfma_f32_16x16x32_bf16 v[120:123], a[80:83], a[160:163], v[120:123]\n"
    "ds_read_b128 a[152:155], v218 offset:38912\n"
    "v_cvt_pk_bf16_f32 v233, v6, v7\n"
    "v_mfma_f32_16x16x32_bf16 v[120:123], a[84:87], a[164:167], v[120:123]\n"
    "ds_read_b128 a[136:139], v208 offset:47104\n"
    "v_cvt_pk_bf16_f32 v234, v32, v33\n"
    "v_cvt_pk_bf16_f32 v235, v34, v35\n"
    "v_mfma_f32_16x16x32_bf16 v[144:147], a[88:91], a[160:163], v[144:147]\n"
    "s_nop 1\n"
    "v_permlane16_swap_b32 v232, v234\n"
    "v_mfma_f32_16x16x32_bf16 v[144:147], a[92:95], a[164:167], v[144:147]\n"
    "ds_read_b128 a[140:143], v209 offset:47104\n"
    "v_permlane16_swap_b32 v233, v235\n"
    "v_mfma_f32_16x16x32_bf16 v[168:171], a[96:99], a[160:163], v[168:171]\n"
    "ds_read_b128 a[156:159], v219 offset:38912\n"
    "s_add_u32 s69, s66, 0\n"
    "buffer_store_dwordx4 v[232:235], v243, s[56:59], s69 offen offset:2048 sc1\n"
    "v_mfma_f32_16x16x32_bf16 v[168:171], a[100:103], a[164:167], v[168:171]\n"
    "v_cvt_pk_bf16_f32 v228, v8, v9\n"
    "v_mfma_f32_16x16x32_bf16 v[192:195], a[104:107], a[160:163], v[192:195]\n"
    "v_cvt_pk_bf16_f32 v229, v10, v11\n"
    "v_cvt_pk_bf16_f32 v230, v36, v37\n"
    "v_mfma_f32_16x16x32_bf16 v[192:195], a[108:111], a[164:167], v[192:195]\n"
    "v_cvt_pk_bf16_f32 v231, v38, v39\n"
    "v_mfma_f32_16x16x32_bf16 v[124:127], a[80:83], a[168:171], v[124:127]\n"
    "ds_read_b128 a[160:163], v218 offset:40960\n"
    "s_nop 1\n"
    "v_permlane16_swap_b32 v228, v230\n"
    "s_waitcnt lgkmcnt(15)\n"
    "v_mfma_f32_16x16x32_bf16 v[124:127], a[84:87], a[172:175], v[124:127]\n"
    "v_permlane16_swap_b32 v229, v231\n"
    "s_add_u32 s69, s66, 4096\n"
    "v_mfma_f32_16x16x32_bf16 v[148:151], a[88:91], a[168:171], v[148:151]\n"
    "buffer_store_dwordx4 v[228:231], v243, s[56:59], s69 offen offset:0 sc1\n"
    "v_mfma_f32_16x16x32_bf16 v[148:151], a[92:95], a[172:175], v[148:151]\n"
    "v_cvt_pk_bf16_f32 v232, v12, v13\n"
    "v_cvt_pk_bf16_f32 v233, v14, v15\n"
    "v_mfma_f32_16x16x32_bf16 v[172:175], a[96:99], a[168:171], v[172:175]\n"
    "ds_read_b128 a[164:167], v219 offset:40960\n"
    "v_cvt_pk_bf16_f32 v234, v40, v41\n"
    "v_mfma_f32_16x16x32_bf16 v[172:175], a[100:103], a[172:175], v[172:175]\n"
    "v_cvt_pk_bf16_f32 v235, v42, v43\n"
    "s_nop 1\n"
    "v_mfma_f32_16x16x32_bf16 v[196:199], a[104:107], a[168:171], v[196:199]\n"
    "v_permlane16_swap_b32 v232, v234\n"
    "v_mfma_f32_16x16x32_bf16 v[196:199], a[108:111], a[172:175], v[196:199]\n"
    "v_permlane16_swap_b32 v233, v235\n"
    "s_add_u32 s69, s66, 4096\n"
    "v_mfma_f32_16x16x32_bf16 v[128:131], a[80:83], a[176:179], v[128:131]\n"
    "ds_read_b128 a[168:171], v218 offset:43008\n"
    "buffer_store_dwordx4 v[232:235], v243, s[56:59], s69 offen offset:2048 sc1\n"
    "v_mfma_f32_16x16x32_bf16 v[128:131], a[84:87], a[180:183], v[128:131]\n"
    "v_cvt_pk_bf16_f32 v228, v16, v17\n"
    "v_cvt_pk_bf16_f32 v229, v18, v19\n"
    "v_mfma_f32_16x16x32_bf16 v[152:155], a[88:91], a[176:179], v[152:155]\n"
    "v_cvt_pk_bf16_f32 v230, v44, v45\n"
    "v_cvt_pk_bf16_f32 v231, v46, v47\n"
    "v_mfma_f32_16x16x32_bf16 v[152:155], a[92:95], a[180:183], v[152:155]\n"
    "s_nop 1\n"
    "v_mfma_f32_16x16x32_bf16 v[176:179], a[96:99], a[176:179], v[176:179]\n"
    "ds_read_b128 a[172:175], v219 offset:43008\n"
    "v_permlane16_swap_b32 v228, v230\n"
    "v_permlane16_swap_b32 v229, v231\n"
    "v_mfma_f32_16x16x32_bf16 v[176:179], a[100:103], a[180:183], v[176:179]\n"
    "s_add_u32 s69, s66, 8192\n"
    "v_mfma_f32_16x16x32_bf16 v[200:203], a[104:107], a[176:179], v[200:203]\n"
    "buffer_store_dwordx4 v[228:231], v243, s[56:59], s69 offen offset:0 sc1\n"
    "v_cvt_pk_bf16_f32 v232, v20, v21\n"
    "v_mfma_f32_16x16x32_bf16 v[200:203], a[108:111], a[180:183], v[200:203]\n"
    "v_cvt_pk_bf16_f32 v233, v22, v23\n"
    "v_mfma_f32_16x16x32_bf16 v[132:135], a[80:83], a[184:187], v[132:135]\n"
    "ds_read_b128 a[176:179], v218 offset:45056\n"
    "v_cvt_pk_bf16_f32 v234, v48, v49\n"
    "v_cvt_pk_bf16_f32 v235, v50, v51\n"
    "v_mfma_f32_16x16x32_bf16 v[132:135], a[84:87], a[188:191], v[132:135]\n"
    "s_nop 1\n"
    "v_permlane16_swap_b32 v232, v234\n"
    "v_mfma_f32_16x16x32_bf16 v[156:159], a[88:91], a[184:187], v[156:159]\n"
    "v_permlane16_swap_b32 v233, v235\n"
    "v_mfma_f32_16x16x32_bf16 v[156:159], a[92:95], a[188:191], v[156:159]\n"
    "s_add_u32 s69, s66, 8192\n"
    "buffer_store_dwordx4 v[232:235], v243, s[56:59], s69 offen offset:2048 sc1\n"
    "v_mfma_f32_16x16x32_bf16 v[180:183], a[96:99], a[184:187], v[180:183]\n"
    "ds_read_b128 a[180:183], v219 offset:45056\n"
    "v_cvt_pk_bf16_f32 v228, v24, v25\n"
    "v_mfma_f32_16x16x32_bf16 v[180:183], a[100:103], a[188:191], v[180:183]\n"
    "v_cvt_pk_bf16_f32 v229, v26, v27\n"
    "v_cvt_pk_bf16_f32 v230, v52, v53\n"
    "v_mfma_f32_16x16x32_bf16 v[204:207], a[104:107], a[184:187], v[204:207]\n"
    "v_cvt_pk_bf16_f32 v231, v54, v55\n"
    "v_mfma_f32_16x16x32_bf16 v[204:207], a[108:111], a[188:191], v[204:207]\n"
    "s_nop 1\n"
    "v_permlane16_swap_b32 v228, v230\n"
    "v_mfma_f32_16x16x32_bf16 a[64:67], a[80:83], a[192:195], a[64:67]\n"
    "ds_read_b128 a[184:187], v218 offset:47104\n"
    "v_permlane16_swap_b32 v229, v231\n"
    "s_add_u32 s69, s66, 12288\n"
    "v_mfma_f32_16x16x32_bf16 a[64:67], a[84:87], a[196:199], a[64:67]\n"
    "buffer_store_dwordx4 v[228:231], v243, s[56:59], s69 offen offset:0 sc1\n"
    "v_mfma_f32_16x16x32_bf16 a[68:71], a[88:91], a[192:195], a[68:71]\n"
    "v_accvgpr_read_b32 v244, a32\n"
    "v_accvgpr_read_b32 v245, a33\n"
    "v_mfma_f32_16x16x32_bf16 a[68:71], a[92:95], a[196:199], a[68:71]\n"
    "v_accvgpr_read_b32 v246, a34\n"
    "v_mfma_f32_16x16x32_bf16 a[72:75], a[96:99], a[192:195], a[72:75]\n"
    "ds_read_b128 a[188:191], v219 offset:47104\n"
    "v_accvgpr_read_b32 v247, a35\n"
    "v_cvt_pk_bf16_f32 v232, v244, v245\n"
    "v_mfma_f32_16x16x32_bf16 a[72:75], a[100:103], a[196:199], a[72:75]\n"
    "v_cvt_pk_bf16_f32 v233, v246, v247\n"
    "v_mfma_f32_16x16x32_bf16 a[76:79], a[104:107], a[192:195], a[76:79]\n"
    "v_accvgpr_read_b32 v248, a36\n"
    "v_accvgpr_read_b32 v249, a37\n"
    "v_mfma_f32_16x16x32_bf16 a[76:79], a[108:111], a[196:199], a[76:79]\n"
    "v_accvgpr_read_b32 v250, a38\n"
    "v_mfma_f32_16x16x32_bf16 a[48:51], a[80:83], a[200:203], a[48:51]\n"
    "ds_read_b128 a[192:195], v218 offset:49152\n"
    "v_accvgpr_read_b32 v251, a39\n"
    "v_cvt_pk_bf16_f32 v234, v248, v249\n"
    "v_mfma_f32_16x16x32_bf16 a[48:51], a[84:87], a[204:207], a[48:51]\n"
    "v_cvt_pk_bf16_f32 v235, v250, v251\n"
    "s_nop 1\n"
    "v_mfma_f32_16x16x32_bf16 a[52:55], a[88:91], a[200:203], a[52:55]\n"
    "v_permlane16_swap_b32 v232, v234\n"
    "v_mfma_f32_16x16x32_bf16 a[52:55], a[92:95], a[204:207], a[52:55]\n"
    "v_permlane16_swap_b32 v233, v235\n"
    "s_add_u32 s69, s66, 12288\n"
    "v_mfma_f32_16x16x32_bf16 a[56:59], a[96:99], a[200:203], a[56:59]\n"
    "ds_read_b128 a[196:199], v219 offset:49152\n"
    "buffer_store_dwordx4 v[232:235], v243, s[56:59], s69 offen offset:2048 sc1\n"
    "v_mfma_f32_16x16x32_bf16 a[56:59], a[100:103], a[204:207], a[56:59]\n"
    "v_accvgpr_read_b32 v244, a0\n"
    "v_accvgpr_read_b32 v245, a1\n"
    "v_mfma_f32_16x16x32_bf16 a[60:63], a[104:107], a[200:203], a[60:63]\n"
    "v_accvgpr_read_b32 v246, a2\n"
    "v_mfma_f32_16x16x32_bf16 a[60:63], a[108:111], a[204:207], a[60:63]\n"
    "v_accvgpr_read_b32 v247, a3\n"
    "v_cvt_pk_bf16_f32 v228, v244, v245\n"
    "s_waitcnt lgkmcnt(15)\n"
    "v_mfma_f32_16x16x32_bf16 a[16:19], a[80:83], a[208:211], a[16:19]\n"
    "ds_read_b128 a[200:203], v218 offset:51200\n"
    "v_cvt_pk_bf16_f32 v229, v246, v247\n"
    "v_accvgpr_read_b32 v248, a4\n"
    "v_mfma_f32_16x16x32_bf16 a[16:19], a[84:87], a[212:215], a[16:19]\n"
    "v_accvgpr_read_b32 v249, a5\n"
    "v_mfma_f32_16x16x32_bf16 a[20:23], a[88:91], a[208:211], a[20:23]\n"
    "v_accvgpr_read_b32 v250, a6\n"
    "v_accvgpr_read_b32 v251, a7\n"
    "v_mfma_f32_16x16x32_bf16 a[20:23], a[92:95], a[212:215], a[20:23]\n"
    "v_cvt_pk_bf16_f32 v230, v248, v249\n"
    "v_mfma_f32_16x16x32_bf16 a[24:27], a[96:99], a[208:211], a[24:27]\n"
    "ds_read_b128 a[204:207], v219 offset:51200\n"
    "v_cvt_pk_bf16_f32 v231, v250, v251\n"
    "s_nop 1\n"
    "v_mfma_f32_16x16x32_bf16 a[24:27], a[100:103], a[212:215], a[24:27]\n"
    "v_permlane16_swap_b32 v228, v230\n"
    "v_mfma_f32_16x16x32_bf16 a[28:31], a[104:107], a[208:211], a[28:31]\n"
    "v_permlane16_swap_b32 v229, v231\n"
    "s_add_u32 s69, s66, 16384\n"
    "v_mfma_f32_16x16x32_bf16 a[28:31], a[108:111], a[212:215], a[28:31]\n"
    "buffer_store_dwordx4 v[228:231], v243, s[56:59], s69 offen offset:0 sc1\n"
    "v_mfma_f32_16x16x32_bf16 v[112:115], a[112:115], a[144:147], v[112:115]\n"
    "ds_read_b128 a[208:211], v218 offset:53248\n"
    "v_mfma_f32_16x16x32_bf16 v[112:115], a[116:119], a[148:151], v[112:115]\n"
    "ds_read_b128 a[212:215], v219 offset:53248\n"
    "v_mfma_f32_16x16x32_bf16 v[136:139], a[120:123], a[144:147], v[136:139]\n"
    "v_mfma_f32_16x16x32_bf16 v[136:139], a[124:127], a[148:151], v[136:139]\n"
    "v_cvt_pk_bf16_f32 v228, v56, v57\n"
    "v_cvt_pk_bf16_f32 v229, v58, v59\n"
    "v_mfma_f32_16x16x32_bf16 v[160:163], a[128:131], a[144:147], v[160:163]\n"
    "v_cvt_pk_bf16_f32 v230, v84, v85\n"
    "s_waitcnt lgkmcnt(14)\n"
    "v_mfma_f32_16x16x32_bf16 v[160:163], a[132:135], a[148:151], v[160:163]\n"
    "v_cvt_pk_bf16_f32 v231, v86, v87\n"
    "s_nop 1\n"
    "v_mfma_f32_16x16x32_bf16 v[184:187], a[136:139], a[144:147], v[184:187]\n"
    "v_permlane16_swap_b32 v228, v230\n"
    "v_mfma_f32_16x16x32_bf16 v[184:187], a[140:143], a[148:151], v[184:187]\n"
    "v_permlane16_swap_b32 v229, v231\n"
    "s_add_u32 s69, s66, 0\n"
    "v_mfma_f32_16x16x32_bf16 v[116:119], a[112:115], a[152:155], v[116:119]\n"
    "buffer_store_dwordx4 v[228:231], v243, s[56:59], s69 offen offset:1024 sc1\n"
    "v_mfma_f32_16x16x32_bf16 v[116:119], a[116:119], a[156:159], v[116:119]\n"
    "v_cvt_pk_bf16_f32 v232, v60, v61\n"
    "v_mfma_f32_16x16x32_bf16 v[140:143], a[120:123], a[152:155], v[140:143]\n"
    "v_cvt_pk_bf16_f32 v233, v62, v63\n"
    "v_cvt_pk_bf16_f32 v234, v88, v89\n"
    "v_mfma_f32_16x16x32_bf16 v[140:143], a[124:127], a[156:159], v[140:143]\n"
    "v_cvt_pk_bf16_f32 v235, v90, v91\n"
    "v_mfma_f32_16x16x32_bf16 v[164:167], a[128:131], a[152:155], v[164:167]\n"
    "s_nop 1\n"
    "v_permlane16_swap_b32 v232, v234\n"
    "v_mfma_f32_16x16x32_bf16 v[164:167], a[132:135], a[156:159], v[164:167]\n"
    "v_permlane16_swap_b32 v233, v235\n"
    "v_mfma_f32_16x16x32_bf16 v[188:191], a[136:139], a[152:155], v[188:191]\n"
    "s_add_u32 s69, s66, 0\n"
    "v_mfma_f32_16x16x32_bf16 v[188:191], a[140:143], a[156:159], v[188:191]\n"
    "buffer_store_dwordx4 v[232:235], v243, s[56:59], s69 offen offset:3072 sc1\n"
    "v_cvt_pk_bf16_f32 v228, v64, v65\n"
    "s_waitcnt lgkmcnt(12)\n"
    "v_mfma_f32_16x16x32_bf16 v[120:123], a[112:115], a[160:163], v[120:123]\n"
    "v_cvt_pk_bf16_f32 v229, v66, v67\n"
    "v_mfma_f32_16x16x32_bf16 v[120:123], a[116:119], a[164:167], v[120:123]\n"
    "v_cvt_pk_bf16_f32 v230, v92, v93\n"
    "v_cvt_pk_bf16_f32 v231, v94, v95\n"
    "v_mfma_f32_16x16x32_bf16 v[144:147], a[120:123], a[160:163], v[144:147]\n"
    "s_nop 1\n"
    "v_mfma_f32_16x16x32_bf16 v[144:147], a[124:127], a[164:167], v[144:147]\n"
    "v_permlane16_swap_b32 v228, v230\n"
    "v_mfma_f32_16x16x32_bf16 v[168:171], a[128:131], a[160:163], v[168:171]\n"
    "v_permlane16_swap_b32 v229, v231\n"
    "s_add_u32 s69, s66, 4096\n"
    "v_mfma_f32_16x16x32_bf16 v[168:171], a[132:135], a[164:167], v[168:171]\n"
    "buffer_store_dwordx4 v[228:231], v243, s[56:59], s69 offen offset:1024 sc1\n"
    "v_mfma_f32_16x16x32_bf16 v[192:195], a[136:139], a[160:163], v[192:195]\n"
    "v_cvt_pk_bf16_f32 v232, v68, v69\n"
    "v_cvt_pk_bf16_f32 v233, v70, v71\n"
    "v_mfma_f32_16x16x32_bf16 v[192:195], a[140:143], a[164:167], v[192:195]\n"
    "v_cvt_pk_bf16_f32 v234, v96, v97\n"
    "s_waitcnt lgkmcnt(10)\n"
    "v_mfma_f32_16x16x32_bf16 v[124:127], a[112:115], a[168:171], v[124:127]\n"
    "v_cvt_pk_bf16_f32 v235, v98, v99\n"
    "v_mfma_f32_16x16x32_bf16 v[124:127], a[116:119], a[172:175], v[124:127]\n"
    "s_nop 1\n"
    "v_permlane16_swap_b32 v232, v234\n"
    "v_mfma_f32_16x16x32_bf16 v[148:151], a[120:123], a[168:171], v[148:151]\n"
    "v_permlane16_swap_b32 v233, v235\n"
    "v_mfma_f32_16x16x32_bf16 v[148:151], a[124:127], a[172:175], v[148:151]\n"
    "s_add_u32 s69, s66, 4096\n"
    "buffer_store_dwordx4 v[232:235], v243, s[56:59], s69 offen offset:3072 sc1\n"
    "v_mfma_f32_16x16x32_bf16 v[172:175], a[128:131], a[168:171], v[172:175]\n"
    "v_cvt_pk_bf16_f32 v228, v72, v73\n"
    "v_mfma_f32_16x16x32_bf16 v[172:175], a[132:135], a[172:175], v[172:175]\n"
    "v_cvt_pk_bf16_f32 v229, v74, v75\n"
    "v_mfma_f32_16x16x32_bf16 v[196:199], a[136:139], a[168:171], v[196:199]\n"
    "v_cvt_pk_bf16_f32 v230, v100, v101\n"
    "v_cvt_pk_bf16_f32 v231, v102, v103\n"
    "v_mfma_f32_16x16x32_bf16 v[196:199], a[140:143], a[172:175], v[196:199]\n"
    "s_nop 1\n"
    "s_waitcnt lgkmcnt(8)\n"
    "v_mfma_f32_16x16x32_bf16 v[128:131], a[112:115], a[176:179], v[128:131]\n"
    "v_permlane16_swap_b32 v228, v230\n"
    "v_permlane16_swap_b32 v229, v231\n"
    "v_mfma_f32_16x16x32_bf16 v[128:131], a[116:119], a[180:183], v[128:131]\n"
    "s_add_u32 s69, s66, 8192\n"
    "v_mfma_f32_16x16x32_bf16 v[152:155], a[120:123], a[176:179], v[152:155]\n"
    "buffer_store_dwordx4 v[228:231], v243, s[56:59], s69 offen offset:1024 sc1\n"
    "v_mfma_f32_16x16x32_bf16 v[152:155], a[124:127], a[180:183], v[152:155]\n"
    "v_cvt_pk_bf16_f32 v232, v76, v77\n"
    "v_cvt_pk_bf16_f32 v233, v78, v79\n"
    "v_mfma_f32_16x16x32_bf16 v[176:179], a[128:131], a[176:179], v[176:179]\n"
    "v_cvt_pk_bf16_f32 v234, v104, v105\n"
    "v_mfma_f32_16x16x32_bf16 v[176:179], a[132:135], a[180:183], v[176:179]\n"
    "v_cvt_pk_bf16_f32 v235, v106, v107\n"
    "s_nop 1\n"
    "v_mfma_f32_16x16x32_bf16 v[200:203], a[136:139], a[176:179], v[200:203]\n"
    "v_permlane16_swap_b32 v232, v234\n"
    "v_mfma_f32_16x16x32_bf16 v[200:203], a[140:143], a[180:183], v[200:203]\n"
    "v_permlane16_swap_b32 v233, v235\n"
    "s_add_u32 s69, s66, 8192\n"
    "s_waitcnt lgkmcnt(6)\n"
    "v_mfma_f32_16x16x32_bf16 v[132:135], a[112:115], a[184:187], v[132:135]\n"
    "buffer_store_dwordx4 v[232:235], v243, s[56:59], s69 offen offset:3072 sc1\n"
    "v_mfma_f32_16x16x32_bf16 v[132:135], a[116:119], a[188:191], v[132:135]\n"
    "v_cvt_pk_bf16_f32 v228, v80, v81\n"
    "v_mfma_f32_16x16x32_bf16 v[156:159], a[120:123], a[184:187], v[156:159]\n"
    "v_cvt_pk_bf16_f32 v229, v82, v83\n"
    "v_cvt_pk_bf16_f32 v230, v108, v109\n"
    "v_mfma_f32_16x16x32_bf16 v[156:159], a[124:127], a[188:191], v[156:159]\n"
    "v_cvt_pk_bf16_f32 v231, v110, v111\n"
    "v_mfma_f32_16x16x32_bf16 v[180:183], a[128:131], a[184:187], v[180:183]\n"
    "s_nop 1\n"
    "v_permlane16_swap_b32 v228, v230\n"
    "v_mfma_f32_16x16x32_bf16 v[180:183], a[132:135], a[188:191], v[180:183]\n"
    "v_permlane16_swap_b32 v229, v231\n"
    "v_mfma_f32_16x16x32_bf16 v[204:207], a[136:139], a[184:187], v[204:207]\n"
    "s_add_u32 s69, s66, 12288\n"
    "v_mfma_f32_16x16x32_bf16 v[204:207], a[140:143], a[188:191], v[204:207]\n"
    "buffer_store_dwordx4 v[228:231], v243, s[56:59], s69 offen offset:1024 sc1\n"
    "v_accvgpr_read_b32 v244, a40\n"
    "s_waitcnt lgkmcnt(4)\n"
    "v_mfma_f32_16x16x32_bf16 a[64:67], a[112:115], a[192:195], a[64:67]\n"
    "v_accvgpr_read_b32 v245, a41\n"
    "v_mfma_f32_16x16x32_bf16 a[64:67], a[116:119], a[196:199], a[64:67]\n"
    "v_accvgpr_read_b32 v246, a42\n"
    "v_accvgpr_read_b32 v247, a43\n"
    "v_mfma_f32_16x16x32_bf16 a[68:71], a[120:123], a[192:195], a[68:71]\n"
    "v_cvt_pk_bf16_f32 v232, v244, v245\n"
    "v_mfma_f32_16x16x32_bf16 a[68:71], a[124:127], a[196:199], a[68:71]\n"
    "v_cvt_pk_bf16_f32 v233, v246, v247\n"
    "v_mfma_f32_16x16x32_bf16 a[72:75], a[128:131], a[192:195], a[72:75]\n"
    "v_accvgpr_read_b32 v248, a44\n"
    "v_accvgpr_read_b32 v249, a45\n"
    "v_mfma_f32_16x16x32_bf16 a[72:75], a[132:135], a[196:199], a[72:75]\n"
    "v_accvgpr_read_b32 v250, a46\n"
    "v_mfma_f32_16x16x32_bf16 a[76:79], a[136:139], a[192:195], a[76:79]\n"
    "v_accvgpr_read_b32 v251, a47\n"
    "v_cvt_pk_bf16_f32 v234, v248, v249\n"
    "v_mfma_f32_16x16x32_bf16 a[76:79], a[140:143], a[196:199], a[76:79]\n"
    "v_cvt_pk_bf16_f32 v235, v250, v251\n"
    "s_waitcnt lgkmcnt(2)\n"
    "v_mfma_f32_16x16x32_bf16 a[48:51], a[112:115], a[200:203], a[48:51]\n"
    "s_nop 1\n"
    "v_mfma_f32_16x16x32_bf16 a[48:51], a[116:119], a[204:207], a[48:51]\n"
    "v_permlane16_swap_b32 v232, v234\n"
    "v_permlane16_swap_b32 v233, v235\n"
    "v_mfma_f32_16x16x32_bf16 a[52:55], a[120:123], a[200:203], a[52:55]\n"
    "s_add_u32 s69, s66, 12288\n"
    "v_mfma_f32_16x16x32_bf16 a[52:55], a[124:127], a[204:207], a[52:55]\n"
    "buffer_store_dwordx4 v[232:235], v243, s[56:59], s69 offen offset:3072 sc1\n"
    "v_accvgpr_read_b32 v244, a8\n"
    "v_mfma_f32_16x16x32_bf16 a[56:59], a[128:131], a[200:203], a[56:59]\n"
    "v_accvgpr_read_b32 v245, a9\n"
    "v_mfma_f32_16x16x32_bf16 a[56:59], a[132:135], a[204:207], a[56:59]\n"
    "v_accvgpr_read_b32 v246, a10\n"
    "v_mfma_f32_16x16x32_bf16 a[60:63], a[136:139], a[200:203], a[60:63]\n"
    "v_accvgpr_read_b32 v247, a11\n"
    "v_cvt_pk_bf16_f32 v228, v244, v245\n"
    "v_mfma_f32_16x16x32_bf16 a[60:63], a[140:143], a[204:207], a[60:63]\n"
    "v_cvt_pk_bf16_f32 v229, v246, v247\n"
    "s_waitcnt lgkmcnt(0)\n"
    "v_mfma_f32_16x16x32_bf16 a[16:19], a[112:115], a[208:211], a[16:19]\n"
    "v_accvgpr_read_b32 v248, a12\n"
    "v_accvgpr_read_b32 v249, a13\n"
    "v_mfma_f32_16x16x32_bf16 a[16:19], a[116:119], a[212:215], a[16:19]\n"
    "v_accvgpr_read_b32 v250, a14\n"
    "v_mfma_f32_16x16x32_bf16 a[20:23], a[120:123], a[208:211], a[20:23]\n"
    "v_accvgpr_read_b32 v251, a15\n"
    "v_mfma_f32_16x16x32_bf16 a[20:23], a[124:127], a[212:215], a[20:23]\n"
    "v_cvt_pk_bf16_f32 v230, v248, v249\n"
    "v_cvt_pk_bf16_f32 v231, v250, v251\n"
    "v_mfma_f32_16x16x32_bf16 a[24:27], a[128:131], a[208:211], a[24:27]\n"
    "s_nop 1\n"
    "v_mfma_f32_16x16x32_bf16 a[24:27], a[132:135], a[212:215], a[24:27]\n"
    "v_permlane16_swap_b32 v228, v230\n"
    "v_permlane16_swap_b32 v229, v231\n"
    "v_mfma_f32_16x16x32_bf16 a[28:31], a[136:139], a[208:211], a[28:31]\n"
    "s_add_u32 s69, s66, 16384\n"
    "v_mfma_f32_16x16x32_bf16 a[28:31], a[140:143], a[212:215], a[28:31]\n"
    "buffer_store_dwordx4 v[228:231], v243, s[56:59], s69 offen offset:1024 sc1\n"
    "s_nop 7\n"
    "s_lshl_b32 s99, s64, 5\n"
    "s_add_u32 s99, s99, s63\n"
    "s_lshl_b32 s99, s99, 3\n"
    "v_cvt_pk_bf16_f32 v228, v112, v113\n"
    "v_cvt_pk_bf16_f32 v229, v114, v115\n"
    "v_cvt_pk_bf16_f32 v230, v136, v137\n"
    "v_cvt_pk_bf16_f32 v231, v138, v139\n"
    "s_nop 1\n"
    "v_permlane16_swap_b32 v228, v230\n"
    "v_permlane16_swap_b32 v229, v231\n"
    "s_add_u32 s69, s66, 131072\n"
    "buffer_store_dwordx4 v[228:231], v243, s[56:59], s69 offen offset:0 sc1\n"
    "v_cvt_pk_bf16_f32 v232, v116, v117\n"
    "v_cvt_pk_bf16_f32 v233, v118, v119\n"
    "v_cvt_pk_bf16_f32 v234, v140, v141\n"
    "v_cvt_pk_bf16_f32 v235, v142, v143\n"
    "s_nop 1\n"
    "v_permlane16_swap_b32 v232, v234\n"
    "v_permlane16_swap_b32 v233, v235\n"
    "s_add_u32 s69, s66, 131072\n"
    "buffer_store_dwordx4 v[232:235], v243, s[56:59], s69 offen offset:2048 sc1\n"
    "v_cvt_pk_bf16_f32 v228, v120, v121\n"
    "v_cvt_pk_bf16_f32 v229, v122, v123\n"
    "v_cvt_pk_bf16_f32 v230, v144, v145\n"
    "v_cvt_pk_bf16_f32 v231, v146, v147\n"
    "s_nop 1\n"
    "v_permlane16_swap_b32 v228, v230\n"
    "v_permlane16_swap_b32 v229, v231\n"
    "s_add_u32 s69, s66, 135168\n"
    "buffer_store_dwordx4 v[228:231], v243, s[56:59], s69 offen offset:0 sc1\n"
    "v_cvt_pk_bf16_f32 v232, v124, v125\n"
    "v_cvt_pk_bf16_f32 v233, v126, v127\n"
    "v_cvt_pk_bf16_f32 v234, v148, v149\n"
    "v_cvt_pk_bf16_f32 v235, v150, v151\n"
    "s_nop 1\n"
    "v_permlane16_swap_b32 v232, v234\n"
    "v_permlane16_swap_b32 v233, v235\n"
    "s_add_u32 s69, s66, 135168\n"
    "buffer_store_dwordx4 v[232:235], v243, s[56:59], s69 offen offset:2048 sc1\n"
    "v_cvt_pk_bf16_f32 v228, v128, v129\n"
    "v_cvt_pk_bf16_f32 v229, v130, v131\n"
    "v_cvt_pk_bf16_f32 v230, v152, v153\n"
    "v_cvt_pk_bf16_f32 v231, v154, v155\n"
    "s_nop 1\n"
    "v_permlane16_swap_b32 v228, v230\n"
    "v_permlane16_swap_b32 v229, v231\n"
    "s_add_u32 s69, s66, 139264\n"
    "buffer_store_dwordx4 v[228:231], v243, s[56:59], s69 offen offset:0 sc1\n"
    "v_cvt_pk_bf16_f32 v232, v132, v133\n"
    "v_cvt_pk_bf16_f32 v233, v134, v135\n"
    "v_cvt_pk_bf16_f32 v234, v156, v157\n"
    "v_cvt_pk_bf16_f32 v235, v158, v159\n"
    "s_nop 1\n"
    "v_permlane16_swap_b32 v232, v234\n"
    "v_permlane16_swap_b32 v233, v235\n"
    "s_add_u32 s69, s66, 139264\n"
    "buffer_store_dwordx4 v[232:235], v243, s[56:59], s69 offen offset:2048 sc1\n"
    "v_accvgpr_read_b32 v244, a64\n"
    "v_accvgpr_read_b32 v245, a65\n"
    "v_accvgpr_read_b32 v246, a66\n"
    "v_accvgpr_read_b32 v247, a67\n"
    "v_cvt_pk_bf16_f32 v228, v244, v245\n"
    "v_cvt_pk_bf16_f32 v229, v246, v247\n"
    "v_accvgpr_read_b32 v248, a68\n"
    "v_accvgpr_read_b32 v249, a69\n"
    "v_accvgpr_read_b32 v250, a70\n"
    "v_accvgpr_read_b32 v251, a71\n"
    "v_cvt_pk_bf16_f32 v230, v248, v249\n"
    "v_cvt_pk_bf16_f32 v231, v250, v251\n"
    "s_nop 1\n"
    "v_permlane16_swap_b32 v228, v230\n"
    "v_permlane16_swap_b32 v229, v231\n"
    "s_add_u32 s69, s66, 143360\n"
    "buffer_store_dwordx4 v[228:231], v243, s[56:59], s69 offen offset:0 sc1\n"
    "v_accvgpr_read_b32 v244, a48\n"
    "v_accvgpr_read_b32 v245, a49\n"
    "v_accvgpr_read_b32 v246, a50\n"
    "v_accvgpr_read_b32 v247, a51\n"
    "v_cvt_pk_bf16_f32 v232, v244, v245\n"
    "v_cvt_pk_bf16_f32 v233, v246, v247\n"
    "v_accvgpr_read_b32 v248, a52\n"
    "v_accvgpr_read_b32 v249, a53\n"
    "v_accvgpr_read_b32 v250, a54\n"
    "v_accvgpr_read_b32 v251, a55\n"
    "v_cvt_pk_bf16_f32 v234, v248, v249\n"
    "v_cvt_pk_bf16_f32 v235, v250, v251\n"
    "s_nop 1\n"
    "v_permlane16_swap_b32 v232, v234\n"
    "v_permlane16_swap_b32 v233, v235\n"
    "s_add_u32 s69, s66, 143360\n"
    "buffer_store_dwordx4 v[232:235], v243, s[56:59], s69 offen offset:2048 sc1\n"
    "v_accvgpr_read_b32 v244, a16\n"
    "v_accvgpr_read_b32 v245, a17\n"
    "v_accvgpr_read_b32 v246, a18\n"
    "v_accvgpr_read_b32 v247, a19\n"
    "v_cvt_pk_bf16_f32 v228, v244, v245\n"
    "v_cvt_pk_bf16_f32 v229, v246, v247\n"
    "v_accvgpr_read_b32 v248, a20\n"
    "v_accvgpr_read_b32 v249, a21\n"
    "v_accvgpr_read_b32 v250, a22\n"
    "v_accvgpr_read_b32 v251, a23\n"
    "v_cvt_pk_bf16_f32 v230, v248, v249\n"
    "v_cvt_pk_bf16_f32 v231, v250, v251\n"
    "s_nop 1\n"
    "v_permlane16_swap_b32 v228, v230\n"
    "v_permlane16_swap_b32 v229, v231\n"
    "s_add_u32 s69, s66, 147456\n"
    "buffer_store_dwordx4 v[228:231], v243, s[56:59], s69 offen offset:0 sc1\n"
    "v_cvt_pk_bf16_f32 v228, v160, v161\n"
    "v_cvt_pk_bf16_f32 v229, v162, v163\n"
    "v_cvt_pk_bf16_f32 v230, v184, v185\n"
    "v_cvt_pk_bf16_f32 v231, v186, v187\n"
    "s_nop 1\n"
    "v_permlane16_swap_b32 v228, v230\n"
    "v_permlane16_swap_b32 v229, v231\n"
    "s_add_u32 s69, s66, 131072\n"
    "buffer_store_dwordx4 v[228:231], v243, s[56:59], s69 offen offset:1024 sc1\n"
    "v_cvt_pk_bf16_f32 v232, v164, v165\n"
    "v_cvt_pk_bf16_f32 v233, v166, v167\n"
    "v_cvt_pk_bf16_f32 v234, v188, v189\n"
    "v_cvt_pk_bf16_f32 v235, v190, v191\n"
    "s_nop 1\n"
    "v_permlane16_swap_b32 v232, v234\n"
    "v_permlane16_swap_b32 v233, v235\n"
    "s_add_u32 s69, s66, 131072\n"
    "buffer_store_dwordx4 v[232:235], v243, s[56:59], s69 offen offset:3072 sc1\n"
    "v_cvt_pk_bf16_f32 v228, v168, v169\n"
    "v_cvt_pk_bf16_f32 v229, v170, v171\n"
    "v_cvt_pk_bf16_f32 v230, v192, v193\n"
    "v_cvt_pk_bf16_f32 v231, v194, v195\n"
    "s_nop 1\n"
    "v_permlane16_swap_b32 v228, v230\n"
    "v_permlane16_swap_b32 v229, v231\n"
    "s_add_u32 s69, s66, 135168\n"
    "buffer_store_dwordx4 v[228:231], v243, s[56:59], s69 offen offset:1024 sc1\n"
    "s_waitcnt vmcnt(12)\n"
    "s_barrier\n"
    "s_cmp_lg_u32 s60, 0\n"
    "s_cbranch_scc1 LABD_%=_11\n"
    "s_add_u32 s98, s99, 2048\n"
    "v_mov_b32 v249, s98\n"
    "v_mov_b32 v252, s4\n"
    "v_mov_b32 v253, s5\n"
    "s_mov_b64 s[10:11], exec\n"
    "s_mov_b64 exec, 1\n"
    "global_store_dwordx2 v249, v[252:253], s[6:7] sc1\n"
    "s_mov_b64 exec, s[10:11]\n"
    "LABD_%=_11:\n"
    "v_cvt_pk_bf16_f32 v232, v172, v173\n"
    "v_cvt_pk_bf16_f32 v233, v174, v175\n"
    "v_cvt_pk_bf16_f32 v234, v196, v197\n"
    "v_cvt_pk_bf16_f32 v235, v198, v199\n"
    "s_nop 1\n"
    "v_permlane16_swap_b32 v232, v234\n"
    "v_permlane16_swap_b32 v233, v235\n"
    "s_add_u32 s69, s66, 135168\n"
    "buffer_store_dwordx4 v[232:235], v243, s[56:59], s69 offen offset:3072 sc1\n"
    "v_cvt_pk_bf16_f32 v228, v176, v177\n"
    "v_cvt_pk_bf16_f32 v229, v178, v179\n"
    "v_cvt_pk_bf16_f32 v230, v200, v201\n"
    "v_cvt_pk_bf16_f32 v231, v202, v203\n"
    "s_nop 1\n"
    "v_permlane16_swap_b32 v228, v230\n"
    "v_permlane16_swap_b32 v229, v231\n"
    "s_add_u32 s69, s66, 139264\n"
    "buffer_store_dwordx4 v[228:231], v243, s[56:59], s69 offen offset:1024 sc1\n"
    "v_cvt_pk_bf16_f32 v232, v180, v181\n"
    "v_cvt_pk_bf16_f32 v233, v182, v183\n"
    "v_cvt_pk_bf16_f32 v234, v204, v205\n"
    "v_cvt_pk_bf16_f32 v235, v206, v207\n"
    "s_nop 1\n"
    "v_permlane16_swap_b32 v232, v234\n"
    "v_permlane16_swap_b32 v233, v235\n"
    "s_add_u32 s69, s66, 139264\n"
    "buffer_store_dwordx4 v[232:235], v243, s[56:59], s69 offen offset:3072 sc1\n"
    "v_accvgpr_read_b32 v244, a72\n"
    "v_accvgpr_read_b32 v245, a73\n"
    "v_accvgpr_read_b32 v246, a74\n"
    "v_accvgpr_read_b32 v247, a75\n"
    "v_cvt_pk_bf16_f32 v228, v244, v245\n"
    "v_cvt_pk_bf16_f32 v229, v246, v247\n"
    "v_accvgpr_read_b32 v248, a76\n"
    "v_accvgpr_read_b32 v249, a77\n"
    "v_accvgpr_read_b32 v250, a78\n"
    "v_accvgpr_read_b32 v251, a79\n"
    "v_cvt_pk_bf16_f32 v230, v248, v249\n"
    "v_cvt_pk_bf16_f32 v231, v250, v251\n"
    "s_nop 1\n"
    "v_permlane16_swap_b32 v228, v230\n"
    "v_permlane16_swap_b32 v229, v231\n"
    "s_add_u32 s69, s66, 143360\n"
    "buffer_store_dwordx4 v[228:231], v243, s[56:59], s69 offen offset:1024 sc1\n"
    "v_accvgpr_read_b32 v244, a56\n"
    "v_accvgpr_read_b32 v245, a57\n"
    "v_accvgpr_read_b32 v246, a58\n"
    "v_accvgpr_read_b32 v247, a59\n"
    "v_cvt_pk_bf16_f32 v232, v244, v245\n"
    "v_cvt_pk_bf16_f32 v233, v246, v247\n"
    "v_accvgpr_read_b32 v248, a60\n"
    "v_accvgpr_read_b32 v249, a61\n"
    "v_accvgpr_read_b32 v250, a62\n"
    "v_accvgpr_read_b32 v251, a63\n"
    "v_cvt_pk_bf16_f32 v234, v248, v249\n"
    "v_cvt_pk_bf16_f32 v235, v250, v251\n"
    "s_nop 1\n"
    "v_permlane16_swap_b32 v232, v234\n"
    "v_permlane16_swap_b32 v233, v235\n"
    "s_add_u32 s69, s66, 143360\n"
    "buffer_store_dwordx4 v[232:235], v243, s[56:59], s69 offen offset:3072 sc1\n"
    "v_accvgpr_read_b32 v244, a24\n"
    "v_accvgpr_read_b32 v245, a25\n"
    "v_accvgpr_read_b32 v246, a26\n"
    "v_accvgpr_read_b32 v247, a27\n"
    "v_cvt_pk_bf16_f32 v228, v244, v245\n"
    "v_cvt_pk_bf16_f32 v229, v246, v247\n"
    "v_accvgpr_read_b32 v248, a28\n"
    "v_accvgpr_read_b32 v249, a29\n"
    "v_accvgpr_read_b32 v250, a30\n"
    "v_accvgpr_read_b32 v251, a31\n"
    "v_cvt_pk_bf16_f32 v230, v248, v249\n"
    "v_cvt_pk_bf16_f32 v231, v250, v251\n"
    "s_nop 1\n"
    "v_permlane16_swap_b32 v228, v230\n"
    "v_permlane16_swap_b32 v229, v231\n"
    "s_add_u32 s69, s66, 147456\n"
    "buffer_store_dwordx4 v[228:231], v243, s[56:59], s69 offen offset:1024 sc1\n"
    "s_add_u32 s99, s99, 4096\n"
    "LP1E_%=:\n"
    "s_load_dwordx16 s[16:31], %1, 64\n"
    "s_load_dwordx4 s[36:39], %1, 128\n"
    "s_load_dwordx2 s[42:43], %1, 152\n"
    "v_and_b32 v240, 63, v255\n"
    "v_and_b32 v241, 15, v240\n"
    "v_lshrrev_b32 v242, 4, v240\n"
    "s_and_b32 s63, s8, 1\n"
    "s_lshl_b32 s63, s63, 1\n"
    "s_lshr_b32 s71, s8, 1\n"
    "s_and_b32 s71, s71, 1\n"
    "s_or_b32 s63, s63, s71\n"
    "s_lshr_b32 s71, s8, 2\n"
    "s_and_b32 s71, s71, 1\n"
    "s_lshr_b32 s72, s8, 3\n"
    "s_cmp_ge_u32 s63, 2\n"
    "s_cbranch_scc1 LROLED_%=\n"
    "s_lshl_b32 s71, s71, 3\n"
    "s_lshr_b32 s73, s72, 2\n"
    "s_add_u32 s64, s71, s73\n"
    "s_and_b32 s65, s72, 3\n"
    "s_lshl_b32 s79, s64, 2\n"
    "s_add_u32 s79, s79, s65\n"
    "s_branch LROLEE_%=\n"
    "LROLED_%=:\n"
    "s_lshl_b32 s71, s71, 2\n"
    "s_lshr_b32 s73, s72, 3\n"
    "s_add_u32 s64, s71, s73\n"
    "s_and_b32 s65, s72, 7\n"
    "s_lshl_b32 s79, s64, 3\n"
    "s_add_u32 s79, s79, s65\n"
    "LROLEE_%=:\n"
    "s_waitcnt lgkmcnt(0)\n"
    "s_cmp_ge_u32 s63, 2\n"
    "s_cbranch_scc1 LP2D_%=\n"
    "v_lshrrev_b32 v243, 3, v240\n"
    "v_and_b32 v244, 7, v240\n"
    "v_xor_b32 v244, v244, v243\n"
    "v_lshlrev_b32 v244, 4, v244\n"
    "v_lshl_add_u32 v245, v243, 12, v244\n"
    "s_lshl_b32 s71, s64, 19\n"
    "s_lshl_b32 s72, s60, 17\n"
    "s_add_u32 s71, s71, s72\n"
    "s_add_u32 s72, s71, 0\n"
    "v_add_u32 v160, s72, v245\n"
    "s_add_u32 s72, s71, 31744\n"
    "v_add_u32 v161, s72, v245\n"
    "s_add_u32 s72, s71, 63488\n"
    "v_add_u32 v162, s72, v245\n"
    "s_add_u32 s72, s71, 95232\n"
    "v_add_u32 v163, s72, v245\n"
    "s_lshl_b32 s71, s65, 15\n"
    "s_lshl_b32 s72, s60, 13\n"
    "s_add_u32 s71, s71, s72\n"
    "v_lshlrev_b32 v243, 4, v240\n"
    "v_add_u32 v168, s71, v243\n"
    "v_add_u32 v169, 4096, v168\n"
    "s_mov_b32 s66, 0\n"
    "s_mov_b32 s67, 0\n"
    "s_mov_b32 s69, 32\n"
    "s_cmp_eq_u32 s63, 0\n"
    "s_cselect_b32 s70, s16, s18\n"
    "s_cselect_b32 s71, s17, s19\n"
    "s_cselect_b32 s72, s20, s22\n"
    "s_cselect_b32 s73, s21, s23\n"
    "s_mov_b32 s44, s70\n"
    "s_and_b32 s45, s71, 0xffff\n"
    "s_mov_b32 s46, 8388608\n"
    "s_mov_b32 s47, 0x20000\n"
    "s_mov_b32 s48, s72\n"
    "s_and_b32 s49, s73, 0xffff\n"
    "s_mov_b32 s50, 4194304\n"
    "s_mov_b32 s51, 0x20000\n"
    "s_lshl_b32 s74, s60, 12\n"
    "s_mov_b32 s78, 5\n"
    "s_mov_b32 s56, s48\n"
    "s_mov_b32 s57, s49\n"
    "s_mov_b32 s58, 0\n"
    "s_mov_b32 s59, 0x20000\n"
    "v_and_b32 v243, 7, v241\n"
    "v_xor_b32 v243, v242, v243\n"
    "v_xor_b32 v244, 4, v243\n"
    "v_lshlrev_b32 v245, 7, v241\n"
    "v_lshl_add_u32 v128, v243, 4, v245\n"
    "v_lshl_add_u32 v129, v244, 4, v245\n"
    "v_add_u32 v130, 49152, v128\n"
    "v_add_u32 v131, 49152, v129\n"
    "v_lshlrev_b32 v251, 4, v240\n"
    "v_and_b32 v200, 7, v241\n"
    "v_lshrrev_b32 v201, 1, v242\n"
    "v_xor_b32 v200, v200, v201\n"
    "v_and_b32 v201, 1, v242\n"
    "v_lshlrev_b32 v201, 3, v201\n"
    "v_lshl_add_u32 v201, v241, 7, v201\n"
    "s_lshl_b32 s70, s60, 13\n"
    "s_add_u32 s70, s70, 98304\n"
    "v_add_u32 v201, s70, v201\n"
    "v_xor_b32 v202, 0, v200\n"
    "v_lshl_add_u32 v244, v202, 4, v201\n"
    "v_xor_b32 v202, 2, v200\n"
    "v_lshl_add_u32 v245, v202, 4, v201\n"
    "v_xor_b32 v202, 4, v200\n"
    "v_lshl_add_u32 v246, v202, 4, v201\n"
    "v_xor_b32 v202, 6, v200\n"
    "v_lshl_add_u32 v247, v202, 4, v201\n"
    "v_lshrrev_b32 v200, 3, v240\n"
    "v_and_b32 v201, 7, v240\n"
    "v_xor_b32 v201, v201, v200\n"
    "v_lshlrev_b32 v201, 4, v201\n"
    "v_lshl_add_u32 v248, v200, 7, v201\n"
    "v_add_u32 v248, s70, v248\n"
    "s_mov_b32 s56, s28\n"
    "s_and_b32 s57, s29, 0xffff\n"
    "s_mov_b32 s58, 4194304\n"
    "s_mov_b32 s59, 0x20000\n"
    "v_lshrrev_b32 v254, 3, v240\n"
    "v_lshlrev_b32 v254, 11, v254\n"
    "v_and_b32 v200, 7, v240\n"
    "v_lshl_add_u32 v254, v200, 4, v254\n"
    "s_lshl_b32 s70, s64, 18\n"
    "s_lshl_b32 s71, s63, 17\n"
    "s_add_u32 s70, s70, s71\n"
    "s_lshl_b32 s71, s65, 9\n"
    "s_add_u32 s70, s70, s71\n"
    "s_lshl_b32 s71, s60, 7\n"
    "s_add_u32 s70, s70, s71\n"
    "v_add_u32 v254, s70, v254\n"
    "s_mov_b32 s73, 0\n"
    "buffer_load_dwordx4 v[208:211], v254, s[56:59], s73 offen\n"
    "s_mov_b32 s73, 16384\n"
    "buffer_load_dwordx4 v[212:215], v254, s[56:59], s73 offen\n"
    "s_mov_b32 s73, 32768\n"
    "buffer_load_dwordx4 v[216:219], v254, s[56:59], s73 offen\n"
    "s_mov_b32 s73, 49152\n"
    "buffer_load_dwordx4 v[220:223], v254, s[56:59], s73 offen\n"
    "s_mov_b32 s73, 65536\n"
    "buffer_load_dwordx4 v[224:227], v254, s[56:59], s73 offen\n"
    "s_mov_b32 s73, 81920\n"
    "buffer_load_dwordx4 v[228:231], v254, s[56:59], s73 offen\n"
    "s_mov_b32 s73, 98304\n"
    "buffer_load_dwordx4 v[232:235], v254, s[56:59], s73 offen\n"
    "s_mov_b32 s73, 114688\n"
    "buffer_load_dwordx4 v[236:239], v254, s[56:59], s73 offen\n"
    "v_mov_b32 v0, 0\n"
    "v_mov_b32 v1, 0\n"
    "v_mov_b32 v2, 0\n"
    "v_mov_b32 v3, 0\n"
    "v_mov_b32 v4, 0\n"
    "v_mov_b32 v5, 0\n"
    "v_mov_b32 v6, 0\n"
    "v_mov_b32 v7, 0\n"
    "v_mov_b32 v8, 0\n"
    "v_mov_b32 v9, 0\n"
    "v_mov_b32 v10, 0\n"
    "v_mov_b32 v11, 0\n"
    "v_mov_b32 v12, 0\n"
    "v_mov_b32 v13, 0\n"
    "v_mov_b32 v14, 0\n"
    "v_mov_b32 v15, 0\n"
    "v_mov_b32 v16, 0\n"
    "v_mov_b32 v17, 0\n"
    "v_mov_b32 v18, 0\n"
    "v_mov_b32 v19, 0\n"
    "v_mov_b32 v20, 0\n"
    "v_mov_b32 v21, 0\n"
    "v_mov_b32 v22, 0\n"
    "v_mov_b32 v23, 0\n"
    "v_mov_b32 v24, 0\n"
    "v_mov_b32 v25, 0\n"
    "v_mov_b32 v26, 0\n"
    "v_mov_b32 v27, 0\n"
    "v_mov_b32 v28, 0\n"
    "v_mov_b32 v29, 0\n"
    "v_mov_b32 v30, 0\n"
    "v_mov_b32 v31, 0\n"
    "v_mov_b32 v32, 0\n"
    "v_mov_b32 v33, 0\n"
    "v_mov_b32 v34, 0\n"
    "v_mov_b32 v35, 0\n"
    "v_mov_b32 v36, 0\n"
    "v_mov_b32 v37, 0\n"
    "v_mov_b32 v38, 0\n"
    "v_mov_b32 v39, 0\n"
    "v_mov_b32 v40, 0\n"
    "v_mov_b32 v41, 0\n"
    "v_mov_b32 v42, 0\n"
    "v_mov_b32 v43, 0\n"
    "v_mov_b32 v44, 0\n"
    "v_mov_b32 v45, 0\n"
    "v_mov_b32 v46, 0\n"
    "v_mov_b32 v47, 0\n"
    "v_mov_b32 v48, 0\n"
    "v_mov_b32 v49, 0\n"
    "v_mov_b32 v50, 0\n"
    "v_mov_b32 v51, 0\n"
    "v_mov_b32 v52, 0\n"
    "v_mov_b32 v53, 0\n"
    "v_mov_b32 v54, 0\n"
    "v_mov_b32 v55, 0\n"
    "v_mov_b32 v56, 0\n"
    "v_mov_b32 v57, 0\n"
    "v_mov_b32 v58, 0\n"
    "v_mov_b32 v59, 0\n"
    "v_mov_b32 v60, 0\n"
    "v_mov_b32 v61, 0\n"
    "v_mov_b32 v62, 0\n"
    "v_mov_b32 v63, 0\n"
    "v_mov_b32 v64, 0\n"
    "v_mov_b32 v65, 0\n"
    "v_mov_b32 v66, 0\n"
    "v_mov_b32 v67, 0\n"
    "v_mov_b32 v68, 0\n"
    "v_mov_b32 v69, 0\n"
    "v_mov_b32 v70, 0\n"
    "v_mov_b32 v71, 0\n"
    "v_mov_b32 v72, 0\n"
    "v_mov_b32 v73, 0\n"
    "v_mov_b32 v74, 0\n"
    "v_mov_b32 v75, 0\n"
    "v_mov_b32 v76, 0\n"
    "v_mov_b32 v77, 0\n"
    "v_mov_b32 v78, 0\n"
    "v_mov_b32 v79, 0\n"
    "v_mov_b32 v80, 0\n"
    "v_mov_b32 v81, 0\n"
    "v_mov_b32 v82, 0\n"
    "v_mov_b32 v83, 0\n"
    "v_mov_b32 v84, 0\n"
    "v_mov_b32 v85, 0\n"
    "v_mov_b32 v86, 0\n"
    "v_mov_b32 v87, 0\n"
    "v_mov_b32 v88, 0\n"
    "v_mov_b32 v89, 0\n"
    "v_mov_b32 v90, 0\n"
    "v_mov_b32 v91, 0\n"
    "v_mov_b32 v92, 0\n"
    "v_mov_b32 v93, 0\n"
    "v_mov_b32 v94, 0\n"
    "v_mov_b32 v95, 0\n"
    "v_mov_b32 v96, 0\n"
    "v_mov_b32 v97, 0\n"
    "v_mov_b32 v98, 0\n"
    "v_mov_b32 v99, 0\n"
    "v_mov_b32 v100, 0\n"
    "v_mov_b32 v101, 0\n"
    "v_mov_b32 v102, 0\n"
    "v_mov_b32 v103, 0\n"
    "v_mov_b32 v104, 0\n"
    "v_mov_b32 v105, 0\n"
    "v_mov_b32 v106, 0\n"
    "v_mov_b32 v107, 0\n"
    "v_mov_b32 v108, 0\n"
    "v_mov_b32 v109, 0\n"
    "v_mov_b32 v110, 0\n"
    "v_mov_b32 v111, 0\n"
    "v_mov_b32 v112, 0\n"
    "v_mov_b32 v113, 0\n"
    "v_mov_b32 v114, 0\n"
    "v_mov_b32 v115, 0\n"
    "v_mov_b32 v116, 0\n"
    "v_mov_b32 v117, 0\n"
    "v_mov_b32 v118, 0\n"
    "v_mov_b32 v119, 0\n"
    "v_mov_b32 v120, 0\n"
    "v_mov_b32 v121, 0\n"
    "v_mov_b32 v122, 0\n"
    "v_mov_b32 v123, 0\n"
    "v_mov_b32 v124, 0\n"
    "v_mov_b32 v125, 0\n"
    "v_mov_b32 v126, 0\n"
    "v_mov_b32 v127, 0\n"
    "s_waitcnt vmcnt(0)\n"
    "s_barrier\n"
    "s_cmp_lg_u32 s60, 0\n"
    "s_cbranch_scc1 LPP_%=_12\n"
    "v_mov_b32 v249, s99\n"
    "v_mov_b32 v252, s4\n"
    "v_mov_b32 v253, s5\n"
    "s_mov_b64 s[10:11], exec\n"
    "s_mov_b64 exec, 1\n"
    "global_store_dwordx2 v249, v[252:253], s[6:7] sc1\n"
    "s_mov_b64 exec, s[10:11]\n"
    "s_sub_u32 s99, s99, 2048\n"
    "v_mov_b32 v249, s99\n"
    "v_mov_b32 v252, s4\n"
    "v_mov_b32 v253, s5\n"
    "s_mov_b64 s[10:11], exec\n"
    "s_mov_b64 exec, 1\n"
    "global_store_dwordx2 v249, v[252:253], s[6:7] sc1\n"
    "s_mov_b64 exec, s[10:11]\n"
    "LPP_%=_12:\n"
    "s_mul_i32 s70, s65, 16\n"
    "s_add_u32 s71, s70, 15\n"
    "s_cmp_eq_u32 s65, 1\n"
    "s_cselect_b32 s71, 28, s71\n"
    "s_cmp_eq_u32 s65, 2\n"
    "s_cselect_b32 s70, 28, s70\n"
    "s_cselect_b32 s71, 30, s71\n"
    "s_cmp_eq_u32 s65, 3\n"
    "s_cselect_b32 s70, 30, s70\n"
    "s_cselect_b32 s71, 31, s71\n"
    "v_and_b32 v200, 7, v240\n"
    "v_add_u32 v200, s70, v200\n"
    "v_lshrrev_b32 v201, 3, v240\n"
    "v_lshlrev_b32 v201, 5, v201\n"
    "v_min_u32 v202, s71, v200\n"
    "v_add_u32 v249, v201, v202\n"
    "v_lshlrev_b32 v249, 3, v249\n"
    "v_add_u32 v249, 2048, v249\n"
    "v_add_u32 v200, 8, v200\n"
    "v_min_u32 v202, s71, v200\n"
    "v_add_u32 v203, v201, v202\n"
    "v_lshlrev_b32 v203, 3, v203\n"
    "v_add_u32 v203, 2048, v203\n"
    "s_cmp_lg_u32 s60, 0\n"
    "s_cbranch_scc1 LPA_%=_13\n"
    "global_load_dwordx2 v[252:253], v249, s[6:7] sc1\n"
    "global_load_dwordx2 v[204:205], v203, s[6:7] sc1\n"
    "LPA_%=_13:\n"
    "s_add_u32 m0, s74, 0\n"
    "s_nop 0\n"
    "buffer_load_dwordx4 v160, s[44:47], s66 offen offset:0  lds\n"
    "buffer_load_dwordx4 v161, s[44:47], s66 offen offset:1024  lds\n"
    "buffer_load_dwordx4 v162, s[44:47], s66 offen offset:2048  lds\n"
    "buffer_load_dwordx4 v163, s[44:47], s66 offen offset:3072  lds\n"
    "s_add_u32 s66, s66, 256\n"
    "s_cmp_eq_u32 s66, 4096\n"
    "s_cselect_b32 s66, 128, s66\n"
    "s_add_u32 m0, s74, 16384\n"
    "s_nop 0\n"
    "buffer_load_dwordx4 v160, s[44:47], s66 offen offset:0  lds\n"
    "buffer_load_dwordx4 v161, s[44:47], s66 offen offset:1024  lds\n"
    "buffer_load_dwordx4 v162, s[44:47], s66 offen offset:2048  lds\n"
    "buffer_load_dwordx4 v163, s[44:47], s66 offen offset:3072  lds\n"
    "s_add_u32 s66, s66, 256\n"
    "s_cmp_eq_u32 s66, 4096\n"
    "s_cselect_b32 s66, 128, s66\n"
    "s_mov_b32 s58, 0\n"
    "s_cmp_lg_u32 s60, 0\n"
    "s_cbranch_scc1 LPB_%=_13\n"
    "s_waitcnt vmcnt(8)\n"
    "v_cmp_ne_u64 vcc, s[4:5], v[252:253]\n"
    "v_cmp_ne_u64 s[10:11], s[4:5], v[204:205]\n"
    "s_nop 4\n"
    "s_or_b64 vcc, vcc, s[10:11]\n"
    "s_cmp_eq_u64 vcc, 0\n"
    "s_cselect_b32 s70, 1, 0\n"
    "v_mov_b32 v200, s70\n"
    "v_mov_b32 v201, 131072\n"
    "ds_write_b32 v201, v200\n"
    "s_waitcnt lgkmcnt(0)\n"
    "LPB_%=_13:\n"
    "s_barrier\n"
    "v_mov_b32 v201, 131072\n"
    "ds_read_b32 v200, v201\n"
    "s_waitcnt lgkmcnt(0)\n"
    "v_readfirstlane_b32 s70, v200\n"
    "s_nop 3\n"
    "s_cmp_eq_u32 s70, 1\n"
    "s_cbranch_scc1 LPR_%=_13\n"
    "s_cmp_lg_u32 s60, 0\n"
    "s_cbranch_scc1 LPC_%=_13\n"
    "s_mov_b32 s3, 0\n"
    "global_load_dwordx2 v[252:253], v249, s[6:7] sc1\n"
    "global_load_dwordx2 v[204:205], v203, s[6:7] sc1\n"
    "LW2_%=_14:\n"
    "global_load_dwordx2 v[206:207], v249, s[6:7] sc1\n"
    "global_load_dwordx2 v[196:197], v203, s[6:7] sc1\n"
    "s_waitcnt vmcnt(2)\n"
    "v_cmp_ne_u64 vcc, s[4:5], v[252:253]\n"
    "v_cmp_ne_u64 s[10:11], s[4:5], v[204:205]\n"
    "s_nop 4\n"
    "s_or_b64 vcc, vcc, s[10:11]\n"
    "s_cbranch_vccz LW2D_%=_14\n"
    "global_load_dwordx2 v[252:253], v249, s[6:7] sc1\n"
    "global_load_dwordx2 v[204:205], v203, s[6:7] sc1\n"
    "s_waitcnt vmcnt(2)\n"
    "v_cmp_ne_u64 vcc, s[4:5], v[206:207]\n"
    "v_cmp_ne_u64 s[10:11], s[4:5], v[196:197]\n"
    "s_nop 4\n"
    "s_or_b64 vcc, vcc, s[10:11]\n"
    "s_cbranch_vccz LW2D_%=_14\n"
    "s_add_u32 s3, s3, 1\n"
    "s_cmp_lt_u32 s3, 0x10000\n"
    "s_cbranch_scc1 LW2_%=_14\n"
    "LW2D_%=_14:\n"
    "LPC_%=_13:\n"
    "s_barrier\n"
    "LPR_%=_13:\n"
    "v_add_u32 v249, 2048, v249\n"
    "v_add_u32 v203, 2048, v203\n"
    "s_add_u32 m0, s74, 32768\n"
    "s_nop 0\n"
    "buffer_load_dwordx4 v160, s[44:47], s66 offen offset:0  lds\n"
    "buffer_load_dwordx4 v161, s[44:47], s66 offen offset:1024  lds\n"
    "buffer_load_dwordx4 v162, s[44:47], s66 offen offset:2048  lds\n"
    "buffer_load_dwordx4 v163, s[44:47], s66 offen offset:3072  lds\n"
    "s_add_u32 s66, s66, 256\n"
    "s_cmp_eq_u32 s66, 4096\n"
    "s_cselect_b32 s66, 128, s66\n"
    "buffer_load_dwordx4 a[64:67], v168, s[48:51], s67 offen offset:0 sc1\n"
    "buffer_load_dwordx4 a[68:71], v168, s[48:51], s67 offen offset:1024 sc1\n"
    "buffer_load_dwordx4 a[72:75], v168, s[48:51], s67 offen offset:2048 sc1\n"
    "buffer_load_dwordx4 a[76:79], v168, s[48:51], s67 offen offset:3072 sc1\n"
    "buffer_load_dwordx4 a[80:83], v169, s[48:51], s67 offen offset:0 sc1\n"
    "buffer_load_dwordx4 a[84:87], v169, s[48:51], s67 offen offset:1024 sc1\n"
    "buffer_load_dwordx4 a[88:91], v169, s[48:51], s67 offen offset:2048 sc1\n"
    "buffer_load_dwordx4 a[92:95], v169, s[48:51], s67 offen offset:3072 sc1\n"
    "s_add_u32 s67, s67, 262144\n"
    "s_cmp_eq_u32 s67, 4194304\n"
    "s_cselect_b32 s67, 131072, s67\n"
    "s_waitcnt vmcnt(12)\n"
    "s_barrier\n"
    "ds_read_b128 a[0:3], v128 offset:0\n"
    "ds_read_b128 a[4:7], v129 offset:0\n"
    "ds_read_b128 a[8:11], v128 offset:2048\n"
    "ds_read_b128 a[12:15], v129 offset:2048\n"
    "ds_read_b128 a[16:19], v128 offset:4096\n"
    "ds_read_b128 a[20:23], v129 offset:4096\n"
    "ds_read_b128 a[24:27], v128 offset:6144\n"
    "ds_read_b128 a[28:31], v129 offset:6144\n"
    "ds_read_b128 a[32:35], v128 offset:8192\n"
    "ds_read_b128 a[36:39], v129 offset:8192\n"
    "ds_read_b128 a[40:43], v128 offset:10240\n"
    "ds_read_b128 a[44:47], v129 offset:10240\n"
    "ds_read_b128 a[48:51], v128 offset:12288\n"
    "ds_read_b128 a[52:55], v129 offset:12288\n"
    "s_add_u32 m0, s74, 49152\n"
    "s_nop 0\n"
    "buffer_load_dwordx4 v160, s[44:47], s66 offen offset:0  lds\n"
    "buffer_load_dwordx4 v161, s[44:47], s66 offen offset:1024  lds\n"
    "buffer_load_dwordx4 v162, s[44:47], s66 offen offset:2048  lds\n"
    "buffer_load_dwordx4 v163, s[44:47], s66 offen offset:3072  lds\n"
    "s_add_u32 s66, s66, 256\n"
    "s_cmp_eq_u32 s66, 4096\n"
    "s_cselect_b32 s66, 128, s66\n"
    "buffer_load_dwordx4 a[96:99], v168, s[48:51], s67 offen offset:0 sc1\n"
    "buffer_load_dwordx4 a[100:103], v168, s[48:51], s67 offen offset:1024 sc1\n"
    "buffer_load_dwordx4 a[104:107], v168, s[48:51], s67 offen offset:2048 sc1\n"
    "buffer_load_dwordx4 a[108:111], v168, s[48:51], s67 offen offset:3072 sc1\n"
    "buffer_load_dwordx4 a[112:115], v169, s[48:51], s67 offen offset:0 sc1\n"
    "buffer_load_dwordx4 a[116:119], v169, s[48:51], s67 offen offset:1024 sc1\n"
    "buffer_load_dwordx4 a[120:123], v169, s[48:51], s67 offen offset:2048 sc1\n"
    "buffer_load_dwordx4 a[124:127], v169, s[48:51], s67 offen offset:3072 sc1\n"
    "s_add_u32 s67, s67, 262144\n"
    "s_cmp_eq_u32 s67, 4194304\n"
    "s_cselect_b32 s67, 131072, s67\n"
    "s_add_u32 m0, s74, 65536\n"
    "s_nop 0\n"
    "buffer_load_dwordx4 v160, s[44:47], s66 offen offset:0  lds\n"
    "buffer_load_dwordx4 v161, s[44:47], s66 offen offset:1024  lds\n"
    "buffer_load_dwordx4 v162, s[44:47], s66 offen offset:2048  lds\n"
    "buffer_load_dwordx4 v163, s[44:47], s66 offen offset:3072  lds\n"
    "s_add_u32 s66, s66, 256\n"
    "s_cmp_eq_u32 s66, 4096\n"
    "s_cselect_b32 s66, 128, s66\n"
    "buffer_load_dwordx4 a[128:131], v168, s[48:51], s67 offen offset:0 sc1\n"
    "buffer_load_dwordx4 a[132:135], v168, s[48:51], s67 offen offset:1024 sc1\n"
    "buffer_load_dwordx4 a[136:139], v168, s[48:51], s67 offen offset:2048 sc1\n"
    "buffer_load_dwordx4 a[140:143], v168, s[48:51], s67 offen offset:3072 sc1\n"
    "buffer_load_dwordx4 a[144:147], v169, s[48:51], s67 offen offset:0 sc1\n"
    "buffer_load_dwordx4 a[148:151], v169, s[48:51], s67 offen offset:1024 sc1\n"
    "buffer_load_dwordx4 a[152:155], v169, s[48:51], s67 offen offset:2048 sc1\n"
    "buffer_load_dwordx4 a[156:159], v169, s[48:51], s67 offen offset:3072 sc1\n"
    "s_add_u32 s67, s67, 262144\n"
    "s_cmp_eq_u32 s67, 4194304\n"
    "s_cselect_b32 s67, 131072, s67\n"
    "s_lshr_b32 s70, s8, 3\n"
    "s_lshr_b32 s71, s8, 2\n"
    "s_and_b32 s71, s71, 1\n"
    "s_lshl_b32 s71, s71, 5\n"
    "s_add_u32 s70, s70, s71\n"
    "s_lshl_b32 s70, s70, 2\n"
    "s_add_u32 s70, s70, s60\n"
    "s_lshl_b32 s70, s70, 6\n"
    "v_add_u32 v200, s70, v240\n"
    "s_mov_b32 s52, s44\n"
    "s_mov_b32 s53, s45\n"
    "s_mov_b32 s55, 0x20000\n"
    "s_mov_b32 s54, 8388608\n"
    "v_lshrrev_b32 v201, 3, v200\n"
    "v_lshlrev_b32 v201, 12, v201\n"
    "v_and_b32 v200, 7, v200\n"
    "v_add_u32 v200, 16, v200\n"
    "v_lshl_add_u32 v200, v200, 7, v201\n"
    "buffer_load_dword v250, v200, s[52:55], 0 offen offset:1024\n"
    "s_waitcnt lgkmcnt(0)\n"
    "s_mov_b32 s68, 5\n"
    "s_branch LY_pos0_%=\n"
    "LY_loop_%=:\n"
    "LY_pos0_%=:\n"
    "s_waitcnt vmcnt(24)\n"
    "s_barrier\n"
    "s_waitcnt lgkmcnt(12)\n"
    "v_mfma_f32_16x16x32_bf16 v[0:3], a[64:67], a[0:3], v[0:3]\n"
    "ds_read_b128 a[56:59], v128 offset:14336\n"
    "v_mfma_f32_16x16x32_bf16 v[0:3], a[68:71], a[4:7], v[0:3]\n"
    "ds_read_b128 a[60:63], v129 offset:14336\n"
    "v_mfma_f32_16x16x32_bf16 v[4:7], a[72:75], a[0:3], v[4:7]\n"
    "v_mfma_f32_16x16x32_bf16 v[4:7], a[76:79], a[4:7], v[4:7]\n"
    "v_mfma_f32_16x16x32_bf16 v[8:11], a[80:83], a[0:3], v[8:11]\n"
    "s_add_u32 m0, s74, 81920\n"
    "s_nop 0\n"
    "buffer_load_dwordx4 v160, s[44:47], s66 offen offset:0  lds\n"
    "v_mfma_f32_16x16x32_bf16 v[8:11], a[84:87], a[4:7], v[8:11]\n"
    "v_mfma_f32_16x16x32_bf16 v[12:15], a[88:91], a[0:3], v[12:15]\n"
    "v_mfma_f32_16x16x32_bf16 v[12:15], a[92:95], a[4:7], v[12:15]\n"
    "s_waitcnt lgkmcnt(12)\n"
    "v_mfma_f32_16x16x32_bf16 v[16:19], a[64:67], a[8:11], v[16:19]\n"
    "ds_read_b128 a[0:3], v128 offset:16384\n"
    "v_mfma_f32_16x16x32_bf16 v[16:19], a[68:71], a[12:15], v[16:19]\n"
    "buffer_load_dwordx4 v161, s[44:47], s66 offen offset:1024  lds\n"
    "v_mfma_f32_16x16x32_bf16 v[20:23], a[72:75], a[8:11], v[20:23]\n"
    "v_mfma_f32_16x16x32_bf16 v[20:23], a[76:79], a[12:15], v[20:23]\n"
    "v_mfma_f32_16x16x32_bf16 v[24:27], a[80:83], a[8:11], v[24:27]\n"
    "ds_read_b128 a[4:7], v129 offset:16384\n"
    "v_mfma_f32_16x16x32_bf16 v[24:27], a[84:87], a[12:15], v[24:27]\n"
    "v_mfma_f32_16x16x32_bf16 v[28:31], a[88:91], a[8:11], v[28:31]\n"
    "buffer_load_dwordx4 v162, s[44:47], s66 offen offset:2048  lds\n"
    "v_mfma_f32_16x16x32_bf16 v[28:31], a[92:95], a[12:15], v[28:31]\n"
    "s_waitcnt lgkmcnt(12)\n"
    "v_mfma_f32_16x16x32_bf16 v[32:35], a[64:67], a[16:19], v[32:35]\n"
    "ds_read_b128 a[8:11], v128 offset:18432\n"
    "v_mfma_f32_16x16x32_bf16 v[32:35], a[68:71], a[20:23], v[32:35]\n"
    "v_mfma_f32_16x16x32_bf16 v[36:39], a[72:75], a[16:19], v[36:39]\n"
    "v_mfma_f32_16x16x32_bf16 v[36:39], a[76:79], a[20:23], v[36:39]\n"
    "buffer_load_dwordx4 v163, s[44:47], s66 offen offset:3072  lds\n"
    "v_mfma_f32_16x16x32_bf16 v[40:43], a[80:83], a[16:19], v[40:43]\n"
    "ds_read_b128 a[12:15], v129 offset:18432\n"
    "v_mfma_f32_16x16x32_bf16 v[40:43], a[84:87], a[20:23], v[40:43]\n"
    "v_mfma_f32_16x16x32_bf16 v[44:47], a[88:91], a[16:19], v[44:47]\n"
    "v_mfma_f32_16x16x32_bf16 v[44:47], a[92:95], a[20:23], v[44:47]\n"
    "s_waitcnt lgkmcnt(12)\n"
    "v_mfma_f32_16x16x32_bf16 v[48:51], a[64:67], a[24:27], v[48:51]\n"
    "ds_read_b128 a[16:19], v128 offset:20480\n"
    "v_mfma_f32_16x16x32_bf16 v[48:51], a[68:71], a[28:31], v[48:51]\n"
    "s_add_u32 s66, s66, 256\n"
    "s_cmp_eq_u32 s66, 4096\n"
    "s_cselect_b32 s66, 128, s66\n"
    "v_mfma_f32_16x16x32_bf16 v[52:55], a[72:75], a[24:27], v[52:55]\n"
    "v_mfma_f32_16x16x32_bf16 v[52:55], a[76:79], a[28:31], v[52:55]\n"
    "v_mfma_f32_16x16x32_bf16 v[56:59], a[80:83], a[24:27], v[56:59]\n"
    "ds_read_b128 a[20:23], v129 offset:20480\n"
    "buffer_load_dwordx4 a[160:163], v168, s[48:51], s67 offen offset:0 sc1\n"
    "v_mfma_f32_16x16x32_bf16 v[56:59], a[84:87], a[28:31], v[56:59]\n"
    "v_mfma_f32_16x16x32_bf16 v[60:63], a[88:91], a[24:27], v[60:63]\n"
    "v_mfma_f32_16x16x32_bf16 v[60:63], a[92:95], a[28:31], v[60:63]\n"
    "s_waitcnt lgkmcnt(12)\n"
    "v_mfma_f32_16x16x32_bf16 v[64:67], a[64:67], a[32:35], v[64:67]\n"
    "ds_read_b128 a[24:27], v128 offset:22528\n"
    "buffer_load_dwordx4 a[164:167], v168, s[48:51], s67 offen offset:1024 sc1\n"
    "v_mfma_f32_16x16x32_bf16 v[64:67], a[68:71], a[36:39], v[64:67]\n"
    "v_mfma_f32_16x16x32_bf16 v[68:71], a[72:75], a[32:35], v[68:71]\n"
    "v_mfma_f32_16x16x32_bf16 v[68:71], a[76:79], a[36:39], v[68:71]\n"
    "v_mfma_f32_16x16x32_bf16 v[72:75], a[80:83], a[32:35], v[72:75]\n"
    "ds_read_b128 a[28:31], v129 offset:22528\n"
    "buffer_load_dwordx4 a[168:171], v168, s[48:51], s67 offen offset:2048 sc1\n"
    "v_mfma_f32_16x16x32_bf16 v[72:75], a[84:87], a[36:39], v[72:75]\n"
    "v_mfma_f32_16x16x32_bf16 v[76:79], a[88:91], a[32:35], v[76:79]\n"
    "v_mfma_f32_16x16x32_bf16 v[76:79], a[92:95], a[36:39], v[76:79]\n"
    "s_waitcnt lgkmcnt(12)\n"
    "v_mfma_f32_16x16x32_bf16 v[80:83], a[64:67], a[40:43], v[80:83]\n"
    "ds_read_b128 a[32:35], v128 offset:24576\n"
    "buffer_load_dwordx4 a[172:175], v168, s[48:51], s67 offen offset:3072 sc1\n"
    "v_mfma_f32_16x16x32_bf16 v[80:83], a[68:71], a[44:47], v[80:83]\n"
    "v_mfma_f32_16x16x32_bf16 v[84:87], a[72:75], a[40:43], v[84:87]\n"
    "v_mfma_f32_16x16x32_bf16 v[84:87], a[76:79], a[44:47], v[84:87]\n"
    "v_mfma_f32_16x16x32_bf16 v[88:91], a[80:83], a[40:43], v[88:91]\n"
    "ds_read_b128 a[36:39], v129 offset:24576\n"
    "buffer_load_dwordx4 a[176:179], v169, s[48:51], s67 offen offset:0 sc1\n"
    "v_mfma_f32_16x16x32_bf16 v[88:91], a[84:87], a[44:47], v[88:91]\n"
    "v_mfma_f32_16x16x32_bf16 v[92:95], a[88:91], a[40:43], v[92:95]\n"
    "v_mfma_f32_16x16x32_bf16 v[92:95], a[92:95], a[44:47], v[92:95]\n"
    "s_waitcnt lgkmcnt(12)\n"
    "v_mfma_f32_16x16x32_bf16 v[96:99], a[64:67], a[48:51], v[96:99]\n"
    "ds_read_b128 a[40:43], v128 offset:26624\n"
    "buffer_load_dwordx4 a[180:183], v169, s[48:51], s67 offen offset:1024 sc1\n"
    "v_mfma_f32_16x16x32_bf16 v[96:99], a[68:71], a[52:55], v[96:99]\n"
    "v_mfma_f32_16x16x32_bf16 v[100:103], a[72:75], a[48:51], v[100:103]\n"
    "v_mfma_f32_16x16x32_bf16 v[100:103], a[76:79], a[52:55], v[100:103]\n"
    "v_mfma_f32_16x16x32_bf16 v[104:107], a[80:83], a[48:51], v[104:107]\n"
    "ds_read_b128 a[44:47], v129 offset:26624\n"
    "buffer_load_dwordx4 a[184:187], v169, s[48:51], s67 offen offset:2048 sc1\n"
    "v_mfma_f32_16x16x32_bf16 v[104:107], a[84:87], a[52:55], v[104:107]\n"
    "v_mfma_f32_16x16x32_bf16 v[108:111], a[88:91], a[48:51], v[108:111]\n"
    "v_mfma_f32_16x16x32_bf16 v[108:111], a[92:95], a[52:55], v[108:111]\n"
    "s_waitcnt lgkmcnt(12)\n"
    "v_mfma_f32_16x16x32_bf16 v[112:115], a[64:67], a[56:59], v[112:115]\n"
    "ds_read_b128 a[48:51], v128 offset:28672\n"
    "buffer_load_dwordx4 a[188:191], v169, s[48:51], s67 offen offset:3072 sc1\n"
    "v_mfma_f32_16x16x32_bf16 v[112:115], a[68:71], a[60:63], v[112:115]\n"
    "v_mfma_f32_16x16x32_bf16 v[116:119], a[72:75], a[56:59], v[116:119]\n"
    "v_mfma_f32_16x16x32_bf16 v[116:119], a[76:79], a[60:63], v[116:119]\n"
    "ds_read_b128 a[52:55], v129 offset:28672\n"
    "v_mfma_f32_16x16x32_bf16 v[120:123], a[80:83], a[56:59], v[120:123]\n"
    "v_mfma_f32_16x16x32_bf16 v[120:123], a[84:87], a[60:63], v[120:123]\n"
    "s_add_u32 s67, s67, 262144\n"
    "s_cmp_eq_u32 s67, 4194304\n"
    "s_cselect_b32 s67, 131072, s67\n"
    "v_mfma_f32_16x16x32_bf16 v[124:127], a[88:91], a[56:59], v[124:127]\n"
    "s_add_u32 s78, s78, 1\n"
    "s_cmp_ge_u32 s78, s69\n"
    "s_cselect_b32 s46, 0, s46\n"
    "s_sub_u32 s70, s78, 2\n"
    "s_cmp_ge_u32 s70, s69\n"
    "s_cselect_b32 s50, 0, s50\n"
    "v_mfma_f32_16x16x32_bf16 v[124:127], a[92:95], a[60:63], v[124:127]\n"
    "s_cmp_eq_u32 s68, 4\n"
    "s_cbranch_scc0 LEOI_%=\n"
    "global_load_dwordx2 v[252:253], v249, s[6:7] sc1\n"
    "global_load_dwordx2 v[204:205], v203, s[6:7] sc1\n"
    "LEOI_%=:\n"
    "LY_pos1_%=:\n"
    "s_waitcnt vmcnt(24)\n"
    "s_barrier\n"
    "s_waitcnt lgkmcnt(12)\n"
    "v_mfma_f32_16x16x32_bf16 v[0:3], a[96:99], a[0:3], v[0:3]\n"
    "ds_read_b128 a[56:59], v128 offset:30720\n"
    "v_mfma_f32_16x16x32_bf16 v[0:3], a[100:103], a[4:7], v[0:3]\n"
    "ds_read_b128 a[60:63], v129 offset:30720\n"
    "v_mfma_f32_16x16x32_bf16 v[4:7], a[104:107], a[0:3], v[4:7]\n"
    "v_mfma_f32_16x16x32_bf16 v[4:7], a[108:111], a[4:7], v[4:7]\n"
    "v_mfma_f32_16x16x32_bf16 v[8:11], a[112:115], a[0:3], v[8:11]\n"
    "s_add_u32 m0, s74, 0\n"
    "s_nop 0\n"
    "buffer_load_dwordx4 v160, s[44:47], s66 offen offset:0  lds\n"
    "v_mfma_f32_16x16x32_bf16 v[8:11], a[116:119], a[4:7], v[8:11]\n"
    "v_mfma_f32_16x16x32_bf16 v[12:15], a[120:123], a[0:3], v[12:15]\n"
    "v_mfma_f32_16x16x32_bf16 v[12:15], a[124:127], a[4:7], v[12:15]\n"
    "s_waitcnt lgkmcnt(13)\n"
    "v_mfma_f32_16x16x32_bf16 v[16:19], a[96:99], a[8:11], v[16:19]\n"
    "ds_read_b128 a[0:3], v128 offset:32768\n"
    "s_cmp_eq_u32 s68, 3\n"
    "s_cbranch_scc0 LEOC_%=\n"
    "v_cmp_ne_u64 vcc, s[4:5], v[252:253]\n"
    "v_cmp_ne_u64 s[10:11], s[4:5], v[204:205]\n"
    "s_nop 4\n"
    "s_or_b64 vcc, vcc, s[10:11]\n"
    "s_cbranch_vccz LEOC_%=\n"
    "s_mov_b32 s3, 0\n"
    "global_load_dwordx2 v[252:253], v249, s[6:7] sc1\n"
    "global_load_dwordx2 v[204:205], v203, s[6:7] sc1\n"
    "LW2_%=_900:\n"
    "global_load_dwordx2 v[206:207], v249, s[6:7] sc1\n"
    "global_load_dwordx2 v[196:197], v203, s[6:7] sc1\n"
    "s_waitcnt vmcnt(2)\n"
    "v_cmp_ne_u64 vcc, s[4:5], v[252:253]\n"
    "v_cmp_ne_u64 s[10:11], s[4:5], v[204:205]\n"
    "s_nop 4\n"
    "s_or_b64 vcc, vcc, s[10:11]\n"
    "s_cbranch_vccz LW2D_%=_900\n"
    "global_load_dwordx2 v[252:253], v249, s[6:7] sc1\n"
    "global_load_dwordx2 v[204:205], v203, s[6:7] sc1\n"
    "s_waitcnt vmcnt(2)\n"
    "v_cmp_ne_u64 vcc, s[4:5], v[206:207]\n"
    "v_cmp_ne_u64 s[10:11], s[4:5], v[196:197]\n"
    "s_nop 4\n"
    "s_or_b64 vcc, vcc, s[10:11]\n"
    "s_cbranch_vccz LW2D_%=_900\n"
    "s_add_u32 s3, s3, 1\n"
    "s_cmp_lt_u32 s3, 0x10000\n"
    "s_cbranch_scc1 LW2_%=_900\n"
    "LW2D_%=_900:\n"
    "LEOC_%=:\n"
    "s_waitcnt lgkmcnt(12)\n"
    "v_mfma_f32_16x16x32_bf16 v[16:19], a[100:103], a[12:15], v[16:19]\n"
    "buffer_load_dwordx4 v161, s[44:47], s66 offen offset:1024  lds\n"
    "v_mfma_f32_16x16x32_bf16 v[20:23], a[104:107], a[8:11], v[20:23]\n"
    "v_mfma_f32_16x16x32_bf16 v[20:23], a[108:111], a[12:15], v[20:23]\n"
    "v_mfma_f32_16x16x32_bf16 v[24:27], a[112:115], a[8:11], v[24:27]\n"
    "ds_read_b128 a[4:7], v129 offset:32768\n"
    "v_mfma_f32_16x16x32_bf16 v[24:27], a[116:119], a[12:15], v[24:27]\n"
    "v_mfma_f32_16x16x32_bf16 v[28:31], a[120:123], a[8:11], v[28:31]\n"
    "buffer_load_dwordx4 v162, s[44:47], s66 offen offset:2048  lds\n"
    "v_mfma_f32_16x16x32_bf16 v[28:31], a[124:127], a[12:15], v[28:31]\n"
    "v_mfma_f32_16x16x32_bf16 v[32:35], a[96:99], a[16:19], v[32:35]\n"
    "ds_read_b128 a[8:11], v128 offset:34816\n"
    "s_waitcnt lgkmcnt(12)\n"
    "v_mfma_f32_16x16x32_bf16 v[32:35], a[100:103], a[20:23], v[32:35]\n"
    "v_mfma_f32_16x16x32_bf16 v[36:39], a[104:107], a[16:19], v[36:39]\n"
    "v_mfma_f32_16x16x32_bf16 v[36:39], a[108:111], a[20:23], v[36:39]\n"
    "buffer_load_dwordx4 v163, s[44:47], s66 offen offset:3072  lds\n"
    "v_mfma_f32_16x16x32_bf16 v[40:43], a[112:115], a[16:19], v[40:43]\n"
    "ds_read_b128 a[12:15], v129 offset:34816\n"
    "v_mfma_f32_16x16x32_bf16 v[40:43], a[116:119], a[20:23], v[40:43]\n"
    "v_mfma_f32_16x16x32_bf16 v[44:47], a[120:123], a[16:19], v[44:47]\n"
    "v_mfma_f32_16x16x32_bf16 v[44:47], a[124:127], a[20:23], v[44:47]\n"
    "v_mfma_f32_16x16x32_bf16 v[48:51], a[96:99], a[24:27], v[48:51]\n"
    "ds_read_b128 a[16:19], v128 offset:36864\n"
    "s_waitcnt lgkmcnt(13)\n"
    "v_mfma_f32_16x16x32_bf16 v[48:51], a[100:103], a[28:31], v[48:51]\n"
    "s_add_u32 s66, s66, 256\n"
    "s_cmp_eq_u32 s66, 4096\n"
    "s_cselect_b32 s66, 128, s66\n"
    "v_mfma_f32_16x16x32_bf16 v[52:55], a[104:107], a[24:27], v[52:55]\n"
    "v_mfma_f32_16x16x32_bf16 v[52:55], a[108:111], a[28:31], v[52:55]\n"
    "v_mfma_f32_16x16x32_bf16 v[56:59], a[112:115], a[24:27], v[56:59]\n"
    "ds_read_b128 a[20:23], v129 offset:36864\n"
    "buffer_load_dwordx4 a[192:195], v168, s[48:51], s67 offen offset:0 sc1\n"
    "v_mfma_f32_16x16x32_bf16 v[56:59], a[116:119], a[28:31], v[56:59]\n"
    "v_mfma_f32_16x16x32_bf16 v[60:63], a[120:123], a[24:27], v[60:63]\n"
    "v_mfma_f32_16x16x32_bf16 v[60:63], a[124:127], a[28:31], v[60:63]\n"
    "s_waitcnt lgkmcnt(12)\n"
    "v_mfma_f32_16x16x32_bf16 v[64:67], a[96:99], a[32:35], v[64:67]\n"
    "ds_read_b128 a[24:27], v128 offset:38912\n"
    "buffer_load_dwordx4 a[196:199], v168, s[48:51], s67 offen offset:1024 sc1\n"
    "v_mfma_f32_16x16x32_bf16 v[64:67], a[100:103], a[36:39], v[64:67]\n"
    "v_mfma_f32_16x16x32_bf16 v[68:71], a[104:107], a[32:35], v[68:71]\n"
    "v_mfma_f32_16x16x32_bf16 v[68:71], a[108:111], a[36:39], v[68:71]\n"
    "v_mfma_f32_16x16x32_bf16 v[72:75], a[112:115], a[32:35], v[72:75]\n"
    "ds_read_b128 a[28:31], v129 offset:38912\n"
    "buffer_load_dwordx4 a[200:203], v168, s[48:51], s67 offen offset:2048 sc1\n"
    "v_mfma_f32_16x16x32_bf16 v[72:75], a[116:119], a[36:39], v[72:75]\n"
    "v_mfma_f32_16x16x32_bf16 v[76:79], a[120:123], a[32:35], v[76:79]\n"
    "v_mfma_f32_16x16x32_bf16 v[76:79], a[124:127], a[36:39], v[76:79]\n"
    "s_waitcnt lgkmcnt(12)\n"
    "v_mfma_f32_16x16x32_bf16 v[80:83], a[96:99], a[40:43], v[80:83]\n"
    "ds_read_b128 a[32:35], v128 offset:40960\n"
    "buffer_load_dwordx4 a[204:207], v168, s[48:51], s67 offen offset:3072 sc1\n"
    "v_mfma_f32_16x16x32_bf16 v[80:83], a[100:103], a[44:47], v[80:83]\n"
    "v_mfma_f32_16x16x32_bf16 v[84:87], a[104:107], a[40:43], v[84:87]\n"
    "v_mfma_f32_16x16x32_bf16 v[84:87], a[108:111], a[44:47], v[84:87]\n"
    "v_mfma_f32_16x16x32_bf16 v[88:91], a[112:115], a[40:43], v[88:91]\n"
    "ds_read_b128 a[36:39], v129 offset:40960\n"
    "buffer_load_dwordx4 a[208:211], v169, s[48:51], s67 offen offset:0 sc1\n"
    "v_mfma_f32_16x16x32_bf16 v[88:91], a[116:119], a[44:47], v[88:91]\n"
    "v_mfma_f32_16x16x32_bf16 v[92:95], a[120:123], a[40:43], v[92:95]\n"
    "v_mfma_f32_16x16x32_bf16 v[92:95], a[124:127], a[44:47], v[92:95]\n"
    "s_waitcnt lgkmcnt(12)\n"
    "v_mfma_f32_16x16x32_bf16 v[96:99], a[96:99], a[48:51], v[96:99]\n"
    "ds_read_b128 a[40:43], v128 offset:43008\n"
    "buffer_load_dwordx4 a[212:215], v169, s[48:51], s67 offen offset:1024 sc1\n"
    "v_mfma_f32_16x16x32_bf16 v[96:99], a[100:103], a[52:55], v[96:99]\n"
    "v_mfma_f32_16x16x32_bf16 v[100:103], a[104:107], a[48:51], v[100:103]\n"
    "v_mfma_f32_16x16x32_bf16 v[100:103], a[108:111], a[52:55], v[100:103]\n"
    "v_mfma_f32_16x16x32_bf16 v[104:107], a[112:115], a[48:51], v[104:107]\n"
    "ds_read_b128 a[44:47], v129 offset:43008\n"
    "buffer_load_dwordx4 a[216:219], v169, s[48:51], s67 offen offset:2048 sc1\n"
    "v_mfma_f32_16x16x32_bf16 v[104:107], a[116:119], a[52:55], v[104:107]\n"
    "v_mfma_f32_16x16x32_bf16 v[108:111], a[120:123], a[48:51], v[108:111]\n"
    "v_mfma_f32_16x16x32_bf16 v[108:111], a[124:127], a[52:55], v[108:111]\n"
    "s_waitcnt lgkmcnt(12)\n"
    "v_mfma_f32_16x16x32_bf16 v[112:115], a[96:99], a[56:59], v[112:115]\n"
    "ds_read_b128 a[48:51], v128 offset:45056\n"
    "buffer_load_dwordx4 a[220:223], v169, s[48:51], s67 offen offset:3072 sc1\n"
    "v_mfma_f32_16x16x32_bf16 v[112:115], a[100:103], a[60:63], v[112:115]\n"
    "v_mfma_f32_16x16x32_bf16 v[116:119], a[104:107], a[56:59], v[116:119]\n"
    "v_mfma_f32_16x16x32_bf16 v[116:119], a[108:111], a[60:63], v[116:119]\n"
    "ds_read_b128 a[52:55], v129 offset:45056\n"
    "v_mfma_f32_16x16x32_bf16 v[120:123], a[112:115], a[56:59], v[120:123]\n"
    "v_mfma_f32_16x16x32_bf16 v[120:123], a[116:119], a[60:63], v[120:123]\n"
    "s_add_u32 s67, s67, 262144\n"
    "s_cmp_eq_u32 s67, 4194304\n"
    "s_cselect_b32 s67, 131072, s67\n"
    "v_mfma_f32_16x16x32_bf16 v[124:127], a[120:123], a[56:59], v[124:127]\n"
    "s_add_u32 s78, s78, 1\n"
    "s_cmp_ge_u32 s78, s69\n"
    "s_cselect_b32 s46, 0, s46\n"
    "s_sub_u32 s70, s78, 2\n"
    "s_cmp_ge_u32 s70, s69\n"
    "s_cselect_b32 s50, 0, s50\n"
    "v_mfma_f32_16x16x32_bf16 v[124:127], a[124:127], a[60:63], v[124:127]\n"
    "LY_pos2_%=:\n"
    "s_waitcnt vmcnt(24)\n"
    "s_barrier\n"
    "s_waitcnt lgkmcnt(12)\n"
    "v_mfma_f32_16x16x32_bf16 v[0:3], a[128:131], a[0:3], v[0:3]\n"
    "ds_read_b128 a[56:59], v128 offset:47104\n"
    "v_mfma_f32_16x16x32_bf16 v[0:3], a[132:135], a[4:7], v[0:3]\n"
    "ds_read_b128 a[60:63], v129 offset:47104\n"
    "v_mfma_f32_16x16x32_bf16 v[4:7], a[136:139], a[0:3], v[4:7]\n"
    "v_mfma_f32_16x16x32_bf16 v[4:7], a[140:143], a[4:7], v[4:7]\n"
    "v_mfma_f32_16x16x32_bf16 v[8:11], a[144:147], a[0:3], v[8:11]\n"
    "s_add_u32 m0, s74, 16384\n"
    "s_nop 0\n"
    "buffer_load_dwordx4 v160, s[44:47], s66 offen offset:0  lds\n"
    "v_mfma_f32_16x16x32_bf16 v[8:11], a[148:151], a[4:7], v[8:11]\n"
    "v_mfma_f32_16x16x32_bf16 v[12:15], a[152:155], a[0:3], v[12:15]\n"
    "v_mfma_f32_16x16x32_bf16 v[12:15], a[156:159], a[4:7], v[12:15]\n"
    "s_waitcnt lgkmcnt(12)\n"
    "v_mfma_f32_16x16x32_bf16 v[16:19], a[128:131], a[8:11], v[16:19]\n"
    "ds_read_b128 a[0:3], v130 offset:0\n"
    "v_mfma_f32_16x16x32_bf16 v[16:19], a[132:135], a[12:15], v[16:19]\n"
    "buffer_load_dwordx4 v161, s[44:47], s66 offen offset:1024  lds\n"
    "v_mfma_f32_16x16x32_bf16 v[20:23], a[136:139], a[8:11], v[20:23]\n"
    "v_mfma_f32_16x16x32_bf16 v[20:23], a[140:143], a[12:15], v[20:23]\n"
    "v_mfma_f32_16x16x32_bf16 v[24:27], a[144:147], a[8:11], v[24:27]\n"
    "ds_read_b128 a[4:7], v131 offset:0\n"
    "v_mfma_f32_16x16x32_bf16 v[24:27], a[148:151], a[12:15], v[24:27]\n"
    "v_mfma_f32_16x16x32_bf16 v[28:31], a[152:155], a[8:11], v[28:31]\n"
    "buffer_load_dwordx4 v162, s[44:47], s66 offen offset:2048  lds\n"
    "v_mfma_f32_16x16x32_bf16 v[28:31], a[156:159], a[12:15], v[28:31]\n"
    "s_waitcnt lgkmcnt(12)\n"
    "v_mfma_f32_16x16x32_bf16 v[32:35], a[128:131], a[16:19], v[32:35]\n"
    "ds_read_b128 a[8:11], v130 offset:2048\n"
    "v_mfma_f32_16x16x32_bf16 v[32:35], a[132:135], a[20:23], v[32:35]\n"
    "v_mfma_f32_16x16x32_bf16 v[36:39], a[136:139], a[16:19], v[36:39]\n"
    "v_mfma_f32_16x16x32_bf16 v[36:39], a[140:143], a[20:23], v[36:39]\n"
    "buffer_load_dwordx4 v163, s[44:47], s66 offen offset:3072  lds\n"
    "v_mfma_f32_16x16x32_bf16 v[40:43], a[144:147], a[16:19], v[40:43]\n"
    "ds_read_b128 a[12:15], v131 offset:2048\n"
    "v_mfma_f32_16x16x32_bf16 v[40:43], a[148:151], a[20:23], v[40:43]\n"
    "v_mfma_f32_16x16x32_bf16 v[44:47], a[152:155], a[16:19], v[44:47]\n"
    "v_mfma_f32_16x16x32_bf16 v[44:47], a[156:159], a[20:23], v[44:47]\n"
    "s_waitcnt lgkmcnt(12)\n"
    "v_mfma_f32_16x16x32_bf16 v[48:51], a[128:131], a[24:27], v[48:51]\n"
    "ds_read_b128 a[16:19], v130 offset:4096\n"
    "v_mfma_f32_16x16x32_bf16 v[48:51], a[132:135], a[28:31], v[48:51]\n"
    "s_add_u32 s66, s66, 256\n"
    "s_cmp_eq_u32 s66, 4096\n"
    "s_cselect_b32 s66, 128, s66\n"
    "v_mfma_f32_16x16x32_bf16 v[52:55], a[136:139], a[24:27], v[52:55]\n"
    "v_mfma_f32_16x16x32_bf16 v[52:55], a[140:143], a[28:31], v[52:55]\n"
    "v_mfma_f32_16x16x32_bf16 v[56:59], a[144:147], a[24:27], v[56:59]\n"
    "ds_read_b128 a[20:23], v131 offset:4096\n"
    "buffer_load_dwordx4 a[224:227], v168, s[48:51], s67 offen offset:0 sc1\n"
    "v_mfma_f32_16x16x32_bf16 v[56:59], a[148:151], a[28:31], v[56:59]\n"
    "v_mfma_f32_16x16x32_bf16 v[60:63], a[152:155], a[24:27], v[60:63]\n"
    "v_mfma_f32_16x16x32_bf16 v[60:63], a[156:159], a[28:31], v[60:63]\n"
    "s_waitcnt lgkmcnt(12)\n"
    "v_mfma_f32_16x16x32_bf16 v[64:67], a[128:131], a[32:35], v[64:67]\n"
    "ds_read_b128 a[24:27], v130 offset:6144\n"
    "buffer_load_dwordx4 a[228:231], v168, s[48:51], s67 offen offset:1024 sc1\n"
    "v_mfma_f32_16x16x32_bf16 v[64:67], a[132:135], a[36:39], v[64:67]\n"
    "v_mfma_f32_16x16x32_bf16 v[68:71], a[136:139], a[32:35], v[68:71]\n"
    "v_mfma_f32_16x16x32_bf16 v[68:71], a[140:143], a[36:39], v[68:71]\n"
    "v_mfma_f32_16x16x32_bf16 v[72:75], a[144:147], a[32:35], v[72:75]\n"
    "ds_read_b128 a[28:31], v131 offset:6144\n"
    "buffer_load_dwordx4 a[232:235], v168, s[48:51], s67 offen offset:2048 sc1\n"
    "v_mfma_f32_16x16x32_bf16 v[72:75], a[148:151], a[36:39], v[72:75]\n"
    "v_mfma_f32_16x16x32_bf16 v[76:79], a[152:155], a[32:35], v[76:79]\n"
    "v_mfma_f32_16x16x32_bf16 v[76:79], a[156:159], a[36:39], v[76:79]\n"
    "s_waitcnt lgkmcnt(12)\n"
    "v_mfma_f32_16x16x32_bf16 v[80:83], a[128:131], a[40:43], v[80:83]\n"
    "ds_read_b128 a[32:35], v130 offset:8192\n"
    "buffer_load_dwordx4 a[236:239], v168, s[48:51], s67 offen offset:3072 sc1\n"
    "v_mfma_f32_16x16x32_bf16 v[80:83], a[132:135], a[44:47], v[80:83]\n"
    "v_mfma_f32_16x16x32_bf16 v[84:87], a[136:139], a[40:43], v[84:87]\n"
    "v_mfma_f32_16x16x32_bf16 v[84:87], a[140:143], a[44:47], v[84:87]\n"
    "v_mfma_f32_16x16x32_bf16 v[88:91], a[144:147], a[40:43], v[88:91]\n"
    "ds_read_b128 a[36:39], v131 offset:8192\n"
    "buffer_load_dwordx4 a[240:243], v169, s[48:51], s67 offen offset:0 sc1\n"
    "v_mfma_f32_16x16x32_bf16 v[88:91], a[148:151], a[44:47], v[88:91]\n"
    "v_mfma_f32_16x16x32_bf16 v[92:95], a[152:155], a[40:43], v[92:95]\n"
    "v_mfma_f32_16x16x32_bf16 v[92:95], a[156:159], a[44:47], v[92:95]\n"
    "s_waitcnt lgkmcnt(12)\n"
    "v_mfma_f32_16x16x32_bf16 v[96:99], a[128:131], a[48:51], v[96:99]\n"
    "ds_read_b128 a[40:43], v130 offset:10240\n"
    "buffer_load_dwordx4 a[244:247], v169, s[48:51], s67 offen offset:1024 sc1\n"
    "v_mfma_f32_16x16x32_bf16 v[96:99], a[132:135], a[52:55], v[96:99]\n"
    "v_mfma_f32_16x16x32_bf16 v[100:103], a[136:139], a[48:51], v[100:103]\n"
    "v_mfma_f32_16x16x32_bf16 v[100:103], a[140:143], a[52:55], v[100:103]\n"
    "v_mfma_f32_16x16x32_bf16 v[104:107], a[144:147], a[48:51], v[104:107]\n"
    "ds_read_b128 a[44:47], v131 offset:10240\n"
    "buffer_load_dwordx4 a[248:251], v169, s[48:51], s67 offen offset:2048 sc1\n"
    "v_mfma_f32_16x16x32_bf16 v[104:107], a[148:151], a[52:55], v[104:107]\n"
    "v_mfma_f32_16x16x32_bf16 v[108:111], a[152:155], a[48:51], v[108:111]\n"
    "v_mfma_f32_16x16x32_bf16 v[108:111], a[156:159], a[52:55], v[108:111]\n"
    "s_waitcnt lgkmcnt(12)\n"
    "v_mfma_f32_16x16x32_bf16 v[112:115], a[128:131], a[56:59], v[112:115]\n"
    "ds_read_b128 a[48:51], v130 offset:12288\n"
    "buffer_load_dwordx4 a[252:255], v169, s[48:51], s67 offen offset:3072 sc1\n"
    "v_mfma_f32_16x16x32_bf16 v[112:115], a[132:135], a[60:63], v[112:115]\n"
    "v_mfma_f32_16x16x32_bf16 v[116:119], a[136:139], a[56:59], v[116:119]\n"
    "v_mfma_f32_16x16x32_bf16 v[116:119], a[140:143], a[60:63], v[116:119]\n"
    "ds_read_b128 a[52:55], v131 offset:12288\n"
    "v_mfma_f32_16x16x32_bf16 v[120:123], a[144:147], a[56:59], v[120:123]\n"
    "v_mfma_f32_16x16x32_bf16 v[120:123], a[148:151], a[60:63], v[120:123]\n"
    "s_add_u32 s67, s67, 262144\n"
    "s_cmp_eq_u32 s67, 4194304\n"
    "s_cselect_b32 s67, 131072, s67\n"
    "v_mfma_f32_16x16x32_bf16 v[124:127], a[152:155], a[56:59], v[124:127]\n"
    "s_add_u32 s78, s78, 1\n"
    "s_cmp_ge_u32 s78, s69\n"
    "s_cselect_b32 s46, 0, s46\n"
    "s_sub_u32 s70, s78, 2\n"
    "s_cmp_ge_u32 s70, s69\n"
    "s_cselect_b32 s50, 0, s50\n"
    "v_mfma_f32_16x16x32_bf16 v[124:127], a[156:159], a[60:63], v[124:127]\n"
    "LY_pos3_%=:\n"
    "s_waitcnt vmcnt(24)\n"
    "s_barrier\n"
    "s_waitcnt lgkmcnt(12)\n"
    "v_mfma_f32_16x16x32_bf16 v[0:3], a[160:163], a[0:3], v[0:3]\n"
    "ds_read_b128 a[56:59], v130 offset:14336\n"
    "v_mfma_f32_16x16x32_bf16 v[0:3], a[164:167], a[4:7], v[0:3]\n"
    "ds_read_b128 a[60:63], v131 offset:14336\n"
    "v_mfma_f32_16x16x32_bf16 v[4:7], a[168:171], a[0:3], v[4:7]\n"
    "v_mfma_f32_16x16x32_bf16 v[4:7], a[172:175], a[4:7], v[4:7]\n"
    "v_mfma_f32_16x16x32_bf16 v[8:11], a[176:179], a[0:3], v[8:11]\n"
    "s_add_u32 m0, s74, 32768\n"
    "s_nop 0\n"
    "buffer_load_dwordx4 v160, s[44:47], s66 offen offset:0  lds\n"
    "v_mfma_f32_16x16x32_bf16 v[8:11], a[180:183], a[4:7], v[8:11]\n"
    "v_mfma_f32_16x16x32_bf16 v[12:15], a[184:187], a[0:3], v[12:15]\n"
    "v_mfma_f32_16x16x32_bf16 v[12:15], a[188:191], a[4:7], v[12:15]\n"
    "s_waitcnt lgkmcnt(12)\n"
    "v_mfma_f32_16x16x32_bf16 v[16:19], a[160:163], a[8:11], v[16:19]\n"
    "ds_read_b128 a[0:3], v130 offset:16384\n"
    "v_mfma_f32_16x16x32_bf16 v[16:19], a[164:167], a[12:15], v[16:19]\n"
    "buffer_load_dwordx4 v161, s[44:47], s66 offen offset:1024  lds\n"
    "v_mfma_f32_16x16x32_bf16 v[20:23], a[168:171], a[8:11], v[20:23]\n"
    "v_mfma_f32_16x16x32_bf16 v[20:23], a[172:175], a[12:15], v[20:23]\n"
    "v_mfma_f32_16x16x32_bf16 v[24:27], a[176:179], a[8:11], v[24:27]\n"
    "ds_read_b128 a[4:7], v131 offset:16384\n"
    "v_mfma_f32_16x16x32_bf16 v[24:27], a[180:183], a[12:15], v[24:27]\n"
    "v_mfma_f32_16x16x32_bf16 v[28:31], a[184:187], a[8:11], v[28:31]\n"
    "buffer_load_dwordx4 v162, s[44:47], s66 offen offset:2048  lds\n"
    "v_mfma_f32_16x16x32_bf16 v[28:31], a[188:191], a[12:15], v[28:31]\n"
    "s_waitcnt lgkmcnt(12)\n"
    "v_mfma_f32_16x16x32_bf16 v[32:35], a[160:163], a[16:19], v[32:35]\n"
    "ds_read_b128 a[8:11], v130 offset:18432\n"
    "v_mfma_f32_16x16x32_bf16 v[32:35], a[164:167], a[20:23], v[32:35]\n"
    "v_mfma_f32_16x16x32_bf16 v[36:39], a[168:171], a[16:19], v[36:39]\n"
    "v_mfma_f32_16x16x32_bf16 v[36:39], a[172:175], a[20:23], v[36:39]\n"
    "buffer_load_dwordx4 v163, s[44:47], s66 offen offset:3072  lds\n"
    "v_mfma_f32_16x16x32_bf16 v[40:43], a[176:179], a[16:19], v[40:43]\n"
    "ds_read_b128 a[12:15], v131 offset:18432\n"
    "v_mfma_f32_16x16x32_bf16 v[40:43], a[180:183], a[20:23], v[40:43]\n"
    "v_mfma_f32_16x16x32_bf16 v[44:47], a[184:187], a[16:19], v[44:47]\n"
    "v_mfma_f32_16x16x32_bf16 v[44:47], a[188:191], a[20:23], v[44:47]\n"
    "s_waitcnt lgkmcnt(12)\n"
    "v_mfma_f32_16x16x32_bf16 v[48:51], a[160:163], a[24:27], v[48:51]\n"
    "ds_read_b128 a[16:19], v130 offset:20480\n"
    "v_mfma_f32_16x16x32_bf16 v[48:51], a[164:167], a[28:31], v[48:51]\n"
    "s_add_u32 s66, s66, 256\n"
    "s_cmp_eq_u32 s66, 4096\n"
    "s_cselect_b32 s66, 128, s66\n"
    "v_mfma_f32_16x16x32_bf16 v[52:55], a[168:171], a[24:27], v[52:55]\n"
    "v_mfma_f32_16x16x32_bf16 v[52:55], a[172:175], a[28:31], v[52:55]\n"
    "v_mfma_f32_16x16x32_bf16 v[56:59], a[176:179], a[24:27], v[56:59]\n"
    "ds_read_b128 a[20:23], v131 offset:20480\n"
    "buffer_load_dwordx4 a[64:67], v168, s[48:51], s67 offen offset:0 sc1\n"
    "v_mfma_f32_16x16x32_bf16 v[56:59], a[180:183], a[28:31], v[56:59]\n"
    "v_mfma_f32_16x16x32_bf16 v[60:63], a[184:187], a[24:27], v[60:63]\n"
    "v_mfma_f32_16x16x32_bf16 v[60:63], a[188:191], a[28:31], v[60:63]\n"
    "s_waitcnt lgkmcnt(12)\n"
    "v_mfma_f32_16x16x32_bf16 v[64:67], a[160:163], a[32:35], v[64:67]\n"
    "ds_read_b128 a[24:27], v130 offset:22528\n"
    "buffer_load_dwordx4 a[68:71], v168, s[48:51], s67 offen offset:1024 sc1\n"
    "v_mfma_f32_16x16x32_bf16 v[64:67], a[164:167], a[36:39], v[64:67]\n"
    "v_mfma_f32_16x16x32_bf16 v[68:71], a[168:171], a[32:35], v[68:71]\n"
    "v_mfma_f32_16x16x32_bf16 v[68:71], a[172:175], a[36:39], v[68:71]\n"
    "v_mfma_f32_16x16x32_bf16 v[72:75], a[176:179], a[32:35], v[72:75]\n"
    "ds_read_b128 a[28:31], v131 offset:22528\n"
    "buffer_load_dwordx4 a[72:75], v168, s[48:51], s67 offen offset:2048 sc1\n"
    "v_mfma_f32_16x16x32_bf16 v[72:75], a[180:183], a[36:39], v[72:75]\n"
    "v_mfma_f32_16x16x32_bf16 v[76:79], a[184:187], a[32:35], v[76:79]\n"
    "v_mfma_f32_16x16x32_bf16 v[76:79], a[188:191], a[36:39], v[76:79]\n"
    "s_waitcnt lgkmcnt(12)\n"
    "v_mfma_f32_16x16x32_bf16 v[80:83], a[160:163], a[40:43], v[80:83]\n"
    "ds_read_b128 a[32:35], v130 offset:24576\n"
    "buffer_load_dwordx4 a[76:79], v168, s[48:51], s67 offen offset:3072 sc1\n"
    "v_mfma_f32_16x16x32_bf16 v[80:83], a[164:167], a[44:47], v[80:83]\n"
    "v_mfma_f32_16x16x32_bf16 v[84:87], a[168:171], a[40:43], v[84:87]\n"
    "v_mfma_f32_16x16x32_bf16 v[84:87], a[172:175], a[44:47], v[84:87]\n"
    "v_mfma_f32_16x16x32_bf16 v[88:91], a[176:179], a[40:43], v[88:91]\n"
    "ds_read_b128 a[36:39], v131 offset:24576\n"
    "buffer_load_dwordx4 a[80:83], v169, s[48:51], s67 offen offset:0 sc1\n"
    "v_mfma_f32_16x16x32_bf16 v[88:91], a[180:183], a[44:47], v[88:91]\n"
    "v_mfma_f32_16x16x32_bf16 v[92:95], a[184:187], a[40:43], v[92:95]\n"
    "v_mfma_f32_16x16x32_bf16 v[92:95], a[188:191], a[44:47], v[92:95]\n"
    "s_waitcnt lgkmcnt(12)\n"
    "v_mfma_f32_16x16x32_bf16 v[96:99], a[160:163], a[48:51], v[96:99]\n"
    "ds_read_b128 a[40:43], v130 offset:26624\n"
    "buffer_load_dwordx4 a[84:87], v169, s[48:51], s67 offen offset:1024 sc1\n"
    "v_mfma_f32_16x16x32_bf16 v[96:99], a[164:167], a[52:55], v[96:99]\n"
    "v_mfma_f32_16x16x32_bf16 v[100:103], a[168:171], a[48:51], v[100:103]\n"
    "v_mfma_f32_16x16x32_bf16 v[100:103], a[172:175], a[52:55], v[100:103]\n"
    "v_mfma_f32_16x16x32_bf16 v[104:107], a[176:179], a[48:51], v[104:107]\n"
    "ds_read_b128 a[44:47], v131 offset:26624\n"
    "buffer_load_dwordx4 a[88:91], v169, s[48:51], s67 offen offset:2048 sc1\n"
    "v_mfma_f32_16x16x32_bf16 v[104:107], a[180:183], a[52:55], v[104:107]\n"
    "v_mfma_f32_16x16x32_bf16 v[108:111], a[184:187], a[48:51], v[108:111]\n"
    "v_mfma_f32_16x16x32_bf16 v[108:111], a[188:191], a[52:55], v[108:111]\n"
    "s_waitcnt lgkmcnt(12)\n"
    "v_mfma_f32_16x16x32_bf16 v[112:115], a[160:163], a[56:59], v[112:115]\n"
    "ds_read_b128 a[48:51], v130 offset:28672\n"
    "buffer_load_dwordx4 a[92:95], v169, s[48:51], s67 offen offset:3072 sc1\n"
    "v_mfma_f32_16x16x32_bf16 v[112:115], a[164:167], a[60:63], v[112:115]\n"
    "v_mfma_f32_16x16x32_bf16 v[116:119], a[168:171], a[56:59], v[116:119]\n"
    "v_mfma_f32_16x16x32_bf16 v[116:119], a[172:175], a[60:63], v[116:119]\n"
    "ds_read_b128 a[52:55], v131 offset:28672\n"
    "v_mfma_f32_16x16x32_bf16 v[120:123], a[176:179], a[56:59], v[120:123]\n"
    "v_mfma_f32_16x16x32_bf16 v[120:123], a[180:183], a[60:63], v[120:123]\n"
    "s_add_u32 s67, s67, 262144\n"
    "s_cmp_eq_u32 s67, 4194304\n"
    "s_cselect_b32 s67, 131072, s67\n"
    "v_mfma_f32_16x16x32_bf16 v[124:127], a[184:187], a[56:59], v[124:127]\n"
    "s_add_u32 s78, s78, 1\n"
    "s_cmp_ge_u32 s78, s69\n"
    "s_cselect_b32 s46, 0, s46\n"
    "s_sub_u32 s70, s78, 2\n"
    "s_cmp_ge_u32 s70, s69\n"
    "s_cselect_b32 s50, 0, s50\n"
    "v_mfma_f32_16x16x32_bf16 v[124:127], a[188:191], a[60:63], v[124:127]\n"
    "LY_pos4_%=:\n"
    "s_waitcnt vmcnt(24)\n"
    "s_barrier\n"
    "s_waitcnt lgkmcnt(12)\n"
    "v_mfma_f32_16x16x32_bf16 v[0:3], a[192:195], a[0:3], v[0:3]\n"
    "ds_read_b128 a[56:59], v130 offset:30720\n"
    "v_mfma_f32_16x16x32_bf16 v[0:3], a[196:199], a[4:7], v[0:3]\n"
    "ds_read_b128 a[60:63], v131 offset:30720\n"
    "v_mfma_f32_16x16x32_bf16 v[4:7], a[200:203], a[0:3], v[4:7]\n"
    "v_mfma_f32_16x16x32_bf16 v[4:7], a[204:207], a[4:7], v[4:7]\n"
    "v_mfma_f32_16x16x32_bf16 v[8:11], a[208:211], a[0:3], v[8:11]\n"
    "s_add_u32 m0, s74, 49152\n"
    "s_nop 0\n"
    "buffer_load_dwordx4 v160, s[44:47], s66 offen offset:0  lds\n"
    "v_mfma_f32_16x16x32_bf16 v[8:11], a[212:215], a[4:7], v[8:11]\n"
    "v_mfma_f32_16x16x32_bf16 v[12:15], a[216:219], a[0:3], v[12:15]\n"
    "v_mfma_f32_16x16x32_bf16 v[12:15], a[220:223], a[4:7], v[12:15]\n"
    "s_waitcnt lgkmcnt(12)\n"
    "v_mfma_f32_16x16x32_bf16 v[16:19], a[192:195], a[8:11], v[16:19]\n"
    "ds_read_b128 a[0:3], v130 offset:32768\n"
    "v_mfma_f32_16x16x32_bf16 v[16:19], a[196:199], a[12:15], v[16:19]\n"
    "buffer_load_dwordx4 v161, s[44:47], s66 offen offset:1024  lds\n"
    "v_mfma_f32_16x16x32_bf16 v[20:23], a[200:203], a[8:11], v[20:23]\n"
    "v_mfma_f32_16x16x32_bf16 v[20:23], a[204:207], a[12:15], v[20:23]\n"
    "v_mfma_f32_16x16x32_bf16 v[24:27], a[208:211], a[8:11], v[24:27]\n"
    "ds_read_b128 a[4:7], v131 offset:32768\n"
    "v_mfma_f32_16x16x32_bf16 v[24:27], a[212:215], a[12:15], v[24:27]\n"
    "v_mfma_f32_16x16x32_bf16 v[28:31], a[216:219], a[8:11], v[28:31]\n"
    "buffer_load_dwordx4 v162, s[44:47], s66 offen offset:2048  lds\n"
    "v_mfma_f32_16x16x32_bf16 v[28:31], a[220:223], a[12:15], v[28:31]\n"
    "s_waitcnt lgkmcnt(12)\n"
    "v_mfma_f32_16x16x32_bf16 v[32:35], a[192:195], a[16:19], v[32:35]\n"
    "ds_read_b128 a[8:11], v130 offset:34816\n"
    "v_mfma_f32_16x16x32_bf16 v[32:35], a[196:199], a[20:23], v[32:35]\n"
    "v_mfma_f32_16x16x32_bf16 v[36:39], a[200:203], a[16:19], v[36:39]\n"
    "v_mfma_f32_16x16x32_bf16 v[36:39], a[204:207], a[20:23], v[36:39]\n"
    "buffer_load_dwordx4 v163, s[44:47], s66 offen offset:3072  lds\n"
    "v_mfma_f32_16x16x32_bf16 v[40:43], a[208:211], a[16:19], v[40:43]\n"
    "ds_read_b128 a[12:15], v131 offset:34816\n"
    "v_mfma_f32_16x16x32_bf16 v[40:43], a[212:215], a[20:23], v[40:43]\n"
    "v_mfma_f32_16x16x32_bf16 v[44:47], a[216:219], a[16:19], v[44:47]\n"
    "v_mfma_f32_16x16x32_bf16 v[44:47], a[220:223], a[20:23], v[44:47]\n"
    "s_waitcnt lgkmcnt(12)\n"
    "v_mfma_f32_16x16x32_bf16 v[48:51], a[192:195], a[24:27], v[48:51]\n"
    "ds_read_b128 a[16:19], v130 offset:36864\n"
    "v_mfma_f32_16x16x32_bf16 v[48:51], a[196:199], a[28:31], v[48:51]\n"
    "s_add_u32 s66, s66, 256\n"
    "s_cmp_eq_u32 s66, 4096\n"
    "s_cselect_b32 s66, 128, s66\n"
    "v_mfma_f32_16x16x32_bf16 v[52:55], a[200:203], a[24:27], v[52:55]\n"
    "v_mfma_f32_16x16x32_bf16 v[52:55], a[204:207], a[28:31], v[52:55]\n"
    "v_mfma_f32_16x16x32_bf16 v[56:59], a[208:211], a[24:27], v[56:59]\n"
    "ds_read_b128 a[20:23], v131 offset:36864\n"
    "buffer_load_dwordx4 a[96:99], v168, s[48:51], s67 offen offset:0 sc1\n"
    "v_mfma_f32_16x16x32_bf16 v[56:59], a[212:215], a[28:31], v[56:59]\n"
    "v_mfma_f32_16x16x32_bf16 v[60:63], a[216:219], a[24:27], v[60:63]\n"
    "v_mfma_f32_16x16x32_bf16 v[60:63], a[220:223], a[28:31], v[60:63]\n"
    "s_waitcnt lgkmcnt(12)\n"
    "v_mfma_f32_16x16x32_bf16 v[64:67], a[192:195], a[32:35], v[64:67]\n"
    "ds_read_b128 a[24:27], v130 offset:38912\n"
    "buffer_load_dwordx4 a[100:103], v168, s[48:51], s67 offen offset:1024 sc1\n"
    "v_mfma_f32_16x16x32_bf16 v[64:67], a[196:199], a[36:39], v[64:67]\n"
    "v_mfma_f32_16x16x32_bf16 v[68:71], a[200:203], a[32:35], v[68:71]\n"
    "v_mfma_f32_16x16x32_bf16 v[68:71], a[204:207], a[36:39], v[68:71]\n"
    "v_mfma_f32_16x16x32_bf16 v[72:75], a[208:211], a[32:35], v[72:75]\n"
    "ds_read_b128 a[28:31], v131 offset:38912\n"
    "buffer_load_dwordx4 a[104:107], v168, s[48:51], s67 offen offset:2048 sc1\n"
    "v_mfma_f32_16x16x32_bf16 v[72:75], a[212:215], a[36:39], v[72:75]\n"
    "v_mfma_f32_16x16x32_bf16 v[76:79], a[216:219], a[32:35], v[76:79]\n"
    "v_mfma_f32_16x16x32_bf16 v[76:79], a[220:223], a[36:39], v[76:79]\n"
    "s_waitcnt lgkmcnt(12)\n"
    "v_mfma_f32_16x16x32_bf16 v[80:83], a[192:195], a[40:43], v[80:83]\n"
    "ds_read_b128 a[32:35], v130 offset:40960\n"
    "buffer_load_dwordx4 a[108:111], v168, s[48:51], s67 offen offset:3072 sc1\n"
    "v_mfma_f32_16x16x32_bf16 v[80:83], a[196:199], a[44:47], v[80:83]\n"
    "v_mfma_f32_16x16x32_bf16 v[84:87], a[200:203], a[40:43], v[84:87]\n"
    "v_mfma_f32_16x16x32_bf16 v[84:87], a[204:207], a[44:47], v[84:87]\n"
    "v_mfma_f32_16x16x32_bf16 v[88:91], a[208:211], a[40:43], v[88:91]\n"
    "ds_read_b128 a[36:39], v131 offset:40960\n"
    "buffer_load_dwordx4 a[112:115], v169, s[48:51], s67 offen offset:0 sc1\n"
    "v_mfma_f32_16x16x32_bf16 v[88:91], a[212:215], a[44:47], v[88:91]\n"
    "v_mfma_f32_16x16x32_bf16 v[92:95], a[216:219], a[40:43], v[92:95]\n"
    "v_mfma_f32_16x16x32_bf16 v[92:95], a[220:223], a[44:47], v[92:95]\n"
    "s_waitcnt lgkmcnt(12)\n"
    "v_mfma_f32_16x16x32_bf16 v[96:99], a[192:195], a[48:51], v[96:99]\n"
    "ds_read_b128 a[40:43], v130 offset:43008\n"
    "buffer_load_dwordx4 a[116:119], v169, s[48:51], s67 offen offset:1024 sc1\n"
    "v_mfma_f32_16x16x32_bf16 v[96:99], a[196:199], a[52:55], v[96:99]\n"
    "v_mfma_f32_16x16x32_bf16 v[100:103], a[200:203], a[48:51], v[100:103]\n"
    "v_mfma_f32_16x16x32_bf16 v[100:103], a[204:207], a[52:55], v[100:103]\n"
    "v_mfma_f32_16x16x32_bf16 v[104:107], a[208:211], a[48:51], v[104:107]\n"
    "ds_read_b128 a[44:47], v131 offset:43008\n"
    "buffer_load_dwordx4 a[120:123], v169, s[48:51], s67 offen offset:2048 sc1\n"
    "v_mfma_f32_16x16x32_bf16 v[104:107], a[212:215], a[52:55], v[104:107]\n"
    "v_mfma_f32_16x16x32_bf16 v[108:111], a[216:219], a[48:51], v[108:111]\n"
    "v_mfma_f32_16x16x32_bf16 v[108:111], a[220:223], a[52:55], v[108:111]\n"
    "s_waitcnt lgkmcnt(12)\n"
    "v_mfma_f32_16x16x32_bf16 v[112:115], a[192:195], a[56:59], v[112:115]\n"
    "ds_read_b128 a[48:51], v130 offset:45056\n"
    "buffer_load_dwordx4 a[124:127], v169, s[48:51], s67 offen offset:3072 sc1\n"
    "v_mfma_f32_16x16x32_bf16 v[112:115], a[196:199], a[60:63], v[112:115]\n"
    "v_mfma_f32_16x16x32_bf16 v[116:119], a[200:203], a[56:59], v[116:119]\n"
    "v_mfma_f32_16x16x32_bf16 v[116:119], a[204:207], a[60:63], v[116:119]\n"
    "ds_read_b128 a[52:55], v131 offset:45056\n"
    "v_mfma_f32_16x16x32_bf16 v[120:123], a[208:211], a[56:59], v[120:123]\n"
    "v_mfma_f32_16x16x32_bf16 v[120:123], a[212:215], a[60:63], v[120:123]\n"
    "s_add_u32 s67, s67, 262144\n"
    "s_cmp_eq_u32 s67, 4194304\n"
    "s_cselect_b32 s67, 131072, s67\n"
    "v_mfma_f32_16x16x32_bf16 v[124:127], a[216:219], a[56:59], v[124:127]\n"
    "s_add_u32 s78, s78, 1\n"
    "s_cmp_ge_u32 s78, s69\n"
    "s_cselect_b32 s46, 0, s46\n"
    "s_sub_u32 s70, s78, 2\n"
    "s_cmp_ge_u32 s70, s69\n"
    "s_cselect_b32 s50, 0, s50\n"
    "v_mfma_f32_16x16x32_bf16 v[124:127], a[220:223], a[60:63], v[124:127]\n"
    "LY_pos5_%=:\n"
    "s_waitcnt vmcnt(24)\n"
    "s_barrier\n"
    "s_waitcnt lgkmcnt(12)\n"
    "v_mfma_f32_16x16x32_bf16 v[0:3], a[224:227], a[0:3], v[0:3]\n"
    "ds_read_b128 a[56:59], v130 offset:47104\n"
    "v_mfma_f32_16x16x32_bf16 v[0:3], a[228:231], a[4:7], v[0:3]\n"
    "ds_read_b128 a[60:63], v131 offset:47104\n"
    "v_mfma_f32_16x16x32_bf16 v[4:7], a[232:235], a[0:3], v[4:7]\n"
    "v_mfma_f32_16x16x32_bf16 v[4:7], a[236:239], a[4:7], v[4:7]\n"
    "v_mfma_f32_16x16x32_bf16 v[8:11], a[240:243], a[0:3], v[8:11]\n"
    "s_add_u32 m0, s74, 65536\n"
    "s_nop 0\n"
    "buffer_load_dwordx4 v160, s[44:47], s66 offen offset:0  lds\n"
    "v_mfma_f32_16x16x32_bf16 v[8:11], a[244:247], a[4:7], v[8:11]\n"
    "v_mfma_f32_16x16x32_bf16 v[12:15], a[248:251], a[0:3], v[12:15]\n"
    "v_mfma_f32_16x16x32_bf16 v[12:15], a[252:255], a[4:7], v[12:15]\n"
    "s_waitcnt lgkmcnt(12)\n"
    "v_mfma_f32_16x16x32_bf16 v[16:19], a[224:227], a[8:11], v[16:19]\n"
    "ds_read_b128 a[0:3], v128 offset:0\n"
    "v_mfma_f32_16x16x32_bf16 v[16:19], a[228:231], a[12:15], v[16:19]\n"
    "buffer_load_dwordx4 v161, s[44:47], s66 offen offset:1024  lds\n"
    "v_mfma_f32_16x16x32_bf16 v[20:23], a[232:235], a[8:11], v[20:23]\n"
    "v_mfma_f32_16x16x32_bf16 v[20:23], a[236:239], a[12:15], v[20:23]\n"
    "v_mfma_f32_16x16x32_bf16 v[24:27], a[240:243], a[8:11], v[24:27]\n"
    "ds_read_b128 a[4:7], v129 offset:0\n"
    "v_mfma_f32_16x16x32_bf16 v[24:27], a[244:247], a[12:15], v[24:27]\n"
    "v_mfma_f32_16x16x32_bf16 v[28:31], a[248:251], a[8:11], v[28:31]\n"
    "buffer_load_dwordx4 v162, s[44:47], s66 offen offset:2048  lds\n"
    "v_mfma_f32_16x16x32_bf16 v[28:31], a[252:255], a[12:15], v[28:31]\n"
    "s_waitcnt lgkmcnt(12)\n"
    "v_mfma_f32_16x16x32_bf16 v[32:35], a[224:227], a[16:19], v[32:35]\n"
    "ds_read_b128 a[8:11], v128 offset:2048\n"
    "v_mfma_f32_16x16x32_bf16 v[32:35], a[228:231], a[20:23], v[32:35]\n"
    "v_mfma_f32_16x16x32_bf16 v[36:39], a[232:235], a[16:19], v[36:39]\n"
    "v_mfma_f32_16x16x32_bf16 v[36:39], a[236:239], a[20:23], v[36:39]\n"
    "buffer_load_dwordx4 v163, s[44:47], s66 offen offset:3072  lds\n"
    "v_mfma_f32_16x16x32_bf16 v[40:43], a[240:243], a[16:19], v[40:43]\n"
    "ds_read_b128 a[12:15], v129 offset:2048\n"
    "v_mfma_f32_16x16x32_bf16 v[40:43], a[244:247], a[20:23], v[40:43]\n"
    "v_mfma_f32_16x16x32_bf16 v[44:47], a[248:251], a[16:19], v[44:47]\n"
    "v_mfma_f32_16x16x32_bf16 v[44:47], a[252:255], a[20:23], v[44:47]\n"
    "s_waitcnt lgkmcnt(12)\n"
    "v_mfma_f32_16x16x32_bf16 v[48:51], a[224:227], a[24:27], v[48:51]\n"
    "ds_read_b128 a[16:19], v128 offset:4096\n"
    "v_mfma_f32_16x16x32_bf16 v[48:51], a[228:231], a[28:31], v[48:51]\n"
    "s_add_u32 s66, s66, 256\n"
    "s_cmp_eq_u32 s66, 4096\n"
    "s_cselect_b32 s66, 128, s66\n"
    "v_mfma_f32_16x16x32_bf16 v[52:55], a[232:235], a[24:27], v[52:55]\n"
    "v_mfma_f32_16x16x32_bf16 v[52:55], a[236:239], a[28:31], v[52:55]\n"
    "v_mfma_f32_16x16x32_bf16 v[56:59], a[240:243], a[24:27], v[56:59]\n"
    "ds_read_b128 a[20:23], v129 offset:4096\n"
    "buffer_load_dwordx4 a[128:131], v168, s[48:51], s67 offen offset:0 sc1\n"
    "v_mfma_f32_16x16x32_bf16 v[56:59], a[244:247], a[28:31], v[56:59]\n"
    "v_mfma_f32_16x16x32_bf16 v[60:63], a[248:251], a[24:27], v[60:63]\n"
    "v_mfma_f32_16x16x32_bf16 v[60:63], a[252:255], a[28:31], v[60:63]\n"
    "s_waitcnt lgkmcnt(12)\n"
    "v_mfma_f32_16x16x32_bf16 v[64:67], a[224:227], a[32:35], v[64:67]\n"
    "ds_read_b128 a[24:27], v128 offset:6144\n"
    "buffer_load_dwordx4 a[132:135], v168, s[48:51], s67 offen offset:1024 sc1\n"
    "v_mfma_f32_16x16x32_bf16 v[64:67], a[228:231], a[36:39], v[64:67]\n"
    "v_mfma_f32_16x16x32_bf16 v[68:71], a[232:235], a[32:35], v[68:71]\n"
    "v_mfma_f32_16x16x32_bf16 v[68:71], a[236:239], a[36:39], v[68:71]\n"
    "v_mfma_f32_16x16x32_bf16 v[72:75], a[240:243], a[32:35], v[72:75]\n"
    "ds_read_b128 a[28:31], v129 offset:6144\n"
    "buffer_load_dwordx4 a[136:139], v168, s[48:51], s67 offen offset:2048 sc1\n"
    "v_mfma_f32_16x16x32_bf16 v[72:75], a[244:247], a[36:39], v[72:75]\n"
    "v_mfma_f32_16x16x32_bf16 v[76:79], a[248:251], a[32:35], v[76:79]\n"
    "v_mfma_f32_16x16x32_bf16 v[76:79], a[252:255], a[36:39], v[76:79]\n"
    "s_waitcnt lgkmcnt(12)\n"
    "v_mfma_f32_16x16x32_bf16 v[80:83], a[224:227], a[40:43], v[80:83]\n"
    "ds_read_b128 a[32:35], v128 offset:8192\n"
    "buffer_load_dwordx4 a[140:143], v168, s[48:51], s67 offen offset:3072 sc1\n"
    "v_mfma_f32_16x16x32_bf16 v[80:83], a[228:231], a[44:47], v[80:83]\n"
    "v_mfma_f32_16x16x32_bf16 v[84:87], a[232:235], a[40:43], v[84:87]\n"
    "v_mfma_f32_16x16x32_bf16 v[84:87], a[236:239], a[44:47], v[84:87]\n"
    "v_mfma_f32_16x16x32_bf16 v[88:91], a[240:243], a[40:43], v[88:91]\n"
    "ds_read_b128 a[36:39], v129 offset:8192\n"
    "buffer_load_dwordx4 a[144:147], v169, s[48:51], s67 offen offset:0 sc1\n"
    "v_mfma_f32_16x16x32_bf16 v[88:91], a[244:247], a[44:47], v[88:91]\n"
    "v_mfma_f32_16x16x32_bf16 v[92:95], a[248:251], a[40:43], v[92:95]\n"
    "v_mfma_f32_16x16x32_bf16 v[92:95], a[252:255], a[44:47], v[92:95]\n"
    "s_waitcnt lgkmcnt(12)\n"
    "v_mfma_f32_16x16x32_bf16 v[96:99], a[224:227], a[48:51], v[96:99]\n"
    "ds_read_b128 a[40:43], v128 offset:10240\n"
    "buffer_load_dwordx4 a[148:151], v169, s[48:51], s67 offen offset:1024 sc1\n"
    "v_mfma_f32_16x16x32_bf16 v[96:99], a[228:231], a[52:55], v[96:99]\n"
    "v_mfma_f32_16x16x32_bf16 v[100:103], a[232:235], a[48:51], v[100:103]\n"
    "v_mfma_f32_16x16x32_bf16 v[100:103], a[236:239], a[52:55], v[100:103]\n"
    "v_mfma_f32_16x16x32_bf16 v[104:107], a[240:243], a[48:51], v[104:107]\n"
    "ds_read_b128 a[44:47], v129 offset:10240\n"
    "buffer_load_dwordx4 a[152:155], v169, s[48:51], s67 offen offset:2048 sc1\n"
    "v_mfma_f32_16x16x32_bf16 v[104:107], a[244:247], a[52:55], v[104:107]\n"
    "v_mfma_f32_16x16x32_bf16 v[108:111], a[248:251], a[48:51], v[108:111]\n"
    "v_mfma_f32_16x16x32_bf16 v[108:111], a[252:255], a[52:55], v[108:111]\n"
    "s_waitcnt lgkmcnt(12)\n"
    "v_mfma_f32_16x16x32_bf16 v[112:115], a[224:227], a[56:59], v[112:115]\n"
    "ds_read_b128 a[48:51], v128 offset:12288\n"
    "buffer_load_dwordx4 a[156:159], v169, s[48:51], s67 offen offset:3072 sc1\n"
    "v_mfma_f32_16x16x32_bf16 v[112:115], a[228:231], a[60:63], v[112:115]\n"
    "v_mfma_f32_16x16x32_bf16 v[116:119], a[232:235], a[56:59], v[116:119]\n"
    "v_mfma_f32_16x16x32_bf16 v[116:119], a[236:239], a[60:63], v[116:119]\n"
    "ds_read_b128 a[52:55], v129 offset:12288\n"
    "v_mfma_f32_16x16x32_bf16 v[120:123], a[240:243], a[56:59], v[120:123]\n"
    "v_mfma_f32_16x16x32_bf16 v[120:123], a[244:247], a[60:63], v[120:123]\n"
    "s_add_u32 s67, s67, 262144\n"
    "s_cmp_eq_u32 s67, 4194304\n"
    "s_cselect_b32 s67, 131072, s67\n"
    "v_mfma_f32_16x16x32_bf16 v[124:127], a[248:251], a[56:59], v[124:127]\n"
    "s_add_u32 s78, s78, 1\n"
    "s_cmp_ge_u32 s78, s69\n"
    "s_cselect_b32 s46, 0, s46\n"
    "s_sub_u32 s70, s78, 2\n"
    "s_cmp_ge_u32 s70, s69\n"
    "s_cselect_b32 s50, 0, s50\n"
    "v_mfma_f32_16x16x32_bf16 v[124:127], a[252:255], a[60:63], v[124:127]\n"
    "s_sub_u32 s68, s68, 1\n"
    "s_cmp_lg_u32 s68, 0\n"
    "s_cbranch_scc1 LY_loop_%=\n"
    "s_cmp_eq_u32 s63, 0\n"
    "s_cbranch_scc0 LQ_%=_15\n"
    "s_mov_b32 s52, s36\n"
    "s_and_b32 s53, s37, 0xffff\n"
    "s_mov_b32 s54, 4194304\n"
    "s_mov_b32 s55, 0x20000\n"
    "s_lshl_b32 s71, s79, 1\n"
    "s_add_u32 s82, s71, s63\n"
    "s_lshl_b32 s70, s82, 15\n"
    "s_lshl_b32 s71, s60, 13\n"
    "s_add_u32 s66, s70, s71\n"
    "s_xor_b32 s67, s66, 32768\n"
    "s_lshl_b32 s82, s82, 2\n"
    "s_add_u32 s82, s82, s60\n"
    "s_lshl_b32 s82, s82, 4\n"
    "s_add_u32 s82, s82, 24576\n"
    "s_lshr_b32 s70, s64, 1\n"
    "s_lshl_b32 s70, s70, 3\n"
    "s_lshl_b32 s71, s65, 1\n"
    "s_add_u32 s70, s70, s71\n"
    "s_lshr_b32 s71, s60, 1\n"
    "s_add_u32 s70, s70, s71\n"
    "s_and_b32 s71, s64, 1\n"
    "s_lshl_b32 s71, s71, 6\n"
    "s_and_b32 s72, s60, 1\n"
    "s_lshl_b32 s72, s72, 4\n"
    "s_add_u32 s71, s71, s72\n"
    "s_lshl_b32 s72, s70, 8\n"
    "s_add_u32 s71, s71, s72\n"
    "s_add_u32 s71, s71, 8192\n"
    "v_and_b32 v249, 1, v240\n"
    "v_lshlrev_b32 v249, 7, v249\n"
    "v_add_u32 v249, s71, v249\n"
    "s_lshl_b32 s9, s70, 17\n"
    "s_and_b32 s71, s64, 1\n"
    "s_lshl_b32 s71, s71, 15\n"
    "s_add_u32 s9, s9, s71\n"
    "s_and_b32 s71, s60, 1\n"
    "s_lshl_b32 s71, s71, 13\n"
    "s_add_u32 s9, s9, s71\n"
    "s_mov_b32 s12, s38\n"
    "s_and_b32 s13, s39, 0xffff\n"
    "s_mov_b32 s14, 8388608\n"
    "s_mov_b32 s15, 0x20000\n"
    "s_waitcnt vmcnt(12)\n"
    "s_barrier\n"
    "s_waitcnt lgkmcnt(3)\n"
    "v_mfma_f32_16x16x32_bf16 v[64:67], a[64:67], a[32:35], v[64:67]\n"
    "ds_read_b128 a[56:59], v128 offset:14336\n"
    "v_mfma_f32_16x16x32_bf16 v[64:67], a[68:71], a[36:39], v[64:67]\n"
    "ds_read_b128 a[60:63], v129 offset:14336\n"
    "v_mfma_f32_16x16x32_bf16 v[68:71], a[72:75], a[32:35], v[68:71]\n"
    "v_mfma_f32_16x16x32_bf16 v[68:71], a[76:79], a[36:39], v[68:71]\n"
    "v_mfma_f32_16x16x32_bf16 v[72:75], a[80:83], a[32:35], v[72:75]\n"
    "v_mfma_f32_16x16x32_bf16 v[72:75], a[84:87], a[36:39], v[72:75]\n"
    "v_mfma_f32_16x16x32_bf16 v[76:79], a[88:91], a[32:35], v[76:79]\n"
    "v_mfma_f32_16x16x32_bf16 v[76:79], a[92:95], a[36:39], v[76:79]\n"
    "v_mfma_f32_16x16x32_bf16 v[80:83], a[64:67], a[40:43], v[80:83]\n"
    "ds_read_b128 a[32:35], v128 offset:24576\n"
    "s_waitcnt lgkmcnt(3)\n"
    "v_mfma_f32_16x16x32_bf16 v[80:83], a[68:71], a[44:47], v[80:83]\n"
    "v_mfma_f32_16x16x32_bf16 v[84:87], a[72:75], a[40:43], v[84:87]\n"
    "v_mfma_f32_16x16x32_bf16 v[84:87], a[76:79], a[44:47], v[84:87]\n"
    "v_mfma_f32_16x16x32_bf16 v[88:91], a[80:83], a[40:43], v[88:91]\n"
    "ds_read_b128 a[36:39], v129 offset:24576\n"
    "v_mfma_f32_16x16x32_bf16 v[88:91], a[84:87], a[44:47], v[88:91]\n"
    "v_mfma_f32_16x16x32_bf16 v[92:95], a[88:91], a[40:43], v[92:95]\n"
    "v_mfma_f32_16x16x32_bf16 v[92:95], a[92:95], a[44:47], v[92:95]\n"
    "v_mfma_f32_16x16x32_bf16 v[96:99], a[64:67], a[48:51], v[96:99]\n"
    "ds_read_b128 a[40:43], v128 offset:26624\n"
    "v_mfma_f32_16x16x32_bf16 v[96:99], a[68:71], a[52:55], v[96:99]\n"
    "v_mfma_f32_16x16x32_bf16 v[100:103], a[72:75], a[48:51], v[100:103]\n"
    "v_mfma_f32_16x16x32_bf16 v[100:103], a[76:79], a[52:55], v[100:103]\n"
    "v_mfma_f32_16x16x32_bf16 v[104:107], a[80:83], a[48:51], v[104:107]\n"
    "ds_read_b128 a[44:47], v129 offset:26624\n"
    "v_mfma_f32_16x16x32_bf16 v[104:107], a[84:87], a[52:55], v[104:107]\n"
    "v_mfma_f32_16x16x32_bf16 v[108:111], a[88:91], a[48:51], v[108:111]\n"
    "v_mfma_f32_16x16x32_bf16 v[108:111], a[92:95], a[52:55], v[108:111]\n"
    "s_waitcnt lgkmcnt(4)\n"
    "v_mfma_f32_16x16x32_bf16 v[112:115], a[64:67], a[56:59], v[112:115]\n"
    "ds_read_b128 a[48:51], v128 offset:28672\n"
    "v_mfma_f32_16x16x32_bf16 v[112:115], a[68:71], a[60:63], v[112:115]\n"
    "v_mfma_f32_16x16x32_bf16 v[116:119], a[72:75], a[56:59], v[116:119]\n"
    "v_mfma_f32_16x16x32_bf16 v[116:119], a[76:79], a[60:63], v[116:119]\n"
    "v_mfma_f32_16x16x32_bf16 v[120:123], a[80:83], a[56:59], v[120:123]\n"
    "ds_read_b128 a[52:55], v129 offset:28672\n"
    "v_mfma_f32_16x16x32_bf16 v[120:123], a[84:87], a[60:63], v[120:123]\n"
    "v_mfma_f32_16x16x32_bf16 v[124:127], a[88:91], a[56:59], v[124:127]\n"
    "v_mfma_f32_16x16x32_bf16 v[124:127], a[92:95], a[60:63], v[124:127]\n"
    "ds_read_b128 a[56:59], v128 offset:30720\n"
    "ds_read_b128 a[60:63], v129 offset:30720\n"
    "s_waitcnt lgkmcnt(4)\n"
    "v_mfma_f32_16x16x32_bf16 v[64:67], a[96:99], a[32:35], v[64:67]\n"
    "v_mfma_f32_16x16x32_bf16 v[64:67], a[100:103], a[36:39], v[64:67]\n"
    "global_load_dwordx2 v[252:253], v249, s[6:7] sc1\n"
    "v_mfma_f32_16x16x32_bf16 v[68:71], a[104:107], a[32:35], v[68:71]\n"
    "v_mfma_f32_16x16x32_bf16 v[68:71], a[108:111], a[36:39], v[68:71]\n"
    "v_mfma_f32_16x16x32_bf16 v[72:75], a[112:115], a[32:35], v[72:75]\n"
    "v_mfma_f32_16x16x32_bf16 v[72:75], a[116:119], a[36:39], v[72:75]\n"
    "v_mfma_f32_16x16x32_bf16 v[76:79], a[120:123], a[32:35], v[76:79]\n"
    "v_mfma_f32_16x16x32_bf16 v[76:79], a[124:127], a[36:39], v[76:79]\n"
    "v_mfma_f32_16x16x32_bf16 v[80:83], a[96:99], a[40:43], v[80:83]\n"
    "v_mfma_f32_16x16x32_bf16 v[80:83], a[100:103], a[44:47], v[80:83]\n"
    "v_mfma_f32_16x16x32_bf16 v[84:87], a[104:107], a[40:43], v[84:87]\n"
    "v_mfma_f32_16x16x32_bf16 v[84:87], a[108:111], a[44:47], v[84:87]\n"
    "v_mfma_f32_16x16x32_bf16 v[88:91], a[112:115], a[40:43], v[88:91]\n"
    "v_cvt_pk_bf16_f32 v200, v64, v65\n"
    "v_cvt_pk_bf16_f32 v201, v66, v67\n"
    "v_cvt_pk_bf16_f32 v202, v68, v69\n"
    "v_cvt_pk_bf16_f32 v203, v70, v71\n"
    "s_add_u32 s71, s66, 0\n"
    "buffer_store_dwordx4 v[200:203], v251, s[52:55], s71 offen offset:0 sc1\n"
    "v_mfma_f32_16x16x32_bf16 v[88:91], a[116:119], a[44:47], v[88:91]\n"
    "v_mfma_f32_16x16x32_bf16 v[92:95], a[120:123], a[40:43], v[92:95]\n"
    "v_mfma_f32_16x16x32_bf16 v[92:95], a[124:127], a[44:47], v[92:95]\n"
    "s_waitcnt lgkmcnt(2)\n"
    "v_mfma_f32_16x16x32_bf16 v[96:99], a[96:99], a[48:51], v[96:99]\n"
    "v_cvt_pk_bf16_f32 v204, v72, v73\n"
    "v_cvt_pk_bf16_f32 v205, v74, v75\n"
    "v_cvt_pk_bf16_f32 v206, v76, v77\n"
    "v_cvt_pk_bf16_f32 v207, v78, v79\n"
    "s_add_u32 s71, s66, 0\n"
    "buffer_store_dwordx4 v[204:207], v251, s[52:55], s71 offen offset:1024 sc1\n"
    "v_mfma_f32_16x16x32_bf16 v[96:99], a[100:103], a[52:55], v[96:99]\n"
    "v_mfma_f32_16x16x32_bf16 v[100:103], a[104:107], a[48:51], v[100:103]\n"
    "v_mfma_f32_16x16x32_bf16 v[100:103], a[108:111], a[52:55], v[100:103]\n"
    "v_mfma_f32_16x16x32_bf16 v[104:107], a[112:115], a[48:51], v[104:107]\n"
    "v_cvt_pk_bf16_f32 v200, v80, v81\n"
    "v_cvt_pk_bf16_f32 v201, v82, v83\n"
    "v_cvt_pk_bf16_f32 v202, v84, v85\n"
    "v_cvt_pk_bf16_f32 v203, v86, v87\n"
    "s_add_u32 s71, s66, 0\n"
    "buffer_store_dwordx4 v[200:203], v251, s[52:55], s71 offen offset:2048 sc1\n"
    "v_mfma_f32_16x16x32_bf16 v[104:107], a[116:119], a[52:55], v[104:107]\n"
    "v_mfma_f32_16x16x32_bf16 v[108:111], a[120:123], a[48:51], v[108:111]\n"
    "v_mfma_f32_16x16x32_bf16 v[108:111], a[124:127], a[52:55], v[108:111]\n"
    "s_waitcnt lgkmcnt(0)\n"
    "v_mfma_f32_16x16x32_bf16 v[112:115], a[96:99], a[56:59], v[112:115]\n"
    "v_cvt_pk_bf16_f32 v204, v88, v89\n"
    "v_cvt_pk_bf16_f32 v205, v90, v91\n"
    "v_cvt_pk_bf16_f32 v206, v92, v93\n"
    "v_cvt_pk_bf16_f32 v207, v94, v95\n"
    "s_add_u32 s71, s66, 0\n"
    "buffer_store_dwordx4 v[204:207], v251, s[52:55], s71 offen offset:3072 sc1\n"
    "v_mfma_f32_16x16x32_bf16 v[112:115], a[100:103], a[60:63], v[112:115]\n"
    "v_mfma_f32_16x16x32_bf16 v[116:119], a[104:107], a[56:59], v[116:119]\n"
    "v_mfma_f32_16x16x32_bf16 v[116:119], a[108:111], a[60:63], v[116:119]\n"
    "v_mfma_f32_16x16x32_bf16 v[120:123], a[112:115], a[56:59], v[120:123]\n"
    "v_cvt_pk_bf16_f32 v200, v96, v97\n"
    "v_cvt_pk_bf16_f32 v201, v98, v99\n"
    "v_cvt_pk_bf16_f32 v202, v100, v101\n"
    "v_cvt_pk_bf16_f32 v203, v102, v103\n"
    "s_add_u32 s71, s66, 4096\n"
    "buffer_store_dwordx4 v[200:203], v251, s[52:55], s71 offen offset:0 sc1\n"
    "v_mfma_f32_16x16x32_bf16 v[120:123], a[116:119], a[60:63], v[120:123]\n"
    "v_mfma_f32_16x16x32_bf16 v[124:127], a[120:123], a[56:59], v[124:127]\n"
    "v_mfma_f32_16x16x32_bf16 v[124:127], a[124:127], a[60:63], v[124:127]\n"
    "v_mfma_f32_16x16x32_bf16 v[0:3], a[64:67], a[0:3], v[0:3]\n"
    "v_mfma_f32_16x16x32_bf16 v[0:3], a[68:71], a[4:7], v[0:3]\n"
    "v_cvt_pk_bf16_f32 v204, v104, v105\n"
    "v_cvt_pk_bf16_f32 v205, v106, v107\n"
    "v_cvt_pk_bf16_f32 v206, v108, v109\n"
    "v_cvt_pk_bf16_f32 v207, v110, v111\n"
    "s_add_u32 s71, s66, 4096\n"
    "buffer_store_dwordx4 v[204:207], v251, s[52:55], s71 offen offset:1024 sc1\n"
    "v_mfma_f32_16x16x32_bf16 v[4:7], a[72:75], a[0:3], v[4:7]\n"
    "v_mfma_f32_16x16x32_bf16 v[4:7], a[76:79], a[4:7], v[4:7]\n"
    "v_mfma_f32_16x16x32_bf16 v[8:11], a[80:83], a[0:3], v[8:11]\n"
    "v_mfma_f32_16x16x32_bf16 v[8:11], a[84:87], a[4:7], v[8:11]\n"
    "v_cvt_pk_bf16_f32 v200, v112, v113\n"
    "v_cvt_pk_bf16_f32 v201, v114, v115\n"
    "v_cvt_pk_bf16_f32 v202, v116, v117\n"
    "v_cvt_pk_bf16_f32 v203, v118, v119\n"
    "s_add_u32 s71, s66, 4096\n"
    "buffer_store_dwordx4 v[200:203], v251, s[52:55], s71 offen offset:2048 sc1\n"
    "v_mfma_f32_16x16x32_bf16 v[12:15], a[88:91], a[0:3], v[12:15]\n"
    "v_mfma_f32_16x16x32_bf16 v[12:15], a[92:95], a[4:7], v[12:15]\n"
    "v_mfma_f32_16x16x32_bf16 v[16:19], a[64:67], a[8:11], v[16:19]\n"
    "ds_read_b128 a[0:3], v128 offset:16384\n"
    "v_mfma_f32_16x16x32_bf16 v[16:19], a[68:71], a[12:15], v[16:19]\n"
    "v_cvt_pk_bf16_f32 v204, v120, v121\n"
    "v_cvt_pk_bf16_f32 v205, v122, v123\n"
    "v_cvt_pk_bf16_f32 v206, v124, v125\n"
    "v_cvt_pk_bf16_f32 v207, v126, v127\n"
    "s_add_u32 s71, s66, 4096\n"
    "buffer_store_dwordx4 v[204:207], v251, s[52:55], s71 offen offset:3072 sc1\n"
    "v_mfma_f32_16x16x32_bf16 v[20:23], a[72:75], a[8:11], v[20:23]\n"
    "v_mfma_f32_16x16x32_bf16 v[20:23], a[76:79], a[12:15], v[20:23]\n"
    "v_mfma_f32_16x16x32_bf16 v[24:27], a[80:83], a[8:11], v[24:27]\n"
    "ds_read_b128 a[4:7], v129 offset:16384\n"
    "v_mfma_f32_16x16x32_bf16 v[24:27], a[84:87], a[12:15], v[24:27]\n"
    "v_mfma_f32_16x16x32_bf16 v[28:31], a[88:91], a[8:11], v[28:31]\n"
    "s_waitcnt vmcnt(8)\n"
    "v_cmp_ne_u64 vcc, s[4:5], v[252:253]\n"
    "v_mfma_f32_16x16x32_bf16 v[28:31], a[92:95], a[12:15], v[28:31]\n"
    "v_mfma_f32_16x16x32_bf16 v[32:35], a[64:67], a[16:19], v[32:35]\n"
    "ds_read_b128 a[8:11], v128 offset:18432\n"
    "v_mfma_f32_16x16x32_bf16 v[32:35], a[68:71], a[20:23], v[32:35]\n"
    "v_mfma_f32_16x16x32_bf16 v[36:39], a[72:75], a[16:19], v[36:39]\n"
    "v_mfma_f32_16x16x32_bf16 v[36:39], a[76:79], a[20:23], v[36:39]\n"
    "v_mfma_f32_16x16x32_bf16 v[40:43], a[80:83], a[16:19], v[40:43]\n"
    "s_cmp_eq_u64 vcc, 0\n"
    "s_cselect_b32 s14, s14, 0\n"
    "s_cselect_b32 s68, 1, 0\n"
    "ds_read_b128 a[12:15], v129 offset:18432\n"
    "v_mfma_f32_16x16x32_bf16 v[40:43], a[84:87], a[20:23], v[40:43]\n"
    "v_mfma_f32_16x16x32_bf16 v[44:47], a[88:91], a[16:19], v[44:47]\n"
    "s_add_u32 s73, s9, 0\n"
    "buffer_load_dwordx4 v[136:139], v251, s[12:15], s73 offen offset:0 sc1\n"
    "v_mfma_f32_16x16x32_bf16 v[44:47], a[92:95], a[20:23], v[44:47]\n"
    "v_mfma_f32_16x16x32_bf16 v[48:51], a[64:67], a[24:27], v[48:51]\n"
    "s_add_u32 s73, s9, 0\n"
    "buffer_load_dwordx4 v[140:143], v251, s[12:15], s73 offen offset:1024 sc1\n"
    "ds_read_b128 a[16:19], v128 offset:20480\n"
    "v_mfma_f32_16x16x32_bf16 v[48:51], a[68:71], a[28:31], v[48:51]\n"
    "v_mfma_f32_16x16x32_bf16 v[52:55], a[72:75], a[24:27], v[52:55]\n"
    "s_add_u32 s73, s9, 65536\n"
    "buffer_load_dwordx4 v[168:171], v251, s[12:15], s73 offen offset:0 sc1\n"
    "v_mfma_f32_16x16x32_bf16 v[52:55], a[76:79], a[28:31], v[52:55]\n"
    "v_mfma_f32_16x16x32_bf16 v[56:59], a[80:83], a[24:27], v[56:59]\n"
    "s_add_u32 s73, s9, 65536\n"
    "buffer_load_dwordx4 v[172:175], v251, s[12:15], s73 offen offset:1024 sc1\n"
    "ds_read_b128 a[20:23], v129 offset:20480\n"
    "v_mfma_f32_16x16x32_bf16 v[56:59], a[84:87], a[28:31], v[56:59]\n"
    "v_mfma_f32_16x16x32_bf16 v[60:63], a[88:91], a[24:27], v[60:63]\n"
    "v_mfma_f32_16x16x32_bf16 v[60:63], a[92:95], a[28:31], v[60:63]\n"
    "ds_read_b128 a[24:27], v128 offset:22528\n"
    "ds_read_b128 a[28:31], v129 offset:22528\n"
    "s_waitcnt lgkmcnt(6)\n"
    "v_mfma_f32_16x16x32_bf16 v[0:3], a[96:99], a[0:3], v[0:3]\n"
    "s_add_u32 s73, s9, 0\n"
    "buffer_load_dwordx4 v[144:147], v251, s[12:15], s73 offen offset:2048 sc1\n"
    "v_mfma_f32_16x16x32_bf16 v[0:3], a[100:103], a[4:7], v[0:3]\n"
    "v_mfma_f32_16x16x32_bf16 v[4:7], a[104:107], a[0:3], v[4:7]\n"
    "s_add_u32 s73, s9, 0\n"
    "buffer_load_dwordx4 v[148:151], v251, s[12:15], s73 offen offset:3072 sc1\n"
    "v_mfma_f32_16x16x32_bf16 v[4:7], a[108:111], a[4:7], v[4:7]\n"
    "v_mfma_f32_16x16x32_bf16 v[8:11], a[112:115], a[0:3], v[8:11]\n"
    "s_add_u32 s73, s9, 65536\n"
    "buffer_load_dwordx4 v[176:179], v251, s[12:15], s73 offen offset:2048 sc1\n"
    "v_mfma_f32_16x16x32_bf16 v[8:11], a[116:119], a[4:7], v[8:11]\n"
    "v_mfma_f32_16x16x32_bf16 v[12:15], a[120:123], a[0:3], v[12:15]\n"
    "v_mfma_f32_16x16x32_bf16 v[12:15], a[124:127], a[4:7], v[12:15]\n"
    "s_add_u32 s73, s9, 65536\n"
    "buffer_load_dwordx4 v[180:183], v251, s[12:15], s73 offen offset:3072 sc1\n"
    "s_waitcnt lgkmcnt(4)\n"
    "v_mfma_f32_16x16x32_bf16 v[16:19], a[96:99], a[8:11], v[16:19]\n"
    "v_mfma_f32_16x16x32_bf16 v[16:19], a[100:103], a[12:15], v[16:19]\n"
    "s_add_u32 s73, s9, 4096\n"
    "buffer_load_dwordx4 v[152:155], v251, s[12:15], s73 offen offset:0 sc1\n"
    "v_mfma_f32_16x16x32_bf16 v[20:23], a[104:107], a[8:11], v[20:23]\n"
    "v_mfma_f32_16x16x32_bf16 v[20:23], a[108:111], a[12:15], v[20:23]\n"
    "s_add_u32 s73, s9, 4096\n"
    "buffer_load_dwordx4 v[156:159], v251, s[12:15], s73 offen offset:1024 sc1\n"
    "v_mfma_f32_16x16x32_bf16 v[24:27], a[112:115], a[8:11], v[24:27]\n"
    "v_mfma_f32_16x16x32_bf16 v[24:27], a[116:119], a[12:15], v[24:27]\n"
    "v_mfma_f32_16x16x32_bf16 v[28:31], a[120:123], a[8:11], v[28:31]\n"
    "s_add_u32 s73, s9, 69632\n"
    "buffer_load_dwordx4 v[184:187], v251, s[12:15], s73 offen offset:0 sc1\n"
    "v_mfma_f32_16x16x32_bf16 v[28:31], a[124:127], a[12:15], v[28:31]\n"
    "s_waitcnt lgkmcnt(2)\n"
    "v_mfma_f32_16x16x32_bf16 v[32:35], a[96:99], a[16:19], v[32:35]\n"
    "s_add_u32 s73, s9, 69632\n"
    "buffer_load_dwordx4 v[188:191], v251, s[12:15], s73 offen offset:1024 sc1\n"
    "v_mfma_f32_16x16x32_bf16 v[32:35], a[100:103], a[20:23], v[32:35]\n"
    "v_mfma_f32_16x16x32_bf16 v[36:39], a[104:107], a[16:19], v[36:39]\n"
    "s_add_u32 s73, s9, 4096\n"
    "buffer_load_dwordx4 v[160:163], v251, s[12:15], s73 offen offset:2048 sc1\n"
    "v_mfma_f32_16x16x32_bf16 v[36:39], a[108:111], a[20:23], v[36:39]\n"
    "v_mfma_f32_16x16x32_bf16 v[40:43], a[112:115], a[16:19], v[40:43]\n"
    "v_mfma_f32_16x16x32_bf16 v[40:43], a[116:119], a[20:23], v[40:43]\n"
    "s_add_u32 s73, s9, 4096\n"
    "buffer_load_dwordx4 v[164:167], v251, s[12:15], s73 offen offset:3072 sc1\n"
    "v_mfma_f32_16x16x32_bf16 v[44:47], a[120:123], a[16:19], v[44:47]\n"
    "v_mfma_f32_16x16x32_bf16 v[44:47], a[124:127], a[20:23], v[44:47]\n"
    "s_add_u32 s73, s9, 69632\n"
    "buffer_load_dwordx4 v[192:195], v251, s[12:15], s73 offen offset:2048 sc1\n"
    "s_waitcnt lgkmcnt(0)\n"
    "v_mfma_f32_16x16x32_bf16 v[48:51], a[96:99], a[24:27], v[48:51]\n"
    "v_mfma_f32_16x16x32_bf16 v[48:51], a[100:103], a[28:31], v[48:51]\n"
    "s_add_u32 s73, s9, 69632\n"
    "buffer_load_dwordx4 v[196:199], v251, s[12:15], s73 offen offset:3072 sc1\n"
    "v_mfma_f32_16x16x32_bf16 v[52:55], a[104:107], a[24:27], v[52:55]\n"
    "v_mfma_f32_16x16x32_bf16 v[52:55], a[108:111], a[28:31], v[52:55]\n"
    "v_mfma_f32_16x16x32_bf16 v[56:59], a[112:115], a[24:27], v[56:59]\n"
    "v_mfma_f32_16x16x32_bf16 v[56:59], a[116:119], a[28:31], v[56:59]\n"
    "v_mfma_f32_16x16x32_bf16 v[60:63], a[120:123], a[24:27], v[60:63]\n"
    "v_mfma_f32_16x16x32_bf16 v[60:63], a[124:127], a[28:31], v[60:63]\n"
    "s_cmp_eq_u32 s68, 1\n"
    "s_cbranch_scc1 LDOK_%=_16\n"
    "s_mov_b64 s[10:11], exec\n"
    "s_mov_b64 exec, -1\n"
    "s_mov_b32 s3, 0\n"
    "global_load_dwordx2 v[252:253], v249, s[6:7] sc1\n"
    "LWP_%=_17:\n"
    "global_load_dwordx2 v[132:133], v249, s[6:7] sc1\n"
    "s_waitcnt vmcnt(1)\n"
    "v_cmp_ne_u64 vcc, s[4:5], v[252:253]\n"
    "s_nop 4\n"
    "s_cbranch_vccz LWD_%=_17\n"
    "global_load_dwordx2 v[252:253], v249, s[6:7] sc1\n"
    "s_waitcnt vmcnt(1)\n"
    "v_cmp_ne_u64 vcc, s[4:5], v[132:133]\n"
    "s_nop 4\n"
    "s_cbranch_vccz LWD_%=_17\n"
    "s_add_u32 s3, s3, 1\n"
    "s_cmp_lt_u32 s3, 0x10000\n"
    "s_cbranch_scc1 LWP_%=_17\n"
    "LWD_%=_17:\n"
    "s_mov_b64 exec, s[10:11]\n"
    "s_mov_b32 s14, 8388608\n"
    "s_add_u32 s73, s9, 0\n"
    "buffer_load_dwordx4 v[136:139], v251, s[12:15], s73 offen offset:0 sc1\n"
    "s_add_u32 s73, s9, 0\n"
    "buffer_load_dwordx4 v[140:143], v251, s[12:15], s73 offen offset:1024 sc1\n"
    "s_add_u32 s73, s9, 65536\n"
    "buffer_load_dwordx4 v[168:171], v251, s[12:15], s73 offen offset:0 sc1\n"
    "s_add_u32 s73, s9, 65536\n"
    "buffer_load_dwordx4 v[172:175], v251, s[12:15], s73 offen offset:1024 sc1\n"
    "s_add_u32 s73, s9, 0\n"
    "buffer_load_dwordx4 v[144:147], v251, s[12:15], s73 offen offset:2048 sc1\n"
    "s_add_u32 s73, s9, 0\n"
    "buffer_load_dwordx4 v[148:151], v251, s[12:15], s73 offen offset:3072 sc1\n"
    "s_add_u32 s73, s9, 65536\n"
    "buffer_load_dwordx4 v[176:179], v251, s[12:15], s73 offen offset:2048 sc1\n"
    "s_add_u32 s73, s9, 65536\n"
    "buffer_load_dwordx4 v[180:183], v251, s[12:15], s73 offen offset:3072 sc1\n"
    "s_add_u32 s73, s9, 4096\n"
    "buffer_load_dwordx4 v[152:155], v251, s[12:15], s73 offen offset:0 sc1\n"
    "s_add_u32 s73, s9, 4096\n"
    "buffer_load_dwordx4 v[156:159], v251, s[12:15], s73 offen offset:1024 sc1\n"
    "s_add_u32 s73, s9, 69632\n"
    "buffer_load_dwordx4 v[184:187], v251, s[12:15], s73 offen offset:0 sc1\n"
    "s_add_u32 s73, s9, 69632\n"
    "buffer_load_dwordx4 v[188:191], v251, s[12:15], s73 offen offset:1024 sc1\n"
    "s_add_u32 s73, s9, 4096\n"
    "buffer_load_dwordx4 v[160:163], v251, s[12:15], s73 offen offset:2048 sc1\n"
    "s_add_u32 s73, s9, 4096\n"
    "buffer_load_dwordx4 v[164:167], v251, s[12:15], s73 offen offset:3072 sc1\n"
    "s_add_u32 s73, s9, 69632\n"
    "buffer_load_dwordx4 v[192:195], v251, s[12:15], s73 offen offset:2048 sc1\n"
    "s_add_u32 s73, s9, 69632\n"
    "buffer_load_dwordx4 v[196:199], v251, s[12:15], s73 offen offset:3072 sc1\n"
    "LDOK_%=_16:\n"
    "s_waitcnt vmcnt(16)\n"
    "v_mov_b32 v249, s82\n"
    "v_mov_b32 v252, s4\n"
    "v_mov_b32 v253, s5\n"
    "s_mov_b64 s[10:11], exec\n"
    "s_mov_b64 exec, 1\n"
    "global_store_dwordx2 v249, v[252:253], s[6:7] sc1\n"
    "s_mov_b64 exec, s[10:11]\n"
    "ds_write_b128 v248, v[208:211] offset:0\n"
    "ds_write_b128 v248, v[212:215] offset:1024\n"
    "ds_write_b128 v248, v[216:219] offset:2048\n"
    "ds_write_b128 v248, v[220:223] offset:3072\n"
    "ds_write_b128 v248, v[224:227] offset:4096\n"
    "ds_write_b128 v248, v[228:231] offset:5120\n"
    "ds_write_b128 v248, v[232:235] offset:6144\n"
    "ds_write_b128 v248, v[236:239] offset:7168\n"
    "s_waitcnt lgkmcnt(0)\n"
    "ds_read_b64 v[208:209], v244 offset:0\n"
    "ds_read_b64 v[210:211], v245 offset:0\n"
    "ds_read_b64 v[212:213], v246 offset:0\n"
    "ds_read_b64 v[214:215], v247 offset:0\n"
    "ds_read_b64 v[216:217], v244 offset:2048\n"
    "ds_read_b64 v[218:219], v245 offset:2048\n"
    "ds_read_b64 v[220:221], v246 offset:2048\n"
    "ds_read_b64 v[222:223], v247 offset:2048\n"
    "ds_read_b64 v[224:225], v244 offset:4096\n"
    "ds_read_b64 v[226:227], v245 offset:4096\n"
    "ds_read_b64 v[228:229], v246 offset:4096\n"
    "ds_read_b64 v[230:231], v247 offset:4096\n"
    "ds_read_b64 v[232:233], v244 offset:6144\n"
    "ds_read_b64 v[234:235], v245 offset:6144\n"
    "ds_read_b64 v[236:237], v246 offset:6144\n"
    "ds_read_b64 v[238:239], v247 offset:6144\n"
    "s_waitcnt lgkmcnt(0)\n"
    "s_xor_b32 s71, s82, 64\n"
    "s_sub_u32 s71, s82, 24576\n"
    "s_xor_b32 s71, s71, 64\n"
    "s_add_u32 s71, s71, 24576\n"
    "v_mov_b32 v249, s71\n"
    "s_waitcnt vmcnt(13)\n"
    "s_mov_b32 s72, 0xbfb8aa3b\n"
    "s_mov_b32 s73, 0xbfb8aa3b\n"
    "s_mov_b32 s71, 0xffff0000\n"
    "v_lshlrev_b32 v200, 16, v136\n"
    "v_and_b32 v201, s71, v136\n"
    "v_lshlrev_b32 v202, 16, v137\n"
    "v_and_b32 v203, s71, v137\n"
    "v_lshlrev_b32 v204, 16, v168\n"
    "v_and_b32 v205, s71, v168\n"
    "v_lshlrev_b32 v206, 16, v169\n"
    "v_and_b32 v207, s71, v169\n"
    "v_pk_add_f32 v[64:65], v[200:201], v[204:205]\n"
    "v_pk_add_f32 v[66:67], v[202:203], v[206:207]\n"
    "v_pk_mul_f32 v[200:201], v[64:65], s[72:73]\n"
    "v_pk_mul_f32 v[202:203], v[66:67], s[72:73]\n"
    "v_exp_f32 v200, v200\n"
    "v_exp_f32 v201, v201\n"
    "v_exp_f32 v202, v202\n"
    "v_exp_f32 v203, v203\n"
    "s_nop 0\n"
    "v_pk_add_f32 v[200:201], v[200:201], 1.0 op_sel_hi:[1,0]\n"
    "v_pk_add_f32 v[202:203], v[202:203], 1.0 op_sel_hi:[1,0]\n"
    "v_rcp_f32 v200, v200\n"
    "v_rcp_f32 v201, v201\n"
    "v_rcp_f32 v202, v202\n"
    "v_rcp_f32 v203, v203\n"
    "s_nop 0\n"
    "v_pk_mul_f32 v[64:65], v[64:65], v[200:201]\n"
    "v_pk_mul_f32 v[66:67], v[66:67], v[202:203]\n"
    "v_lshlrev_b32 v200, 16, v138\n"
    "v_and_b32 v201, s71, v138\n"
    "v_lshlrev_b32 v202, 16, v139\n"
    "v_and_b32 v203, s71, v139\n"
    "v_lshlrev_b32 v204, 16, v170\n"
    "v_and_b32 v205, s71, v170\n"
    "v_lshlrev_b32 v206, 16, v171\n"
    "v_and_b32 v207, s71, v171\n"
    "v_pk_add_f32 v[68:69], v[200:201], v[204:205]\n"
    "v_pk_add_f32 v[70:71], v[202:203], v[206:207]\n"
    "v_pk_mul_f32 v[200:201], v[68:69], s[72:73]\n"
    "v_pk_mul_f32 v[202:203], v[70:71], s[72:73]\n"
    "v_exp_f32 v200, v200\n"
    "v_exp_f32 v201, v201\n"
    "v_exp_f32 v202, v202\n"
    "v_exp_f32 v203, v203\n"
    "s_nop 0\n"
    "v_pk_add_f32 v[200:201], v[200:201], 1.0 op_sel_hi:[1,0]\n"
    "v_pk_add_f32 v[202:203], v[202:203], 1.0 op_sel_hi:[1,0]\n"
    "v_rcp_f32 v200, v200\n"
    "v_rcp_f32 v201, v201\n"
    "v_rcp_f32 v202, v202\n"
    "v_rcp_f32 v203, v203\n"
    "s_nop 0\n"
    "v_pk_mul_f32 v[68:69], v[68:69], v[200:201]\n"
    "v_pk_mul_f32 v[70:71], v[70:71], v[202:203]\n"
    "v_lshlrev_b32 v200, 16, v140\n"
    "v_and_b32 v201, s71, v140\n"
    "v_lshlrev_b32 v202, 16, v141\n"
    "v_and_b32 v203, s71, v141\n"
    "v_lshlrev_b32 v204, 16, v172\n"
    "v_and_b32 v205, s71, v172\n"
    "v_lshlrev_b32 v206, 16, v173\n"
    "v_and_b32 v207, s71, v173\n"
    "v_pk_add_f32 v[72:73], v[200:201], v[204:205]\n"
    "v_pk_add_f32 v[74:75], v[202:203], v[206:207]\n"
    "v_pk_mul_f32 v[200:201], v[72:73], s[72:73]\n"
    "v_pk_mul_f32 v[202:203], v[74:75], s[72:73]\n"
    "v_exp_f32 v200, v200\n"
    "v_exp_f32 v201, v201\n"
    "v_exp_f32 v202, v202\n"
    "v_exp_f32 v203, v203\n"
    "s_nop 0\n"
    "v_pk_add_f32 v[200:201], v[200:201], 1.0 op_sel_hi:[1,0]\n"
    "v_pk_add_f32 v[202:203], v[202:203], 1.0 op_sel_hi:[1,0]\n"
    "v_rcp_f32 v200, v200\n"
    "v_rcp_f32 v201, v201\n"
    "v_rcp_f32 v202, v202\n"
    "v_rcp_f32 v203, v203\n"
    "s_nop 0\n"
    "v_pk_mul_f32 v[72:73], v[72:73], v[200:201]\n"
    "v_pk_mul_f32 v[74:75], v[74:75], v[202:203]\n"
    "v_lshlrev_b32 v200, 16, v142\n"
    "v_and_b32 v201, s71, v142\n"
    "v_lshlrev_b32 v202, 16, v143\n"
    "v_and_b32 v203, s71, v143\n"
    "v_lshlrev_b32 v204, 16, v174\n"
    "v_and_b32 v205, s71, v174\n"
    "v_lshlrev_b32 v206, 16, v175\n"
    "v_and_b32 v207, s71, v175\n"
    "v_pk_add_f32 v[76:77], v[200:201], v[204:205]\n"
    "v_pk_add_f32 v[78:79], v[202:203], v[206:207]\n"
    "v_pk_mul_f32 v[200:201], v[76:77], s[72:73]\n"
    "v_pk_mul_f32 v[202:203], v[78:79], s[72:73]\n"
    "v_exp_f32 v200, v200\n"
    "v_exp_f32 v201, v201\n"
    "v_exp_f32 v202, v202\n"
    "v_exp_f32 v203, v203\n"
    "s_nop 0\n"
    "v_pk_add_f32 v[200:201], v[200:201], 1.0 op_sel_hi:[1,0]\n"
    "v_pk_add_f32 v[202:203], v[202:203], 1.0 op_sel_hi:[1,0]\n"
    "v_rcp_f32 v200, v200\n"
    "v_rcp_f32 v201, v201\n"
    "v_rcp_f32 v202, v202\n"
    "v_rcp_f32 v203, v203\n"
    "s_nop 0\n"
    "v_pk_mul_f32 v[76:77], v[76:77], v[200:201]\n"
    "v_pk_mul_f32 v[78:79], v[78:79], v[202:203]\n"
    "global_load_dwordx2 v[252:253], v249, s[6:7] sc1\n"
    "s_waitcnt vmcnt(10)\n"
    "v_lshlrev_b32 v200, 16, v144\n"
    "v_and_b32 v201, s71, v144\n"
    "v_lshlrev_b32 v202, 16, v145\n"
    "v_and_b32 v203, s71, v145\n"
    "v_lshlrev_b32 v204, 16, v176\n"
    "v_and_b32 v205, s71, v176\n"
    "v_lshlrev_b32 v206, 16, v177\n"
    "v_and_b32 v207, s71, v177\n"
    "v_pk_add_f32 v[80:81], v[200:201], v[204:205]\n"
    "v_pk_add_f32 v[82:83], v[202:203], v[206:207]\n"
    "v_pk_mul_f32 v[200:201], v[80:81], s[72:73]\n"
    "v_pk_mul_f32 v[202:203], v[82:83], s[72:73]\n"
    "v_exp_f32 v200, v200\n"
    "v_exp_f32 v201, v201\n"
    "v_exp_f32 v202, v202\n"
    "v_exp_f32 v203, v203\n"
    "s_nop 0\n"
    "v_pk_add_f32 v[200:201], v[200:201], 1.0 op_sel_hi:[1,0]\n"
    "v_pk_add_f32 v[202:203], v[202:203], 1.0 op_sel_hi:[1,0]\n"
    "v_rcp_f32 v200, v200\n"
    "v_rcp_f32 v201, v201\n"
    "v_rcp_f32 v202, v202\n"
    "v_rcp_f32 v203, v203\n"
    "s_nop 0\n"
    "v_pk_mul_f32 v[80:81], v[80:81], v[200:201]\n"
    "v_pk_mul_f32 v[82:83], v[82:83], v[202:203]\n"
    "v_lshlrev_b32 v200, 16, v146\n"
    "v_and_b32 v201, s71, v146\n"
    "v_lshlrev_b32 v202, 16, v147\n"
    "v_and_b32 v203, s71, v147\n"
    "v_lshlrev_b32 v204, 16, v178\n"
    "v_and_b32 v205, s71, v178\n"
    "v_lshlrev_b32 v206, 16, v179\n"
    "v_and_b32 v207, s71, v179\n"
    "v_pk_add_f32 v[84:85], v[200:201], v[204:205]\n"
    "v_pk_add_f32 v[86:87], v[202:203], v[206:207]\n"
    "v_pk_mul_f32 v[200:201], v[84:85], s[72:73]\n"
    "v_pk_mul_f32 v[202:203], v[86:87], s[72:73]\n"
    "v_exp_f32 v200, v200\n"
    "v_exp_f32 v201, v201\n"
    "v_exp_f32 v202, v202\n"
    "v_exp_f32 v203, v203\n"
    "s_nop 0\n"
    "v_pk_add_f32 v[200:201], v[200:201], 1.0 op_sel_hi:[1,0]\n"
    "v_pk_add_f32 v[202:203], v[202:203], 1.0 op_sel_hi:[1,0]\n"
    "v_rcp_f32 v200, v200\n"
    "v_rcp_f32 v201, v201\n"
    "v_rcp_f32 v202, v202\n"
    "v_rcp_f32 v203, v203\n"
    "s_nop 0\n"
    "v_pk_mul_f32 v[84:85], v[84:85], v[200:201]\n"
    "v_pk_mul_f32 v[86:87], v[86:87], v[202:203]\n"
    "v_lshlrev_b32 v200, 16, v148\n"
    "v_and_b32 v201, s71, v148\n"
    "v_lshlrev_b32 v202, 16, v149\n"
    "v_and_b32 v203, s71, v149\n"
    "v_lshlrev_b32 v204, 16, v180\n"
    "v_and_b32 v205, s71, v180\n"
    "v_lshlrev_b32 v206, 16, v181\n"
    "v_and_b32 v207, s71, v181\n"
    "v_pk_add_f32 v[88:89], v[200:201], v[204:205]\n"
    "v_pk_add_f32 v[90:91], v[202:203], v[206:207]\n"
    "v_pk_mul_f32 v[200:201], v[88:89], s[72:73]\n"
    "v_pk_mul_f32 v[202:203], v[90:91], s[72:73]\n"
    "v_exp_f32 v200, v200\n"
    "v_exp_f32 v201, v201\n"
    "v_exp_f32 v202, v202\n"
    "v_exp_f32 v203, v203\n"
    "s_nop 0\n"
    "v_pk_add_f32 v[200:201], v[200:201], 1.0 op_sel_hi:[1,0]\n"
    "v_pk_add_f32 v[202:203], v[202:203], 1.0 op_sel_hi:[1,0]\n"
    "v_rcp_f32 v200, v200\n"
    "v_rcp_f32 v201, v201\n"
    "v_rcp_f32 v202, v202\n"
    "v_rcp_f32 v203, v203\n"
    "s_nop 0\n"
    "v_pk_mul_f32 v[88:89], v[88:89], v[200:201]\n"
    "v_pk_mul_f32 v[90:91], v[90:91], v[202:203]\n"
    "v_lshlrev_b32 v200, 16, v150\n"
    "v_and_b32 v201, s71, v150\n"
    "v_lshlrev_b32 v202, 16, v151\n"
    "v_and_b32 v203, s71, v151\n"
    "v_lshlrev_b32 v204, 16, v182\n"
    "v_and_b32 v205, s71, v182\n"
    "v_lshlrev_b32 v206, 16, v183\n"
    "v_and_b32 v207, s71, v183\n"
    "v_pk_add_f32 v[92:93], v[200:201], v[204:205]\n"
    "v_pk_add_f32 v[94:95], v[202:203], v[206:207]\n"
    "v_pk_mul_f32 v[200:201], v[92:93], s[72:73]\n"
    "v_pk_mul_f32 v[202:203], v[94:95], s[72:73]\n"
    "v_exp_f32 v200, v200\n"
    "v_exp_f32 v201, v201\n"
    "v_exp_f32 v202, v202\n"
    "v_exp_f32 v203, v203\n"
    "s_nop 0\n"
    "v_pk_add_f32 v[200:201], v[200:201], 1.0 op_sel_hi:[1,0]\n"
    "v_pk_add_f32 v[202:203], v[202:203], 1.0 op_sel_hi:[1,0]\n"
    "v_rcp_f32 v200, v200\n"
    "v_rcp_f32 v201, v201\n"
    "v_rcp_f32 v202, v202\n"
    "v_rcp_f32 v203, v203\n"
    "s_nop 0\n"
    "v_pk_mul_f32 v[92:93], v[92:93], v[200:201]\n"
    "v_pk_mul_f32 v[94:95], v[94:95], v[202:203]\n"
    "s_waitcnt vmcnt(0)\n"
    "v_cmp_ne_u64 vcc, s[4:5], v[252:253]\n"
    "s_nop 4\n"
    "s_cbranch_vccz LPOK_%=_18\n"
    "s_mov_b64 s[10:11], exec\n"
    "s_mov_b64 exec, -1\n"
    "s_mov_b32 s3, 0\n"
    "global_load_dwordx2 v[252:253], v249, s[6:7] sc1\n"
    "LWP_%=_19:\n"
    "global_load_dwordx2 v[132:133], v249, s[6:7] sc1\n"
    "s_waitcnt vmcnt(1)\n"
    "v_cmp_ne_u64 vcc, s[4:5], v[252:253]\n"
    "s_nop 4\n"
    "s_cbranch_vccz LWD_%=_19\n"
    "global_load_dwordx2 v[252:253], v249, s[6:7] sc1\n"
    "s_waitcnt vmcnt(1)\n"
    "v_cmp_ne_u64 vcc, s[4:5], v[132:133]\n"
    "s_nop 4\n"
    "s_cbranch_vccz LWD_%=_19\n"
    "s_add_u32 s3, s3, 1\n"
    "s_cmp_lt_u32 s3, 0x10000\n"
    "s_cbranch_scc1 LWP_%=_19\n"
    "LWD_%=_19:\n"
    "s_mov_b64 exec, s[10:11]\n"
    "LPOK_%=_18:\n"
    "s_add_u32 s66, s67, 0\n"
    "buffer_load_dwordx4 v[136:139], v251, s[52:55], s66 offen offset:0 sc1\n"
    "s_add_u32 s66, s67, 0\n"
    "buffer_load_dwordx4 v[140:143], v251, s[52:55], s66 offen offset:1024 sc1\n"
    "s_add_u32 s66, s67, 0\n"
    "buffer_load_dwordx4 v[144:147], v251, s[52:55], s66 offen offset:2048 sc1\n"
    "s_add_u32 s66, s67, 0\n"
    "buffer_load_dwordx4 v[148:151], v251, s[52:55], s66 offen offset:3072 sc1\n"
    "s_add_u32 s66, s67, 4096\n"
    "buffer_load_dwordx4 v[168:171], v251, s[52:55], s66 offen offset:0 sc1\n"
    "s_add_u32 s66, s67, 4096\n"
    "buffer_load_dwordx4 v[172:175], v251, s[52:55], s66 offen offset:1024 sc1\n"
    "s_add_u32 s66, s67, 4096\n"
    "buffer_load_dwordx4 v[176:179], v251, s[52:55], s66 offen offset:2048 sc1\n"
    "s_add_u32 s66, s67, 4096\n"
    "buffer_load_dwordx4 v[180:183], v251, s[52:55], s66 offen offset:3072 sc1\n"
    "s_mov_b32 s71, 0xffff0000\n"
    "v_lshlrev_b32 v200, 16, v152\n"
    "v_and_b32 v201, s71, v152\n"
    "v_lshlrev_b32 v202, 16, v153\n"
    "v_and_b32 v203, s71, v153\n"
    "v_lshlrev_b32 v204, 16, v184\n"
    "v_and_b32 v205, s71, v184\n"
    "v_lshlrev_b32 v206, 16, v185\n"
    "v_and_b32 v207, s71, v185\n"
    "v_pk_add_f32 v[96:97], v[200:201], v[204:205]\n"
    "v_pk_add_f32 v[98:99], v[202:203], v[206:207]\n"
    "v_pk_mul_f32 v[200:201], v[96:97], s[72:73]\n"
    "v_pk_mul_f32 v[202:203], v[98:99], s[72:73]\n"
    "v_exp_f32 v200, v200\n"
    "v_exp_f32 v201, v201\n"
    "v_exp_f32 v202, v202\n"
    "v_exp_f32 v203, v203\n"
    "s_nop 0\n"
    "v_pk_add_f32 v[200:201], v[200:201], 1.0 op_sel_hi:[1,0]\n"
    "v_pk_add_f32 v[202:203], v[202:203], 1.0 op_sel_hi:[1,0]\n"
    "v_rcp_f32 v200, v200\n"
    "v_rcp_f32 v201, v201\n"
    "v_rcp_f32 v202, v202\n"
    "v_rcp_f32 v203, v203\n"
    "s_nop 0\n"
    "v_pk_mul_f32 v[96:97], v[96:97], v[200:201]\n"
    "v_pk_mul_f32 v[98:99], v[98:99], v[202:203]\n"
    "v_lshlrev_b32 v200, 16, v154\n"
    "v_and_b32 v201, s71, v154\n"
    "v_lshlrev_b32 v202, 16, v155\n"
    "v_and_b32 v203, s71, v155\n"
    "v_lshlrev_b32 v204, 16, v186\n"
    "v_and_b32 v205, s71, v186\n"
    "v_lshlrev_b32 v206, 16, v187\n"
    "v_and_b32 v207, s71, v187\n"
    "v_pk_add_f32 v[100:101], v[200:201], v[204:205]\n"
    "v_pk_add_f32 v[102:103], v[202:203], v[206:207]\n"
    "v_pk_mul_f32 v[200:201], v[100:101], s[72:73]\n"
    "v_pk_mul_f32 v[202:203], v[102:103], s[72:73]\n"
    "v_exp_f32 v200, v200\n"
    "v_exp_f32 v201, v201\n"
    "v_exp_f32 v202, v202\n"
    "v_exp_f32 v203, v203\n"
    "s_nop 0\n"
    "v_pk_add_f32 v[200:201], v[200:201], 1.0 op_sel_hi:[1,0]\n"
    "v_pk_add_f32 v[202:203], v[202:203], 1.0 op_sel_hi:[1,0]\n"
    "v_rcp_f32 v200, v200\n"
    "v_rcp_f32 v201, v201\n"
    "v_rcp_f32 v202, v202\n"
    "v_rcp_f32 v203, v203\n"
    "s_nop 0\n"
    "v_pk_mul_f32 v[100:101], v[100:101], v[200:201]\n"
    "v_pk_mul_f32 v[102:103], v[102:103], v[202:203]\n"
    "v_lshlrev_b32 v200, 16, v156\n"
    "v_and_b32 v201, s71, v156\n"
    "v_lshlrev_b32 v202, 16, v157\n"
    "v_and_b32 v203, s71, v157\n"
    "v_lshlrev_b32 v204, 16, v188\n"
    "v_and_b32 v205, s71, v188\n"
    "v_lshlrev_b32 v206, 16, v189\n"
    "v_and_b32 v207, s71, v189\n"
    "v_pk_add_f32 v[104:105], v[200:201], v[204:205]\n"
    "v_pk_add_f32 v[106:107], v[202:203], v[206:207]\n"
    "v_pk_mul_f32 v[200:201], v[104:105], s[72:73]\n"
    "v_pk_mul_f32 v[202:203], v[106:107], s[72:73]\n"
    "v_exp_f32 v200, v200\n"
    "v_exp_f32 v201, v201\n"
    "v_exp_f32 v202, v202\n"
    "v_exp_f32 v203, v203\n"
    "s_nop 0\n"
    "v_pk_add_f32 v[200:201], v[200:201], 1.0 op_sel_hi:[1,0]\n"
    "v_pk_add_f32 v[202:203], v[202:203], 1.0 op_sel_hi:[1,0]\n"
    "v_rcp_f32 v200, v200\n"
    "v_rcp_f32 v201, v201\n"
    "v_rcp_f32 v202, v202\n"
    "v_rcp_f32 v203, v203\n"
    "s_nop 0\n"
    "v_pk_mul_f32 v[104:105], v[104:105], v[200:201]\n"
    "v_pk_mul_f32 v[106:107], v[106:107], v[202:203]\n"
    "v_lshlrev_b32 v200, 16, v158\n"
    "v_and_b32 v201, s71, v158\n"
    "v_lshlrev_b32 v202, 16, v159\n"
    "v_and_b32 v203, s71, v159\n"
    "v_lshlrev_b32 v204, 16, v190\n"
    "v_and_b32 v205, s71, v190\n"
    "v_lshlrev_b32 v206, 16, v191\n"
    "v_and_b32 v207, s71, v191\n"
    "v_pk_add_f32 v[108:109], v[200:201], v[204:205]\n"
    "v_pk_add_f32 v[110:111], v[202:203], v[206:207]\n"
    "v_pk_mul_f32 v[200:201], v[108:109], s[72:73]\n"
    "v_pk_mul_f32 v[202:203], v[110:111], s[72:73]\n"
    "v_exp_f32 v200, v200\n"
    "v_exp_f32 v201, v201\n"
    "v_exp_f32 v202, v202\n"
    "v_exp_f32 v203, v203\n"
    "s_nop 0\n"
    "v_pk_add_f32 v[200:201], v[200:201], 1.0 op_sel_hi:[1,0]\n"
    "v_pk_add_f32 v[202:203], v[202:203], 1.0 op_sel_hi:[1,0]\n"
    "v_rcp_f32 v200, v200\n"
    "v_rcp_f32 v201, v201\n"
    "v_rcp_f32 v202, v202\n"
    "v_rcp_f32 v203, v203\n"
    "s_nop 0\n"
    "v_pk_mul_f32 v[108:109], v[108:109], v[200:201]\n"
    "v_pk_mul_f32 v[110:111], v[110:111], v[202:203]\n"
    "v_lshlrev_b32 v200, 16, v160\n"
    "v_and_b32 v201, s71, v160\n"
    "v_lshlrev_b32 v202, 16, v161\n"
    "v_and_b32 v203, s71, v161\n"
    "v_lshlrev_b32 v204, 16, v192\n"
    "v_and_b32 v205, s71, v192\n"
    "v_lshlrev_b32 v206, 16, v193\n"
    "v_and_b32 v207, s71, v193\n"
    "v_pk_add_f32 v[112:113], v[200:201], v[204:205]\n"
    "v_pk_add_f32 v[114:115], v[202:203], v[206:207]\n"
    "v_pk_mul_f32 v[200:201], v[112:113], s[72:73]\n"
    "v_pk_mul_f32 v[202:203], v[114:115], s[72:73]\n"
    "v_exp_f32 v200, v200\n"
    "v_exp_f32 v201, v201\n"
    "v_exp_f32 v202, v202\n"
    "v_exp_f32 v203, v203\n"
    "s_nop 0\n"
    "v_pk_add_f32 v[200:201], v[200:201], 1.0 op_sel_hi:[1,0]\n"
    "v_pk_add_f32 v[202:203], v[202:203], 1.0 op_sel_hi:[1,0]\n"
    "v_rcp_f32 v200, v200\n"
    "v_rcp_f32 v201, v201\n"
    "v_rcp_f32 v202, v202\n"
    "v_rcp_f32 v203, v203\n"
    "s_nop 0\n"
    "v_pk_mul_f32 v[112:113], v[112:113], v[200:201]\n"
    "v_pk_mul_f32 v[114:115], v[114:115], v[202:203]\n"
    "v_lshlrev_b32 v200, 16, v162\n"
    "v_and_b32 v201, s71, v162\n"
    "v_lshlrev_b32 v202, 16, v163\n"
    "v_and_b32 v203, s71, v163\n"
    "v_lshlrev_b32 v204, 16, v194\n"
    "v_and_b32 v205, s71, v194\n"
    "v_lshlrev_b32 v206, 16, v195\n"
    "v_and_b32 v207, s71, v195\n"
    "v_pk_add_f32 v[116:117], v[200:201], v[204:205]\n"
    "v_pk_add_f32 v[118:119], v[202:203], v[206:207]\n"
    "v_pk_mul_f32 v[200:201], v[116:117], s[72:73]\n"
    "v_pk_mul_f32 v[202:203], v[118:119], s[72:73]\n"
    "v_exp_f32 v200, v200\n"
    "v_exp_f32 v201, v201\n"
    "v_exp_f32 v202, v202\n"
    "v_exp_f32 v203, v203\n"
    "s_nop 0\n"
    "v_pk_add_f32 v[200:201], v[200:201], 1.0 op_sel_hi:[1,0]\n"
    "v_pk_add_f32 v[202:203], v[202:203], 1.0 op_sel_hi:[1,0]\n"
    "v_rcp_f32 v200, v200\n"
    "v_rcp_f32 v201, v201\n"
    "v_rcp_f32 v202, v202\n"
    "v_rcp_f32 v203, v203\n"
    "s_nop 0\n"
    "v_pk_mul_f32 v[116:117], v[116:117], v[200:201]\n"
    "v_pk_mul_f32 v[118:119], v[118:119], v[202:203]\n"
    "v_lshlrev_b32 v200, 16, v164\n"
    "v_and_b32 v201, s71, v164\n"
    "v_lshlrev_b32 v202, 16, v165\n"
    "v_and_b32 v203, s71, v165\n"
    "v_lshlrev_b32 v204, 16, v196\n"
    "v_and_b32 v205, s71, v196\n"
    "v_lshlrev_b32 v206, 16, v197\n"
    "v_and_b32 v207, s71, v197\n"
    "v_pk_add_f32 v[120:121], v[200:201], v[204:205]\n"
    "v_pk_add_f32 v[122:123], v[202:203], v[206:207]\n"
    "v_pk_mul_f32 v[200:201], v[120:121], s[72:73]\n"
    "v_pk_mul_f32 v[202:203], v[122:123], s[72:73]\n"
    "v_exp_f32 v200, v200\n"
    "v_exp_f32 v201, v201\n"
    "v_exp_f32 v202, v202\n"
    "v_exp_f32 v203, v203\n"
    "s_nop 0\n"
    "v_pk_add_f32 v[200:201], v[200:201], 1.0 op_sel_hi:[1,0]\n"
    "v_pk_add_f32 v[202:203], v[202:203], 1.0 op_sel_hi:[1,0]\n"
    "v_rcp_f32 v200, v200\n"
    "v_rcp_f32 v201, v201\n"
    "v_rcp_f32 v202, v202\n"
    "v_rcp_f32 v203, v203\n"
    "s_nop 0\n"
    "v_pk_mul_f32 v[120:121], v[120:121], v[200:201]\n"
    "v_pk_mul_f32 v[122:123], v[122:123], v[202:203]\n"
    "v_lshlrev_b32 v200, 16, v166\n"
    "v_and_b32 v201, s71, v166\n"
    "v_lshlrev_b32 v202, 16, v167\n"
    "v_and_b32 v203, s71, v167\n"
    "v_lshlrev_b32 v204, 16, v198\n"
    "v_and_b32 v205, s71, v198\n"
    "v_lshlrev_b32 v206, 16, v199\n"
    "v_and_b32 v207, s71, v199\n"
    "v_pk_add_f32 v[124:125], v[200:201], v[204:205]\n"
    "v_pk_add_f32 v[126:127], v[202:203], v[206:207]\n"
    "v_pk_mul_f32 v[200:201], v[124:125], s[72:73]\n"
    "v_pk_mul_f32 v[202:203], v[126:127], s[72:73]\n"
    "v_exp_f32 v200, v200\n"
    "v_exp_f32 v201, v201\n"
    "v_exp_f32 v202, v202\n"
    "v_exp_f32 v203, v203\n"
    "s_nop 0\n"
    "v_pk_add_f32 v[200:201], v[200:201], 1.0 op_sel_hi:[1,0]\n"
    "v_pk_add_f32 v[202:203], v[202:203], 1.0 op_sel_hi:[1,0]\n"
    "v_rcp_f32 v200, v200\n"
    "v_rcp_f32 v201, v201\n"
    "v_rcp_f32 v202, v202\n"
    "v_rcp_f32 v203, v203\n"
    "s_nop 0\n"
    "v_pk_mul_f32 v[124:125], v[124:125], v[200:201]\n"
    "v_pk_mul_f32 v[126:127], v[126:127], v[202:203]\n"
    "v_lshlrev_b32 v200, 16, v208\n"
    "v_and_b32 v201, s71, v208\n"
    "v_lshlrev_b32 v202, 16, v209\n"
    "v_and_b32 v203, s71, v209\n"
    "v_pk_fma_f32 v[0:1], v[0:1], v[64:65], v[200:201]\n"
    "v_pk_fma_f32 v[2:3], v[2:3], v[66:67], v[202:203]\n"
    "v_lshlrev_b32 v200, 16, v210\n"
    "v_and_b32 v201, s71, v210\n"
    "v_lshlrev_b32 v202, 16, v211\n"
    "v_and_b32 v203, s71, v211\n"
    "v_pk_fma_f32 v[4:5], v[4:5], v[68:69], v[200:201]\n"
    "v_pk_fma_f32 v[6:7], v[6:7], v[70:71], v[202:203]\n"
    "v_lshlrev_b32 v200, 16, v212\n"
    "v_and_b32 v201, s71, v212\n"
    "v_lshlrev_b32 v202, 16, v213\n"
    "v_and_b32 v203, s71, v213\n"
    "v_pk_fma_f32 v[8:9], v[8:9], v[72:73], v[200:201]\n"
    "v_pk_fma_f32 v[10:11], v[10:11], v[74:75], v[202:203]\n"
    "v_lshlrev_b32 v200, 16, v214\n"
    "v_and_b32 v201, s71, v214\n"
    "v_lshlrev_b32 v202, 16, v215\n"
    "v_and_b32 v203, s71, v215\n"
    "v_pk_fma_f32 v[12:13], v[12:13], v[76:77], v[200:201]\n"
    "v_pk_fma_f32 v[14:15], v[14:15], v[78:79], v[202:203]\n"
    "v_lshlrev_b32 v200, 16, v216\n"
    "v_and_b32 v201, s71, v216\n"
    "v_lshlrev_b32 v202, 16, v217\n"
    "v_and_b32 v203, s71, v217\n"
    "v_pk_fma_f32 v[16:17], v[16:17], v[80:81], v[200:201]\n"
    "v_pk_fma_f32 v[18:19], v[18:19], v[82:83], v[202:203]\n"
    "v_lshlrev_b32 v200, 16, v218\n"
    "v_and_b32 v201, s71, v218\n"
    "v_lshlrev_b32 v202, 16, v219\n"
    "v_and_b32 v203, s71, v219\n"
    "v_pk_fma_f32 v[20:21], v[20:21], v[84:85], v[200:201]\n"
    "v_pk_fma_f32 v[22:23], v[22:23], v[86:87], v[202:203]\n"
    "v_lshlrev_b32 v200, 16, v220\n"
    "v_and_b32 v201, s71, v220\n"
    "v_lshlrev_b32 v202, 16, v221\n"
    "v_and_b32 v203, s71, v221\n"
    "v_pk_fma_f32 v[24:25], v[24:25], v[88:89], v[200:201]\n"
    "v_pk_fma_f32 v[26:27], v[26:27], v[90:91], v[202:203]\n"
    "v_lshlrev_b32 v200, 16, v222\n"
    "v_and_b32 v201, s71, v222\n"
    "v_lshlrev_b32 v202, 16, v223\n"
    "v_and_b32 v203, s71, v223\n"
    "v_pk_fma_f32 v[28:29], v[28:29], v[92:93], v[200:201]\n"
    "v_pk_fma_f32 v[30:31], v[30:31], v[94:95], v[202:203]\n"
    "v_lshlrev_b32 v200, 16, v224\n"
    "v_and_b32 v201, s71, v224\n"
    "v_lshlrev_b32 v202, 16, v225\n"
    "v_and_b32 v203, s71, v225\n"
    "v_pk_fma_f32 v[32:33], v[32:33], v[96:97], v[200:201]\n"
    "v_pk_fma_f32 v[34:35], v[34:35], v[98:99], v[202:203]\n"
    "v_lshlrev_b32 v200, 16, v226\n"
    "v_and_b32 v201, s71, v226\n"
    "v_lshlrev_b32 v202, 16, v227\n"
    "v_and_b32 v203, s71, v227\n"
    "v_pk_fma_f32 v[36:37], v[36:37], v[100:101], v[200:201]\n"
    "v_pk_fma_f32 v[38:39], v[38:39], v[102:103], v[202:203]\n"
    "v_lshlrev_b32 v200, 16, v228\n"
    "v_and_b32 v201, s71, v228\n"
    "v_lshlrev_b32 v202, 16, v229\n"
    "v_and_b32 v203, s71, v229\n"
    "v_pk_fma_f32 v[40:41], v[40:41], v[104:105], v[200:201]\n"
    "v_pk_fma_f32 v[42:43], v[42:43], v[106:107], v[202:203]\n"
    "v_lshlrev_b32 v200, 16, v230\n"
    "v_and_b32 v201, s71, v230\n"
    "v_lshlrev_b32 v202, 16, v231\n"
    "v_and_b32 v203, s71, v231\n"
    "v_pk_fma_f32 v[44:45], v[44:45], v[108:109], v[200:201]\n"
    "v_pk_fma_f32 v[46:47], v[46:47], v[110:111], v[202:203]\n"
    "v_lshlrev_b32 v200, 16, v232\n"
    "v_and_b32 v201, s71, v232\n"
    "v_lshlrev_b32 v202, 16, v233\n"
    "v_and_b32 v203, s71, v233\n"
    "v_pk_fma_f32 v[48:49], v[48:49], v[112:113], v[200:201]\n"
    "v_pk_fma_f32 v[50:51], v[50:51], v[114:115], v[202:203]\n"
    "v_lshlrev_b32 v200, 16, v234\n"
    "v_and_b32 v201, s71, v234\n"
    "v_lshlrev_b32 v202, 16, v235\n"
    "v_and_b32 v203, s71, v235\n"
    "v_pk_fma_f32 v[52:53], v[52:53], v[116:117], v[200:201]\n"
    "v_pk_fma_f32 v[54:55], v[54:55], v[118:119], v[202:203]\n"
    "v_lshlrev_b32 v200, 16, v236\n"
    "v_and_b32 v201, s71, v236\n"
    "v_lshlrev_b32 v202, 16, v237\n"
    "v_and_b32 v203, s71, v237\n"
    "v_pk_fma_f32 v[56:57], v[56:57], v[120:121], v[200:201]\n"
    "v_pk_fma_f32 v[58:59], v[58:59], v[122:123], v[202:203]\n"
    "v_lshlrev_b32 v200, 16, v238\n"
    "v_and_b32 v201, s71, v238\n"
    "v_lshlrev_b32 v202, 16, v239\n"
    "v_and_b32 v203, s71, v239\n"
    "v_pk_fma_f32 v[60:61], v[60:61], v[124:125], v[200:201]\n"
    "v_pk_fma_f32 v[62:63], v[62:63], v[126:127], v[202:203]\n"
    "s_waitcnt vmcnt(0)\n"
    "v_lshlrev_b32 v200, 16, v136\n"
    "v_and_b32 v201, s71, v136\n"
    "v_lshlrev_b32 v202, 16, v137\n"
    "v_and_b32 v203, s71, v137\n"
    "v_pk_fma_f32 v[0:1], v[200:201], v[64:65], v[0:1] neg_lo:[1,0,0] neg_hi:[1,0,0]\n"
    "v_pk_fma_f32 v[2:3], v[202:203], v[66:67], v[2:3] neg_lo:[1,0,0] neg_hi:[1,0,0]\n"
    "v_cvt_pk_bf16_f32 v204, v0, v1\n"
    "v_cvt_pk_bf16_f32 v205, v2, v3\n"
    "ds_write_b64 v244, v[204:205] offset:0\n"
    "v_lshlrev_b32 v200, 16, v138\n"
    "v_and_b32 v201, s71, v138\n"
    "v_lshlrev_b32 v202, 16, v139\n"
    "v_and_b32 v203, s71, v139\n"
    "v_pk_fma_f32 v[4:5], v[200:201], v[68:69], v[4:5] neg_lo:[1,0,0] neg_hi:[1,0,0]\n"
    "v_pk_fma_f32 v[6:7], v[202:203], v[70:71], v[6:7] neg_lo:[1,0,0] neg_hi:[1,0,0]\n"
    "v_cvt_pk_bf16_f32 v206, v4, v5\n"
    "v_cvt_pk_bf16_f32 v207, v6, v7\n"
    "ds_write_b64 v245, v[206:207] offset:0\n"
    "v_lshlrev_b32 v200, 16, v140\n"
    "v_and_b32 v201, s71, v140\n"
    "v_lshlrev_b32 v202, 16, v141\n"
    "v_and_b32 v203, s71, v141\n"
    "v_pk_fma_f32 v[8:9], v[200:201], v[72:73], v[8:9] neg_lo:[1,0,0] neg_hi:[1,0,0]\n"
    "v_pk_fma_f32 v[10:11], v[202:203], v[74:75], v[10:11] neg_lo:[1,0,0] neg_hi:[1,0,0]\n"
    "v_cvt_pk_bf16_f32 v204, v8, v9\n"
    "v_cvt_pk_bf16_f32 v205, v10, v11\n"
    "ds_write_b64 v246, v[204:205] offset:0\n"
    "v_lshlrev_b32 v200, 16, v142\n"
    "v_and_b32 v201, s71, v142\n"
    "v_lshlrev_b32 v202, 16, v143\n"
    "v_and_b32 v203, s71, v143\n"
    "v_pk_fma_f32 v[12:13], v[200:201], v[76:77], v[12:13] neg_lo:[1,0,0] neg_hi:[1,0,0]\n"
    "v_pk_fma_f32 v[14:15], v[202:203], v[78:79], v[14:15] neg_lo:[1,0,0] neg_hi:[1,0,0]\n"
    "v_cvt_pk_bf16_f32 v206, v12, v13\n"
    "v_cvt_pk_bf16_f32 v207, v14, v15\n"
    "ds_write_b64 v247, v[206:207] offset:0\n"
    "v_lshlrev_b32 v200, 16, v144\n"
    "v_and_b32 v201, s71, v144\n"
    "v_lshlrev_b32 v202, 16, v145\n"
    "v_and_b32 v203, s71, v145\n"
    "v_pk_fma_f32 v[16:17], v[200:201], v[80:81], v[16:17] neg_lo:[1,0,0] neg_hi:[1,0,0]\n"
    "v_pk_fma_f32 v[18:19], v[202:203], v[82:83], v[18:19] neg_lo:[1,0,0] neg_hi:[1,0,0]\n"
    "v_cvt_pk_bf16_f32 v204, v16, v17\n"
    "v_cvt_pk_bf16_f32 v205, v18, v19\n"
    "ds_write_b64 v244, v[204:205] offset:2048\n"
    "v_lshlrev_b32 v200, 16, v146\n"
    "v_and_b32 v201, s71, v146\n"
    "v_lshlrev_b32 v202, 16, v147\n"
    "v_and_b32 v203, s71, v147\n"
    "v_pk_fma_f32 v[20:21], v[200:201], v[84:85], v[20:21] neg_lo:[1,0,0] neg_hi:[1,0,0]\n"
    "v_pk_fma_f32 v[22:23], v[202:203], v[86:87], v[22:23] neg_lo:[1,0,0] neg_hi:[1,0,0]\n"
    "v_cvt_pk_bf16_f32 v206, v20, v21\n"
    "v_cvt_pk_bf16_f32 v207, v22, v23\n"
    "ds_write_b64 v245, v[206:207] offset:2048\n"
    "v_lshlrev_b32 v200, 16, v148\n"
    "v_and_b32 v201, s71, v148\n"
    "v_lshlrev_b32 v202, 16, v149\n"
    "v_and_b32 v203, s71, v149\n"
    "v_pk_fma_f32 v[24:25], v[200:201], v[88:89], v[24:25] neg_lo:[1,0,0] neg_hi:[1,0,0]\n"
    "v_pk_fma_f32 v[26:27], v[202:203], v[90:91], v[26:27] neg_lo:[1,0,0] neg_hi:[1,0,0]\n"
    "v_cvt_pk_bf16_f32 v204, v24, v25\n"
    "v_cvt_pk_bf16_f32 v205, v26, v27\n"
    "ds_write_b64 v246, v[204:205] offset:2048\n"
    "v_lshlrev_b32 v200, 16, v150\n"
    "v_and_b32 v201, s71, v150\n"
    "v_lshlrev_b32 v202, 16, v151\n"
    "v_and_b32 v203, s71, v151\n"
    "v_pk_fma_f32 v[28:29], v[200:201], v[92:93], v[28:29] neg_lo:[1,0,0] neg_hi:[1,0,0]\n"
    "v_pk_fma_f32 v[30:31], v[202:203], v[94:95], v[30:31] neg_lo:[1,0,0] neg_hi:[1,0,0]\n"
    "v_cvt_pk_bf16_f32 v206, v28, v29\n"
    "v_cvt_pk_bf16_f32 v207, v30, v31\n"
    "ds_write_b64 v247, v[206:207] offset:2048\n"
    "v_lshlrev_b32 v200, 16, v168\n"
    "v_and_b32 v201, s71, v168\n"
    "v_lshlrev_b32 v202, 16, v169\n"
    "v_and_b32 v203, s71, v169\n"
    "v_pk_fma_f32 v[32:33], v[200:201], v[96:97], v[32:33] neg_lo:[1,0,0] neg_hi:[1,0,0]\n"
    "v_pk_fma_f32 v[34:35], v[202:203], v[98:99], v[34:35] neg_lo:[1,0,0] neg_hi:[1,0,0]\n"
    "v_cvt_pk_bf16_f32 v204, v32, v33\n"
    "v_cvt_pk_bf16_f32 v205, v34, v35\n"
    "ds_write_b64 v244, v[204:205] offset:4096\n"
    "v_lshlrev_b32 v200, 16, v170\n"
    "v_and_b32 v201, s71, v170\n"
    "v_lshlrev_b32 v202, 16, v171\n"
    "v_and_b32 v203, s71, v171\n"
    "v_pk_fma_f32 v[36:37], v[200:201], v[100:101], v[36:37] neg_lo:[1,0,0] neg_hi:[1,0,0]\n"
    "v_pk_fma_f32 v[38:39], v[202:203], v[102:103], v[38:39] neg_lo:[1,0,0] neg_hi:[1,0,0]\n"
    "v_cvt_pk_bf16_f32 v206, v36, v37\n"
    "v_cvt_pk_bf16_f32 v207, v38, v39\n"
    "ds_write_b64 v245, v[206:207] offset:4096\n"
    "v_lshlrev_b32 v200, 16, v172\n"
    "v_and_b32 v201, s71, v172\n"
    "v_lshlrev_b32 v202, 16, v173\n"
    "v_and_b32 v203, s71, v173\n"
    "v_pk_fma_f32 v[40:41], v[200:201], v[104:105], v[40:41] neg_lo:[1,0,0] neg_hi:[1,0,0]\n"
    "v_pk_fma_f32 v[42:43], v[202:203], v[106:107], v[42:43] neg_lo:[1,0,0] neg_hi:[1,0,0]\n"
    "v_cvt_pk_bf16_f32 v204, v40, v41\n"
    "v_cvt_pk_bf16_f32 v205, v42, v43\n"
    "ds_write_b64 v246, v[204:205] offset:4096\n"
    "v_lshlrev_b32 v200, 16, v174\n"
    "v_and_b32 v201, s71, v174\n"
    "v_lshlrev_b32 v202, 16, v175\n"
    "v_and_b32 v203, s71, v175\n"
    "v_pk_fma_f32 v[44:45], v[200:201], v[108:109], v[44:45] neg_lo:[1,0,0] neg_hi:[1,0,0]\n"
    "v_pk_fma_f32 v[46:47], v[202:203], v[110:111], v[46:47] neg_lo:[1,0,0] neg_hi:[1,0,0]\n"
    "v_cvt_pk_bf16_f32 v206, v44, v45\n"
    "v_cvt_pk_bf16_f32 v207, v46, v47\n"
    "ds_write_b64 v247, v[206:207] offset:4096\n"
    "v_lshlrev_b32 v200, 16, v176\n"
    "v_and_b32 v201, s71, v176\n"
    "v_lshlrev_b32 v202, 16, v177\n"
    "v_and_b32 v203, s71, v177\n"
    "v_pk_fma_f32 v[48:49], v[200:201], v[112:113], v[48:49] neg_lo:[1,0,0] neg_hi:[1,0,0]\n"
    "v_pk_fma_f32 v[50:51], v[202:203], v[114:115], v[50:51] neg_lo:[1,0,0] neg_hi:[1,0,0]\n"
    "v_cvt_pk_bf16_f32 v204, v48, v49\n"
    "v_cvt_pk_bf16_f32 v205, v50, v51\n"
    "ds_write_b64 v244, v[204:205] offset:6144\n"
    "v_lshlrev_b32 v200, 16, v178\n"
    "v_and_b32 v201, s71, v178\n"
    "v_lshlrev_b32 v202, 16, v179\n"
    "v_and_b32 v203, s71, v179\n"
    "v_pk_fma_f32 v[52:53], v[200:201], v[116:117], v[52:53] neg_lo:[1,0,0] neg_hi:[1,0,0]\n"
    "v_pk_fma_f32 v[54:55], v[202:203], v[118:119], v[54:55] neg_lo:[1,0,0] neg_hi:[1,0,0]\n"
    "v_cvt_pk_bf16_f32 v206, v52, v53\n"
    "v_cvt_pk_bf16_f32 v207, v54, v55\n"
    "ds_write_b64 v245, v[206:207] offset:6144\n"
    "v_lshlrev_b32 v200, 16, v180\n"
    "v_and_b32 v201, s71, v180\n"
    "v_lshlrev_b32 v202, 16, v181\n"
    "v_and_b32 v203, s71, v181\n"
    "v_pk_fma_f32 v[56:57], v[200:201], v[120:121], v[56:57] neg_lo:[1,0,0] neg_hi:[1,0,0]\n"
    "v_pk_fma_f32 v[58:59], v[202:203], v[122:123], v[58:59] neg_lo:[1,0,0] neg_hi:[1,0,0]\n"
    "v_cvt_pk_bf16_f32 v204, v56, v57\n"
    "v_cvt_pk_bf16_f32 v205, v58, v59\n"
    "ds_write_b64 v246, v[204:205] offset:6144\n"
    "v_lshlrev_b32 v200, 16, v182\n"
    "v_and_b32 v201, s71, v182\n"
    "v_lshlrev_b32 v202, 16, v183\n"
    "v_and_b32 v203, s71, v183\n"
    "v_pk_fma_f32 v[60:61], v[200:201], v[124:125], v[60:61] neg_lo:[1,0,0] neg_hi:[1,0,0]\n"
    "v_pk_fma_f32 v[62:63], v[202:203], v[126:127], v[62:63] neg_lo:[1,0,0] neg_hi:[1,0,0]\n"
    "v_cvt_pk_bf16_f32 v206, v60, v61\n"
    "v_cvt_pk_bf16_f32 v207, v62, v63\n"
    "ds_write_b64 v247, v[206:207] offset:6144\n"
    "s_waitcnt lgkmcnt(0)\n"
    "ds_read_b128 v[136:139], v248 offset:0\n"
    "ds_read_b128 v[140:143], v248 offset:1024\n"
    "ds_read_b128 v[144:147], v248 offset:2048\n"
    "ds_read_b128 v[148:151], v248 offset:3072\n"
    "ds_read_b128 v[152:155], v248 offset:4096\n"
    "ds_read_b128 v[156:159], v248 offset:5120\n"
    "ds_read_b128 v[160:163], v248 offset:6144\n"
    "ds_read_b128 v[164:167], v248 offset:7168\n"
    "s_mov_b32 s56, s30\n"
    "s_and_b32 s57, s31, 0xffff\n"
    "s_mov_b32 s58, 4194304\n"
    "s_mov_b32 s59, 0x20000\n"
    "s_waitcnt lgkmcnt(7)\n"
    "s_mov_b32 s73, 0\n"
    "buffer_store_dwordx4 v[136:139], v254, s[56:59], s73 offen sc1\n"
    "s_waitcnt lgkmcnt(6)\n"
    "s_mov_b32 s73, 16384\n"
    "buffer_store_dwordx4 v[140:143], v254, s[56:59], s73 offen sc1\n"
    "s_waitcnt lgkmcnt(5)\n"
    "s_mov_b32 s73, 32768\n"
    "buffer_store_dwordx4 v[144:147], v254, s[56:59], s73 offen sc1\n"
    "s_waitcnt lgkmcnt(4)\n"
    "s_mov_b32 s73, 49152\n"
    "buffer_store_dwordx4 v[148:151], v254, s[56:59], s73 offen sc1\n"
    "s_waitcnt lgkmcnt(3)\n"
    "s_mov_b32 s73, 65536\n"
    "buffer_store_dwordx4 v[152:155], v254, s[56:59], s73 offen sc1\n"
    "s_waitcnt lgkmcnt(2)\n"
    "s_mov_b32 s73, 81920\n"
    "buffer_store_dwordx4 v[156:159], v254, s[56:59], s73 offen sc1\n"
    "s_waitcnt lgkmcnt(1)\n"
    "s_mov_b32 s73, 98304\n"
    "buffer_store_dwordx4 v[160:163], v254, s[56:59], s73 offen sc1\n"
    "s_waitcnt lgkmcnt(0)\n"
    "s_mov_b32 s73, 114688\n"
    "buffer_store_dwordx4 v[164:167], v254, s[56:59], s73 offen sc1\n"
    "s_branch LYE_%=_15\n"
    "LQ_%=_15:\n"
    "s_mov_b32 s52, s36\n"
    "s_and_b32 s53, s37, 0xffff\n"
    "s_mov_b32 s54, 4194304\n"
    "s_mov_b32 s55, 0x20000\n"
    "s_lshl_b32 s71, s79, 1\n"
    "s_add_u32 s82, s71, s63\n"
    "s_lshl_b32 s70, s82, 15\n"
    "s_lshl_b32 s71, s60, 13\n"
    "s_add_u32 s66, s70, s71\n"
    "s_xor_b32 s67, s66, 32768\n"
    "s_lshl_b32 s82, s82, 2\n"
    "s_add_u32 s82, s82, s60\n"
    "s_lshl_b32 s82, s82, 4\n"
    "s_add_u32 s82, s82, 24576\n"
    "s_lshr_b32 s70, s64, 1\n"
    "s_lshl_b32 s70, s70, 3\n"
    "s_lshl_b32 s71, s65, 1\n"
    "s_add_u32 s70, s70, s71\n"
    "s_lshr_b32 s71, s60, 1\n"
    "s_add_u32 s70, s70, s71\n"
    "s_and_b32 s71, s64, 1\n"
    "s_lshl_b32 s71, s71, 6\n"
    "s_add_u32 s71, s71, 32\n"
    "s_and_b32 s72, s60, 1\n"
    "s_lshl_b32 s72, s72, 4\n"
    "s_add_u32 s71, s71, s72\n"
    "s_lshl_b32 s72, s70, 8\n"
    "s_add_u32 s71, s71, s72\n"
    "s_add_u32 s71, s71, 8192\n"
    "v_and_b32 v249, 1, v240\n"
    "v_lshlrev_b32 v249, 7, v249\n"
    "v_add_u32 v249, s71, v249\n"
    "s_lshl_b32 s9, s70, 17\n"
    "s_and_b32 s71, s64, 1\n"
    "s_lshl_b32 s71, s71, 15\n"
    "s_add_u32 s9, s9, s71\n"
    "s_add_u32 s9, s9, 16384\n"
    "s_and_b32 s71, s60, 1\n"
    "s_lshl_b32 s71, s71, 13\n"
    "s_add_u32 s9, s9, s71\n"
    "s_mov_b32 s12, s38\n"
    "s_and_b32 s13, s39, 0xffff\n"
    "s_mov_b32 s14, 8388608\n"
    "s_mov_b32 s15, 0x20000\n"
    "s_waitcnt vmcnt(12)\n"
    "s_barrier\n"
    "s_waitcnt lgkmcnt(11)\n"
    "v_mfma_f32_16x16x32_bf16 v[0:3], a[64:67], a[0:3], v[0:3]\n"
    "ds_read_b128 a[56:59], v128 offset:14336\n"
    "v_mfma_f32_16x16x32_bf16 v[0:3], a[68:71], a[4:7], v[0:3]\n"
    "ds_read_b128 a[60:63], v129 offset:14336\n"
    "v_mfma_f32_16x16x32_bf16 v[4:7], a[72:75], a[0:3], v[4:7]\n"
    "v_mfma_f32_16x16x32_bf16 v[4:7], a[76:79], a[4:7], v[4:7]\n"
    "v_mfma_f32_16x16x32_bf16 v[8:11], a[80:83], a[0:3], v[8:11]\n"
    "v_mfma_f32_16x16x32_bf16 v[8:11], a[84:87], a[4:7], v[8:11]\n"
    "v_mfma_f32_16x16x32_bf16 v[12:15], a[88:91], a[0:3], v[12:15]\n"
    "v_mfma_f32_16x16x32_bf16 v[12:15], a[92:95], a[4:7], v[12:15]\n"
    "v_mfma_f32_16x16x32_bf16 v[16:19], a[64:67], a[8:11], v[16:19]\n"
    "ds_read_b128 a[0:3], v128 offset:16384\n"
    "s_waitcnt lgkmcnt(11)\n"
    "v_mfma_f32_16x16x32_bf16 v[16:19], a[68:71], a[12:15], v[16:19]\n"
    "v_mfma_f32_16x16x32_bf16 v[20:23], a[72:75], a[8:11], v[20:23]\n"
    "v_mfma_f32_16x16x32_bf16 v[20:23], a[76:79], a[12:15], v[20:23]\n"
    "v_mfma_f32_16x16x32_bf16 v[24:27], a[80:83], a[8:11], v[24:27]\n"
    "ds_read_b128 a[4:7], v129 offset:16384\n"
    "v_mfma_f32_16x16x32_bf16 v[24:27], a[84:87], a[12:15], v[24:27]\n"
    "v_mfma_f32_16x16x32_bf16 v[28:31], a[88:91], a[8:11], v[28:31]\n"
    "v_mfma_f32_16x16x32_bf16 v[28:31], a[92:95], a[12:15], v[28:31]\n"
    "v_mfma_f32_16x16x32_bf16 v[32:35], a[64:67], a[16:19], v[32:35]\n"
    "ds_read_b128 a[8:11], v128 offset:18432\n"
    "v_mfma_f32_16x16x32_bf16 v[32:35], a[68:71], a[20:23], v[32:35]\n"
    "v_mfma_f32_16x16x32_bf16 v[36:39], a[72:75], a[16:19], v[36:39]\n"
    "v_mfma_f32_16x16x32_bf16 v[36:39], a[76:79], a[20:23], v[36:39]\n"
    "v_mfma_f32_16x16x32_bf16 v[40:43], a[80:83], a[16:19], v[40:43]\n"
    "ds_read_b128 a[12:15], v129 offset:18432\n"
    "v_mfma_f32_16x16x32_bf16 v[40:43], a[84:87], a[20:23], v[40:43]\n"
    "v_mfma_f32_16x16x32_bf16 v[44:47], a[88:91], a[16:19], v[44:47]\n"
    "v_mfma_f32_16x16x32_bf16 v[44:47], a[92:95], a[20:23], v[44:47]\n"
    "s_waitcnt lgkmcnt(12)\n"
    "v_mfma_f32_16x16x32_bf16 v[48:51], a[64:67], a[24:27], v[48:51]\n"
    "ds_read_b128 a[16:19], v128 offset:20480\n"
    "v_mfma_f32_16x16x32_bf16 v[48:51], a[68:71], a[28:31], v[48:51]\n"
    "v_mfma_f32_16x16x32_bf16 v[52:55], a[72:75], a[24:27], v[52:55]\n"
    "v_mfma_f32_16x16x32_bf16 v[52:55], a[76:79], a[28:31], v[52:55]\n"
    "v_mfma_f32_16x16x32_bf16 v[56:59], a[80:83], a[24:27], v[56:59]\n"
    "ds_read_b128 a[20:23], v129 offset:20480\n"
    "v_mfma_f32_16x16x32_bf16 v[56:59], a[84:87], a[28:31], v[56:59]\n"
    "v_mfma_f32_16x16x32_bf16 v[60:63], a[88:91], a[24:27], v[60:63]\n"
    "v_mfma_f32_16x16x32_bf16 v[60:63], a[92:95], a[28:31], v[60:63]\n"
    "ds_read_b128 a[24:27], v128 offset:22528\n"
    "ds_read_b128 a[28:31], v129 offset:22528\n"
    "s_waitcnt lgkmcnt(4)\n"
    "v_mfma_f32_16x16x32_bf16 v[0:3], a[96:99], a[0:3], v[0:3]\n"
    "v_mfma_f32_16x16x32_bf16 v[0:3], a[100:103], a[4:7], v[0:3]\n"
    "global_load_dwordx2 v[252:253], v249, s[6:7] sc1\n"
    "v_mfma_f32_16x16x32_bf16 v[4:7], a[104:107], a[0:3], v[4:7]\n"
    "v_mfma_f32_16x16x32_bf16 v[4:7], a[108:111], a[4:7], v[4:7]\n"
    "v_mfma_f32_16x16x32_bf16 v[8:11], a[112:115], a[0:3], v[8:11]\n"
    "v_mfma_f32_16x16x32_bf16 v[8:11], a[116:119], a[4:7], v[8:11]\n"
    "v_mfma_f32_16x16x32_bf16 v[12:15], a[120:123], a[0:3], v[12:15]\n"
    "v_mfma_f32_16x16x32_bf16 v[12:15], a[124:127], a[4:7], v[12:15]\n"
    "v_mfma_f32_16x16x32_bf16 v[16:19], a[96:99], a[8:11], v[16:19]\n"
    "v_mfma_f32_16x16x32_bf16 v[16:19], a[100:103], a[12:15], v[16:19]\n"
    "v_mfma_f32_16x16x32_bf16 v[20:23], a[104:107], a[8:11], v[20:23]\n"
    "v_mfma_f32_16x16x32_bf16 v[20:23], a[108:111], a[12:15], v[20:23]\n"
    "v_mfma_f32_16x16x32_bf16 v[24:27], a[112:115], a[8:11], v[24:27]\n"
    "v_cvt_pk_bf16_f32 v200, v0, v1\n"
    "v_cvt_pk_bf16_f32 v201, v2, v3\n"
    "v_cvt_pk_bf16_f32 v202, v4, v5\n"
    "v_cvt_pk_bf16_f32 v203, v6, v7\n"
    "s_add_u32 s71, s66, 0\n"
    "buffer_store_dwordx4 v[200:203], v251, s[52:55], s71 offen offset:0 sc1\n"
    "v_mfma_f32_16x16x32_bf16 v[24:27], a[116:119], a[12:15], v[24:27]\n"
    "v_mfma_f32_16x16x32_bf16 v[28:31], a[120:123], a[8:11], v[28:31]\n"
    "v_mfma_f32_16x16x32_bf16 v[28:31], a[124:127], a[12:15], v[28:31]\n"
    "s_waitcnt lgkmcnt(2)\n"
    "v_mfma_f32_16x16x32_bf16 v[32:35], a[96:99], a[16:19], v[32:35]\n"
    "v_cvt_pk_bf16_f32 v204, v8, v9\n"
    "v_cvt_pk_bf16_f32 v205, v10, v11\n"
    "v_cvt_pk_bf16_f32 v206, v12, v13\n"
    "v_cvt_pk_bf16_f32 v207, v14, v15\n"
    "s_add_u32 s71, s66, 0\n"
    "buffer_store_dwordx4 v[204:207], v251, s[52:55], s71 offen offset:1024 sc1\n"
    "v_mfma_f32_16x16x32_bf16 v[32:35], a[100:103], a[20:23], v[32:35]\n"
    "v_mfma_f32_16x16x32_bf16 v[36:39], a[104:107], a[16:19], v[36:39]\n"
    "v_mfma_f32_16x16x32_bf16 v[36:39], a[108:111], a[20:23], v[36:39]\n"
    "v_mfma_f32_16x16x32_bf16 v[40:43], a[112:115], a[16:19], v[40:43]\n"
    "v_cvt_pk_bf16_f32 v200, v16, v17\n"
    "v_cvt_pk_bf16_f32 v201, v18, v19\n"
    "v_cvt_pk_bf16_f32 v202, v20, v21\n"
    "v_cvt_pk_bf16_f32 v203, v22, v23\n"
    "s_add_u32 s71, s66, 0\n"
    "buffer_store_dwordx4 v[200:203], v251, s[52:55], s71 offen offset:2048 sc1\n"
    "v_mfma_f32_16x16x32_bf16 v[40:43], a[116:119], a[20:23], v[40:43]\n"
    "v_mfma_f32_16x16x32_bf16 v[44:47], a[120:123], a[16:19], v[44:47]\n"
    "v_mfma_f32_16x16x32_bf16 v[44:47], a[124:127], a[20:23], v[44:47]\n"
    "s_waitcnt lgkmcnt(0)\n"
    "v_mfma_f32_16x16x32_bf16 v[48:51], a[96:99], a[24:27], v[48:51]\n"
    "v_cvt_pk_bf16_f32 v204, v24, v25\n"
    "v_cvt_pk_bf16_f32 v205, v26, v27\n"
    "v_cvt_pk_bf16_f32 v206, v28, v29\n"
    "v_cvt_pk_bf16_f32 v207, v30, v31\n"
    "s_add_u32 s71, s66, 0\n"
    "buffer_store_dwordx4 v[204:207], v251, s[52:55], s71 offen offset:3072 sc1\n"
    "v_mfma_f32_16x16x32_bf16 v[48:51], a[100:103], a[28:31], v[48:51]\n"
    "v_mfma_f32_16x16x32_bf16 v[52:55], a[104:107], a[24:27], v[52:55]\n"
    "v_mfma_f32_16x16x32_bf16 v[52:55], a[108:111], a[28:31], v[52:55]\n"
    "v_mfma_f32_16x16x32_bf16 v[56:59], a[112:115], a[24:27], v[56:59]\n"
    "v_cvt_pk_bf16_f32 v200, v32, v33\n"
    "v_cvt_pk_bf16_f32 v201, v34, v35\n"
    "v_cvt_pk_bf16_f32 v202, v36, v37\n"
    "v_cvt_pk_bf16_f32 v203, v38, v39\n"
    "s_add_u32 s71, s66, 4096\n"
    "buffer_store_dwordx4 v[200:203], v251, s[52:55], s71 offen offset:0 sc1\n"
    "v_mfma_f32_16x16x32_bf16 v[56:59], a[116:119], a[28:31], v[56:59]\n"
    "v_mfma_f32_16x16x32_bf16 v[60:63], a[120:123], a[24:27], v[60:63]\n"
    "v_mfma_f32_16x16x32_bf16 v[60:63], a[124:127], a[28:31], v[60:63]\n"
    "v_mfma_f32_16x16x32_bf16 v[64:67], a[64:67], a[32:35], v[64:67]\n"
    "v_mfma_f32_16x16x32_bf16 v[64:67], a[68:71], a[36:39], v[64:67]\n"
    "v_cvt_pk_bf16_f32 v204, v40, v41\n"
    "v_cvt_pk_bf16_f32 v205, v42, v43\n"
    "v_cvt_pk_bf16_f32 v206, v44, v45\n"
    "v_cvt_pk_bf16_f32 v207, v46, v47\n"
    "s_add_u32 s71, s66, 4096\n"
    "buffer_store_dwordx4 v[204:207], v251, s[52:55], s71 offen offset:1024 sc1\n"
    "v_mfma_f32_16x16x32_bf16 v[68:71], a[72:75], a[32:35], v[68:71]\n"
    "v_mfma_f32_16x16x32_bf16 v[68:71], a[76:79], a[36:39], v[68:71]\n"
    "v_mfma_f32_16x16x32_bf16 v[72:75], a[80:83], a[32:35], v[72:75]\n"
    "v_mfma_f32_16x16x32_bf16 v[72:75], a[84:87], a[36:39], v[72:75]\n"
    "v_cvt_pk_bf16_f32 v200, v48, v49\n"
    "v_cvt_pk_bf16_f32 v201, v50, v51\n"
    "v_cvt_pk_bf16_f32 v202, v52, v53\n"
    "v_cvt_pk_bf16_f32 v203, v54, v55\n"
    "s_add_u32 s71, s66, 4096\n"
    "buffer_store_dwordx4 v[200:203], v251, s[52:55], s71 offen offset:2048 sc1\n"
    "v_mfma_f32_16x16x32_bf16 v[76:79], a[88:91], a[32:35], v[76:79]\n"
    "v_mfma_f32_16x16x32_bf16 v[76:79], a[92:95], a[36:39], v[76:79]\n"
    "v_mfma_f32_16x16x32_bf16 v[80:83], a[64:67], a[40:43], v[80:83]\n"
    "ds_read_b128 a[32:35], v128 offset:24576\n"
    "v_mfma_f32_16x16x32_bf16 v[80:83], a[68:71], a[44:47], v[80:83]\n"
    "v_cvt_pk_bf16_f32 v204, v56, v57\n"
    "v_cvt_pk_bf16_f32 v205, v58, v59\n"
    "v_cvt_pk_bf16_f32 v206, v60, v61\n"
    "v_cvt_pk_bf16_f32 v207, v62, v63\n"
    "s_add_u32 s71, s66, 4096\n"
    "buffer_store_dwordx4 v[204:207], v251, s[52:55], s71 offen offset:3072 sc1\n"
    "v_mfma_f32_16x16x32_bf16 v[84:87], a[72:75], a[40:43], v[84:87]\n"
    "v_mfma_f32_16x16x32_bf16 v[84:87], a[76:79], a[44:47], v[84:87]\n"
    "v_mfma_f32_16x16x32_bf16 v[88:91], a[80:83], a[40:43], v[88:91]\n"
    "ds_read_b128 a[36:39], v129 offset:24576\n"
    "v_mfma_f32_16x16x32_bf16 v[88:91], a[84:87], a[44:47], v[88:91]\n"
    "v_mfma_f32_16x16x32_bf16 v[92:95], a[88:91], a[40:43], v[92:95]\n"
    "s_waitcnt vmcnt(8)\n"
    "v_cmp_ne_u64 vcc, s[4:5], v[252:253]\n"
    "v_mfma_f32_16x16x32_bf16 v[92:95], a[92:95], a[44:47], v[92:95]\n"
    "v_mfma_f32_16x16x32_bf16 v[96:99], a[64:67], a[48:51], v[96:99]\n"
    "ds_read_b128 a[40:43], v128 offset:26624\n"
    "v_mfma_f32_16x16x32_bf16 v[96:99], a[68:71], a[52:55], v[96:99]\n"
    "v_mfma_f32_16x16x32_bf16 v[100:103], a[72:75], a[48:51], v[100:103]\n"
    "v_mfma_f32_16x16x32_bf16 v[100:103], a[76:79], a[52:55], v[100:103]\n"
    "v_mfma_f32_16x16x32_bf16 v[104:107], a[80:83], a[48:51], v[104:107]\n"
    "s_cmp_eq_u64 vcc, 0\n"
    "s_cselect_b32 s14, s14, 0\n"
    "s_cselect_b32 s68, 1, 0\n"
    "ds_read_b128 a[44:47], v129 offset:26624\n"
    "v_mfma_f32_16x16x32_bf16 v[104:107], a[84:87], a[52:55], v[104:107]\n"
    "v_mfma_f32_16x16x32_bf16 v[108:111], a[88:91], a[48:51], v[108:111]\n"
    "s_add_u32 s73, s9, 0\n"
    "buffer_load_dwordx4 v[136:139], v251, s[12:15], s73 offen offset:0 sc1\n"
    "v_mfma_f32_16x16x32_bf16 v[108:111], a[92:95], a[52:55], v[108:111]\n"
    "v_mfma_f32_16x16x32_bf16 v[112:115], a[64:67], a[56:59], v[112:115]\n"
    "s_add_u32 s73, s9, 0\n"
    "buffer_load_dwordx4 v[140:143], v251, s[12:15], s73 offen offset:1024 sc1\n"
    "ds_read_b128 a[48:51], v128 offset:28672\n"
    "v_mfma_f32_16x16x32_bf16 v[112:115], a[68:71], a[60:63], v[112:115]\n"
    "v_mfma_f32_16x16x32_bf16 v[116:119], a[72:75], a[56:59], v[116:119]\n"
    "s_add_u32 s73, s9, 65536\n"
    "buffer_load_dwordx4 v[168:171], v251, s[12:15], s73 offen offset:0 sc1\n"
    "v_mfma_f32_16x16x32_bf16 v[116:119], a[76:79], a[60:63], v[116:119]\n"
    "v_mfma_f32_16x16x32_bf16 v[120:123], a[80:83], a[56:59], v[120:123]\n"
    "s_add_u32 s73, s9, 65536\n"
    "buffer_load_dwordx4 v[172:175], v251, s[12:15], s73 offen offset:1024 sc1\n"
    "ds_read_b128 a[52:55], v129 offset:28672\n"
    "v_mfma_f32_16x16x32_bf16 v[120:123], a[84:87], a[60:63], v[120:123]\n"
    "v_mfma_f32_16x16x32_bf16 v[124:127], a[88:91], a[56:59], v[124:127]\n"
    "v_mfma_f32_16x16x32_bf16 v[124:127], a[92:95], a[60:63], v[124:127]\n"
    "ds_read_b128 a[56:59], v128 offset:30720\n"
    "ds_read_b128 a[60:63], v129 offset:30720\n"
    "s_waitcnt lgkmcnt(6)\n"
    "v_mfma_f32_16x16x32_bf16 v[64:67], a[96:99], a[32:35], v[64:67]\n"
    "s_add_u32 s73, s9, 0\n"
    "buffer_load_dwordx4 v[144:147], v251, s[12:15], s73 offen offset:2048 sc1\n"
    "v_mfma_f32_16x16x32_bf16 v[64:67], a[100:103], a[36:39], v[64:67]\n"
    "v_mfma_f32_16x16x32_bf16 v[68:71], a[104:107], a[32:35], v[68:71]\n"
    "s_add_u32 s73, s9, 0\n"
    "buffer_load_dwordx4 v[148:151], v251, s[12:15], s73 offen offset:3072 sc1\n"
    "v_mfma_f32_16x16x32_bf16 v[68:71], a[108:111], a[36:39], v[68:71]\n"
    "v_mfma_f32_16x16x32_bf16 v[72:75], a[112:115], a[32:35], v[72:75]\n"
    "s_add_u32 s73, s9, 65536\n"
    "buffer_load_dwordx4 v[176:179], v251, s[12:15], s73 offen offset:2048 sc1\n"
    "v_mfma_f32_16x16x32_bf16 v[72:75], a[116:119], a[36:39], v[72:75]\n"
    "v_mfma_f32_16x16x32_bf16 v[76:79], a[120:123], a[32:35], v[76:79]\n"
    "v_mfma_f32_16x16x32_bf16 v[76:79], a[124:127], a[36:39], v[76:79]\n"
    "s_add_u32 s73, s9, 65536\n"
    "buffer_load_dwordx4 v[180:183], v251, s[12:15], s73 offen offset:3072 sc1\n"
    "s_waitcnt lgkmcnt(4)\n"
    "v_mfma_f32_16x16x32_bf16 v[80:83], a[96:99], a[40:43], v[80:83]\n"
    "v_mfma_f32_16x16x32_bf16 v[80:83], a[100:103], a[44:47], v[80:83]\n"
    "s_add_u32 s73, s9, 4096\n"
    "buffer_load_dwordx4 v[152:155], v251, s[12:15], s73 offen offset:0 sc1\n"
    "v_mfma_f32_16x16x32_bf16 v[84:87], a[104:107], a[40:43], v[84:87]\n"
    "v_mfma_f32_16x16x32_bf16 v[84:87], a[108:111], a[44:47], v[84:87]\n"
    "s_add_u32 s73, s9, 4096\n"
    "buffer_load_dwordx4 v[156:159], v251, s[12:15], s73 offen offset:1024 sc1\n"
    "v_mfma_f32_16x16x32_bf16 v[88:91], a[112:115], a[40:43], v[88:91]\n"
    "v_mfma_f32_16x16x32_bf16 v[88:91], a[116:119], a[44:47], v[88:91]\n"
    "v_mfma_f32_16x16x32_bf16 v[92:95], a[120:123], a[40:43], v[92:95]\n"
    "s_add_u32 s73, s9, 69632\n"
    "buffer_load_dwordx4 v[184:187], v251, s[12:15], s73 offen offset:0 sc1\n"
    "v_mfma_f32_16x16x32_bf16 v[92:95], a[124:127], a[44:47], v[92:95]\n"
    "s_waitcnt lgkmcnt(2)\n"
    "v_mfma_f32_16x16x32_bf16 v[96:99], a[96:99], a[48:51], v[96:99]\n"
    "s_add_u32 s73, s9, 69632\n"
    "buffer_load_dwordx4 v[188:191], v251, s[12:15], s73 offen offset:1024 sc1\n"
    "v_mfma_f32_16x16x32_bf16 v[96:99], a[100:103], a[52:55], v[96:99]\n"
    "v_mfma_f32_16x16x32_bf16 v[100:103], a[104:107], a[48:51], v[100:103]\n"
    "s_add_u32 s73, s9, 4096\n"
    "buffer_load_dwordx4 v[160:163], v251, s[12:15], s73 offen offset:2048 sc1\n"
    "v_mfma_f32_16x16x32_bf16 v[100:103], a[108:111], a[52:55], v[100:103]\n"
    "v_mfma_f32_16x16x32_bf16 v[104:107], a[112:115], a[48:51], v[104:107]\n"
    "v_mfma_f32_16x16x32_bf16 v[104:107], a[116:119], a[52:55], v[104:107]\n"
    "s_add_u32 s73, s9, 4096\n"
    "buffer_load_dwordx4 v[164:167], v251, s[12:15], s73 offen offset:3072 sc1\n"
    "v_mfma_f32_16x16x32_bf16 v[108:111], a[120:123], a[48:51], v[108:111]\n"
    "v_mfma_f32_16x16x32_bf16 v[108:111], a[124:127], a[52:55], v[108:111]\n"
    "s_add_u32 s73, s9, 69632\n"
    "buffer_load_dwordx4 v[192:195], v251, s[12:15], s73 offen offset:2048 sc1\n"
    "s_waitcnt lgkmcnt(0)\n"
    "v_mfma_f32_16x16x32_bf16 v[112:115], a[96:99], a[56:59], v[112:115]\n"
    "v_mfma_f32_16x16x32_bf16 v[112:115], a[100:103], a[60:63], v[112:115]\n"
    "s_add_u32 s73, s9, 69632\n"
    "buffer_load_dwordx4 v[196:199], v251, s[12:15], s73 offen offset:3072 sc1\n"
    "v_mfma_f32_16x16x32_bf16 v[116:119], a[104:107], a[56:59], v[116:119]\n"
    "v_mfma_f32_16x16x32_bf16 v[116:119], a[108:111], a[60:63], v[116:119]\n"
    "v_mfma_f32_16x16x32_bf16 v[120:123], a[112:115], a[56:59], v[120:123]\n"
    "v_mfma_f32_16x16x32_bf16 v[120:123], a[116:119], a[60:63], v[120:123]\n"
    "v_mfma_f32_16x16x32_bf16 v[124:127], a[120:123], a[56:59], v[124:127]\n"
    "v_mfma_f32_16x16x32_bf16 v[124:127], a[124:127], a[60:63], v[124:127]\n"
    "s_cmp_eq_u32 s68, 1\n"
    "s_cbranch_scc1 LDOK_%=_20\n"
    "s_mov_b64 s[10:11], exec\n"
    "s_mov_b64 exec, -1\n"
    "s_mov_b32 s3, 0\n"
    "global_load_dwordx2 v[252:253], v249, s[6:7] sc1\n"
    "LWP_%=_21:\n"
    "global_load_dwordx2 v[132:133], v249, s[6:7] sc1\n"
    "s_waitcnt vmcnt(1)\n"
    "v_cmp_ne_u64 vcc, s[4:5], v[252:253]\n"
    "s_nop 4\n"
    "s_cbranch_vccz LWD_%=_21\n"
    "global_load_dwordx2 v[252:253], v249, s[6:7] sc1\n"
    "s_waitcnt vmcnt(1)\n"
    "v_cmp_ne_u64 vcc, s[4:5], v[132:133]\n"
    "s_nop 4\n"
    "s_cbranch_vccz LWD_%=_21\n"
    "s_add_u32 s3, s3, 1\n"
    "s_cmp_lt_u32 s3, 0x10000\n"
    "s_cbranch_scc1 LWP_%=_21\n"
    "LWD_%=_21:\n"
    "s_mov_b64 exec, s[10:11]\n"
    "s_mov_b32 s14, 8388608\n"
    "s_add_u32 s73, s9, 0\n"
    "buffer_load_dwordx4 v[136:139], v251, s[12:15], s73 offen offset:0 sc1\n"
    "s_add_u32 s73, s9, 0\n"
    "buffer_load_dwordx4 v[140:143], v251, s[12:15], s73 offen offset:1024 sc1\n"
    "s_add_u32 s73, s9, 65536\n"
    "buffer_load_dwordx4 v[168:171], v251, s[12:15], s73 offen offset:0 sc1\n"
    "s_add_u32 s73, s9, 65536\n"
    "buffer_load_dwordx4 v[172:175], v251, s[12:15], s73 offen offset:1024 sc1\n"
    "s_add_u32 s73, s9, 0\n"
    "buffer_load_dwordx4 v[144:147], v251, s[12:15], s73 offen offset:2048 sc1\n"
    "s_add_u32 s73, s9, 0\n"
    "buffer_load_dwordx4 v[148:151], v251, s[12:15], s73 offen offset:3072 sc1\n"
    "s_add_u32 s73, s9, 65536\n"
    "buffer_load_dwordx4 v[176:179], v251, s[12:15], s73 offen offset:2048 sc1\n"
    "s_add_u32 s73, s9, 65536\n"
    "buffer_load_dwordx4 v[180:183], v251, s[12:15], s73 offen offset:3072 sc1\n"
    "s_add_u32 s73, s9, 4096\n"
    "buffer_load_dwordx4 v[152:155], v251, s[12:15], s73 offen offset:0 sc1\n"
    "s_add_u32 s73, s9, 4096\n"
    "buffer_load_dwordx4 v[156:159], v251, s[12:15], s73 offen offset:1024 sc1\n"
    "s_add_u32 s73, s9, 69632\n"
    "buffer_load_dwordx4 v[184:187], v251, s[12:15], s73 offen offset:0 sc1\n"
    "s_add_u32 s73, s9, 69632\n"
    "buffer_load_dwordx4 v[188:191], v251, s[12:15], s73 offen offset:1024 sc1\n"
    "s_add_u32 s73, s9, 4096\n"
    "buffer_load_dwordx4 v[160:163], v251, s[12:15], s73 offen offset:2048 sc1\n"
    "s_add_u32 s73, s9, 4096\n"
    "buffer_load_dwordx4 v[164:167], v251, s[12:15], s73 offen offset:3072 sc1\n"
    "s_add_u32 s73, s9, 69632\n"
    "buffer_load_dwordx4 v[192:195], v251, s[12:15], s73 offen offset:2048 sc1\n"
    "s_add_u32 s73, s9, 69632\n"
    "buffer_load_dwordx4 v[196:199], v251, s[12:15], s73 offen offset:3072 sc1\n"
    "LDOK_%=_20:\n"
    "s_waitcnt vmcnt(16)\n"
    "v_mov_b32 v249, s82\n"
    "v_mov_b32 v252, s4\n"
    "v_mov_b32 v253, s5\n"
    "s_mov_b64 s[10:11], exec\n"
    "s_mov_b64 exec, 1\n"
    "global_store_dwordx2 v249, v[252:253], s[6:7] sc1\n"
    "s_mov_b64 exec, s[10:11]\n"
    "ds_write_b128 v248, v[208:211] offset:0\n"
    "ds_write_b128 v248, v[212:215] offset:1024\n"
    "ds_write_b128 v248, v[216:219] offset:2048\n"
    "ds_write_b128 v248, v[220:223] offset:3072\n"
    "ds_write_b128 v248, v[224:227] offset:4096\n"
    "ds_write_b128 v248, v[228:231] offset:5120\n"
    "ds_write_b128 v248, v[232:235] offset:6144\n"
    "ds_write_b128 v248, v[236:239] offset:7168\n"
    "s_waitcnt lgkmcnt(0)\n"
    "ds_read_b64 v[208:209], v244 offset:0\n"
    "ds_read_b64 v[210:211], v245 offset:0\n"
    "ds_read_b64 v[212:213], v246 offset:0\n"
    "ds_read_b64 v[214:215], v247 offset:0\n"
    "ds_read_b64 v[216:217], v244 offset:2048\n"
    "ds_read_b64 v[218:219], v245 offset:2048\n"
    "ds_read_b64 v[220:221], v246 offset:2048\n"
    "ds_read_b64 v[222:223], v247 offset:2048\n"
    "ds_read_b64 v[224:225], v244 offset:4096\n"
    "ds_read_b64 v[226:227], v245 offset:4096\n"
    "ds_read_b64 v[228:229], v246 offset:4096\n"
    "ds_read_b64 v[230:231], v247 offset:4096\n"
    "ds_read_b64 v[232:233], v244 offset:6144\n"
    "ds_read_b64 v[234:235], v245 offset:6144\n"
    "ds_read_b64 v[236:237], v246 offset:6144\n"
    "ds_read_b64 v[238:239], v247 offset:6144\n"
    "s_waitcnt lgkmcnt(0)\n"
    "s_xor_b32 s71, s82, 64\n"
    "s_sub_u32 s71, s82, 24576\n"
    "s_xor_b32 s71, s71, 64\n"
    "s_add_u32 s71, s71, 24576\n"
    "v_mov_b32 v249, s71\n"
    "s_waitcnt vmcnt(13)\n"
    "s_mov_b32 s72, 0xbfb8aa3b\n"
    "s_mov_b32 s73, 0xbfb8aa3b\n"
    "s_mov_b32 s71, 0xffff0000\n"
    "v_lshlrev_b32 v200, 16, v136\n"
    "v_and_b32 v201, s71, v136\n"
    "v_lshlrev_b32 v202, 16, v137\n"
    "v_and_b32 v203, s71, v137\n"
    "v_lshlrev_b32 v204, 16, v168\n"
    "v_and_b32 v205, s71, v168\n"
    "v_lshlrev_b32 v206, 16, v169\n"
    "v_and_b32 v207, s71, v169\n"
    "v_pk_add_f32 v[0:1], v[200:201], v[204:205]\n"
    "v_pk_add_f32 v[2:3], v[202:203], v[206:207]\n"
    "v_pk_mul_f32 v[200:201], v[0:1], s[72:73]\n"
    "v_pk_mul_f32 v[202:203], v[2:3], s[72:73]\n"
    "v_exp_f32 v200, v200\n"
    "v_exp_f32 v201, v201\n"
    "v_exp_f32 v202, v202\n"
    "v_exp_f32 v203, v203\n"
    "s_nop 0\n"
    "v_pk_add_f32 v[200:201], v[200:201], 1.0 op_sel_hi:[1,0]\n"
    "v_pk_add_f32 v[202:203], v[202:203], 1.0 op_sel_hi:[1,0]\n"
    "v_rcp_f32 v200, v200\n"
    "v_rcp_f32 v201, v201\n"
    "v_rcp_f32 v202, v202\n"
    "v_rcp_f32 v203, v203\n"
    "s_nop 0\n"
    "v_pk_mul_f32 v[0:1], v[0:1], v[200:201]\n"
    "v_pk_mul_f32 v[2:3], v[2:3], v[202:203]\n"
    "v_lshlrev_b32 v200, 16, v138\n"
    "v_and_b32 v201, s71, v138\n"
    "v_lshlrev_b32 v202, 16, v139\n"
    "v_and_b32 v203, s71, v139\n"
    "v_lshlrev_b32 v204, 16, v170\n"
    "v_and_b32 v205, s71, v170\n"
    "v_lshlrev_b32 v206, 16, v171\n"
    "v_and_b32 v207, s71, v171\n"
    "v_pk_add_f32 v[4:5], v[200:201], v[204:205]\n"
    "v_pk_add_f32 v[6:7], v[202:203], v[206:207]\n"
    "v_pk_mul_f32 v[200:201], v[4:5], s[72:73]\n"
    "v_pk_mul_f32 v[202:203], v[6:7], s[72:73]\n"
    "v_exp_f32 v200, v200\n"
    "v_exp_f32 v201, v201\n"
    "v_exp_f32 v202, v202\n"
    "v_exp_f32 v203, v203\n"
    "s_nop 0\n"
    "v_pk_add_f32 v[200:201], v[200:201], 1.0 op_sel_hi:[1,0]\n"
    "v_pk_add_f32 v[202:203], v[202:203], 1.0 op_sel_hi:[1,0]\n"
    "v_rcp_f32 v200, v200\n"
    "v_rcp_f32 v201, v201\n"
    "v_rcp_f32 v202, v202\n"
    "v_rcp_f32 v203, v203\n"
    "s_nop 0\n"
    "v_pk_mul_f32 v[4:5], v[4:5], v[200:201]\n"
    "v_pk_mul_f32 v[6:7], v[6:7], v[202:203]\n"
    "v_lshlrev_b32 v200, 16, v140\n"
    "v_and_b32 v201, s71, v140\n"
    "v_lshlrev_b32 v202, 16, v141\n"
    "v_and_b32 v203, s71, v141\n"
    "v_lshlrev_b32 v204, 16, v172\n"
    "v_and_b32 v205, s71, v172\n"
    "v_lshlrev_b32 v206, 16, v173\n"
    "v_and_b32 v207, s71, v173\n"
    "v_pk_add_f32 v[8:9], v[200:201], v[204:205]\n"
    "v_pk_add_f32 v[10:11], v[202:203], v[206:207]\n"
    "v_pk_mul_f32 v[200:201], v[8:9], s[72:73]\n"
    "v_pk_mul_f32 v[202:203], v[10:11], s[72:73]\n"
    "v_exp_f32 v200, v200\n"
    "v_exp_f32 v201, v201\n"
    "v_exp_f32 v202, v202\n"
    "v_exp_f32 v203, v203\n"
    "s_nop 0\n"
    "v_pk_add_f32 v[200:201], v[200:201], 1.0 op_sel_hi:[1,0]\n"
    "v_pk_add_f32 v[202:203], v[202:203], 1.0 op_sel_hi:[1,0]\n"
    "v_rcp_f32 v200, v200\n"
    "v_rcp_f32 v201, v201\n"
    "v_rcp_f32 v202, v202\n"
    "v_rcp_f32 v203, v203\n"
    "s_nop 0\n"
    "v_pk_mul_f32 v[8:9], v[8:9], v[200:201]\n"
    "v_pk_mul_f32 v[10:11], v[10:11], v[202:203]\n"
    "v_lshlrev_b32 v200, 16, v142\n"
    "v_and_b32 v201, s71, v142\n"
    "v_lshlrev_b32 v202, 16, v143\n"
    "v_and_b32 v203, s71, v143\n"
    "v_lshlrev_b32 v204, 16, v174\n"
    "v_and_b32 v205, s71, v174\n"
    "v_lshlrev_b32 v206, 16, v175\n"
    "v_and_b32 v207, s71, v175\n"
    "v_pk_add_f32 v[12:13], v[200:201], v[204:205]\n"
    "v_pk_add_f32 v[14:15], v[202:203], v[206:207]\n"
    "v_pk_mul_f32 v[200:201], v[12:13], s[72:73]\n"
    "v_pk_mul_f32 v[202:203], v[14:15], s[72:73]\n"
    "v_exp_f32 v200, v200\n"
    "v_exp_f32 v201, v201\n"
    "v_exp_f32 v202, v202\n"
    "v_exp_f32 v203, v203\n"
    "s_nop 0\n"
    "v_pk_add_f32 v[200:201], v[200:201], 1.0 op_sel_hi:[1,0]\n"
    "v_pk_add_f32 v[202:203], v[202:203], 1.0 op_sel_hi:[1,0]\n"
    "v_rcp_f32 v200, v200\n"
    "v_rcp_f32 v201, v201\n"
    "v_rcp_f32 v202, v202\n"
    "v_rcp_f32 v203, v203\n"
    "s_nop 0\n"
    "v_pk_mul_f32 v[12:13], v[12:13], v[200:201]\n"
    "v_pk_mul_f32 v[14:15], v[14:15], v[202:203]\n"
    "global_load_dwordx2 v[252:253], v249, s[6:7] sc1\n"
    "s_waitcnt vmcnt(10)\n"
    "v_lshlrev_b32 v200, 16, v144\n"
    "v_and_b32 v201, s71, v144\n"
    "v_lshlrev_b32 v202, 16, v145\n"
    "v_and_b32 v203, s71, v145\n"
    "v_lshlrev_b32 v204, 16, v176\n"
    "v_and_b32 v205, s71, v176\n"
    "v_lshlrev_b32 v206, 16, v177\n"
    "v_and_b32 v207, s71, v177\n"
    "v_pk_add_f32 v[16:17], v[200:201], v[204:205]\n"
    "v_pk_add_f32 v[18:19], v[202:203], v[206:207]\n"
    "v_pk_mul_f32 v[200:201], v[16:17], s[72:73]\n"
    "v_pk_mul_f32 v[202:203], v[18:19], s[72:73]\n"
    "v_exp_f32 v200, v200\n"
    "v_exp_f32 v201, v201\n"
    "v_exp_f32 v202, v202\n"
    "v_exp_f32 v203, v203\n"
    "s_nop 0\n"
    "v_pk_add_f32 v[200:201], v[200:201], 1.0 op_sel_hi:[1,0]\n"
    "v_pk_add_f32 v[202:203], v[202:203], 1.0 op_sel_hi:[1,0]\n"
    "v_rcp_f32 v200, v200\n"
    "v_rcp_f32 v201, v201\n"
    "v_rcp_f32 v202, v202\n"
    "v_rcp_f32 v203, v203\n"
    "s_nop 0\n"
    "v_pk_mul_f32 v[16:17], v[16:17], v[200:201]\n"
    "v_pk_mul_f32 v[18:19], v[18:19], v[202:203]\n"
    "v_lshlrev_b32 v200, 16, v146\n"
    "v_and_b32 v201, s71, v146\n"
    "v_lshlrev_b32 v202, 16, v147\n"
    "v_and_b32 v203, s71, v147\n"
    "v_lshlrev_b32 v204, 16, v178\n"
    "v_and_b32 v205, s71, v178\n"
    "v_lshlrev_b32 v206, 16, v179\n"
    "v_and_b32 v207, s71, v179\n"
    "v_pk_add_f32 v[20:21], v[200:201], v[204:205]\n"
    "v_pk_add_f32 v[22:23], v[202:203], v[206:207]\n"
    "v_pk_mul_f32 v[200:201], v[20:21], s[72:73]\n"
    "v_pk_mul_f32 v[202:203], v[22:23], s[72:73]\n"
    "v_exp_f32 v200, v200\n"
    "v_exp_f32 v201, v201\n"
    "v_exp_f32 v202, v202\n"
    "v_exp_f32 v203, v203\n"
    "s_nop 0\n"
    "v_pk_add_f32 v[200:201], v[200:201], 1.0 op_sel_hi:[1,0]\n"
    "v_pk_add_f32 v[202:203], v[202:203], 1.0 op_sel_hi:[1,0]\n"
    "v_rcp_f32 v200, v200\n"
    "v_rcp_f32 v201, v201\n"
    "v_rcp_f32 v202, v202\n"
    "v_rcp_f32 v203, v203\n"
    "s_nop 0\n"
    "v_pk_mul_f32 v[20:21], v[20:21], v[200:201]\n"
    "v_pk_mul_f32 v[22:23], v[22:23], v[202:203]\n"
    "v_lshlrev_b32 v200, 16, v148\n"
    "v_and_b32 v201, s71, v148\n"
    "v_lshlrev_b32 v202, 16, v149\n"
    "v_and_b32 v203, s71, v149\n"
    "v_lshlrev_b32 v204, 16, v180\n"
    "v_and_b32 v205, s71, v180\n"
    "v_lshlrev_b32 v206, 16, v181\n"
    "v_and_b32 v207, s71, v181\n"
    "v_pk_add_f32 v[24:25], v[200:201], v[204:205]\n"
    "v_pk_add_f32 v[26:27], v[202:203], v[206:207]\n"
    "v_pk_mul_f32 v[200:201], v[24:25], s[72:73]\n"
    "v_pk_mul_f32 v[202:203], v[26:27], s[72:73]\n"
    "v_exp_f32 v200, v200\n"
    "v_exp_f32 v201, v201\n"
    "v_exp_f32 v202, v202\n"
    "v_exp_f32 v203, v203\n"
    "s_nop 0\n"
    "v_pk_add_f32 v[200:201], v[200:201], 1.0 op_sel_hi:[1,0]\n"
    "v_pk_add_f32 v[202:203], v[202:203], 1.0 op_sel_hi:[1,0]\n"
    "v_rcp_f32 v200, v200\n"
    "v_rcp_f32 v201, v201\n"
    "v_rcp_f32 v202, v202\n"
    "v_rcp_f32 v203, v203\n"
    "s_nop 0\n"
    "v_pk_mul_f32 v[24:25], v[24:25], v[200:201]\n"
    "v_pk_mul_f32 v[26:27], v[26:27], v[202:203]\n"
    "v_lshlrev_b32 v200, 16, v150\n"
    "v_and_b32 v201, s71, v150\n"
    "v_lshlrev_b32 v202, 16, v151\n"
    "v_and_b32 v203, s71, v151\n"
    "v_lshlrev_b32 v204, 16, v182\n"
    "v_and_b32 v205, s71, v182\n"
    "v_lshlrev_b32 v206, 16, v183\n"
    "v_and_b32 v207, s71, v183\n"
    "v_pk_add_f32 v[28:29], v[200:201], v[204:205]\n"
    "v_pk_add_f32 v[30:31], v[202:203], v[206:207]\n"
    "v_pk_mul_f32 v[200:201], v[28:29], s[72:73]\n"
    "v_pk_mul_f32 v[202:203], v[30:31], s[72:73]\n"
    "v_exp_f32 v200, v200\n"
    "v_exp_f32 v201, v201\n"
    "v_exp_f32 v202, v202\n"
    "v_exp_f32 v203, v203\n"
    "s_nop 0\n"
    "v_pk_add_f32 v[200:201], v[200:201], 1.0 op_sel_hi:[1,0]\n"
    "v_pk_add_f32 v[202:203], v[202:203], 1.0 op_sel_hi:[1,0]\n"
    "v_rcp_f32 v200, v200\n"
    "v_rcp_f32 v201, v201\n"
    "v_rcp_f32 v202, v202\n"
    "v_rcp_f32 v203, v203\n"
    "s_nop 0\n"
    "v_pk_mul_f32 v[28:29], v[28:29], v[200:201]\n"
    "v_pk_mul_f32 v[30:31], v[30:31], v[202:203]\n"
    "s_waitcnt vmcnt(0)\n"
    "v_cmp_ne_u64 vcc, s[4:5], v[252:253]\n"
    "s_nop 4\n"
    "s_cbranch_vccz LPOK_%=_22\n"
    "s_mov_b64 s[10:11], exec\n"
    "s_mov_b64 exec, -1\n"
    "s_mov_b32 s3, 0\n"
    "global_load_dwordx2 v[252:253], v249, s[6:7] sc1\n"
    "LWP_%=_23:\n"
    "global_load_dwordx2 v[132:133], v249, s[6:7] sc1\n"
    "s_waitcnt vmcnt(1)\n"
    "v_cmp_ne_u64 vcc, s[4:5], v[252:253]\n"
    "s_nop 4\n"
    "s_cbranch_vccz LWD_%=_23\n"
    "global_load_dwordx2 v[252:253], v249, s[6:7] sc1\n"
    "s_waitcnt vmcnt(1)\n"
    "v_cmp_ne_u64 vcc, s[4:5], v[132:133]\n"
    "s_nop 4\n"
    "s_cbranch_vccz LWD_%=_23\n"
    "s_add_u32 s3, s3, 1\n"
    "s_cmp_lt_u32 s3, 0x10000\n"
    "s_cbranch_scc1 LWP_%=_23\n"
    "LWD_%=_23:\n"
    "s_mov_b64 exec, s[10:11]\n"
    "LPOK_%=_22:\n"
    "s_add_u32 s66, s67, 0\n"
    "buffer_load_dwordx4 v[136:139], v251, s[52:55], s66 offen offset:0 sc1\n"
    "s_add_u32 s66, s67, 0\n"
    "buffer_load_dwordx4 v[140:143], v251, s[52:55], s66 offen offset:1024 sc1\n"
    "s_add_u32 s66, s67, 0\n"
    "buffer_load_dwordx4 v[144:147], v251, s[52:55], s66 offen offset:2048 sc1\n"
    "s_add_u32 s66, s67, 0\n"
    "buffer_load_dwordx4 v[148:151], v251, s[52:55], s66 offen offset:3072 sc1\n"
    "s_add_u32 s66, s67, 4096\n"
    "buffer_load_dwordx4 v[168:171], v251, s[52:55], s66 offen offset:0 sc1\n"
    "s_add_u32 s66, s67, 4096\n"
    "buffer_load_dwordx4 v[172:175], v251, s[52:55], s66 offen offset:1024 sc1\n"
    "s_add_u32 s66, s67, 4096\n"
    "buffer_load_dwordx4 v[176:179], v251, s[52:55], s66 offen offset:2048 sc1\n"
    "s_add_u32 s66, s67, 4096\n"
    "buffer_load_dwordx4 v[180:183], v251, s[52:55], s66 offen offset:3072 sc1\n"
    "s_mov_b32 s71, 0xffff0000\n"
    "v_lshlrev_b32 v200, 16, v152\n"
    "v_and_b32 v201, s71, v152\n"
    "v_lshlrev_b32 v202, 16, v153\n"
    "v_and_b32 v203, s71, v153\n"
    "v_lshlrev_b32 v204, 16, v184\n"
    "v_and_b32 v205, s71, v184\n"
    "v_lshlrev_b32 v206, 16, v185\n"
    "v_and_b32 v207, s71, v185\n"
    "v_pk_add_f32 v[32:33], v[200:201], v[204:205]\n"
    "v_pk_add_f32 v[34:35], v[202:203], v[206:207]\n"
    "v_pk_mul_f32 v[200:201], v[32:33], s[72:73]\n"
    "v_pk_mul_f32 v[202:203], v[34:35], s[72:73]\n"
    "v_exp_f32 v200, v200\n"
    "v_exp_f32 v201, v201\n"
    "v_exp_f32 v202, v202\n"
    "v_exp_f32 v203, v203\n"
    "s_nop 0\n"
    "v_pk_add_f32 v[200:201], v[200:201], 1.0 op_sel_hi:[1,0]\n"
    "v_pk_add_f32 v[202:203], v[202:203], 1.0 op_sel_hi:[1,0]\n"
    "v_rcp_f32 v200, v200\n"
    "v_rcp_f32 v201, v201\n"
    "v_rcp_f32 v202, v202\n"
    "v_rcp_f32 v203, v203\n"
    "s_nop 0\n"
    "v_pk_mul_f32 v[32:33], v[32:33], v[200:201]\n"
    "v_pk_mul_f32 v[34:35], v[34:35], v[202:203]\n"
    "v_lshlrev_b32 v200, 16, v154\n"
    "v_and_b32 v201, s71, v154\n"
    "v_lshlrev_b32 v202, 16, v155\n"
    "v_and_b32 v203, s71, v155\n"
    "v_lshlrev_b32 v204, 16, v186\n"
    "v_and_b32 v205, s71, v186\n"
    "v_lshlrev_b32 v206, 16, v187\n"
    "v_and_b32 v207, s71, v187\n"
    "v_pk_add_f32 v[36:37], v[200:201], v[204:205]\n"
    "v_pk_add_f32 v[38:39], v[202:203], v[206:207]\n"
    "v_pk_mul_f32 v[200:201], v[36:37], s[72:73]\n"
    "v_pk_mul_f32 v[202:203], v[38:39], s[72:73]\n"
    "v_exp_f32 v200, v200\n"
    "v_exp_f32 v201, v201\n"
    "v_exp_f32 v202, v202\n"
    "v_exp_f32 v203, v203\n"
    "s_nop 0\n"
    "v_pk_add_f32 v[200:201], v[200:201], 1.0 op_sel_hi:[1,0]\n"
    "v_pk_add_f32 v[202:203], v[202:203], 1.0 op_sel_hi:[1,0]\n"
    "v_rcp_f32 v200, v200\n"
    "v_rcp_f32 v201, v201\n"
    "v_rcp_f32 v202, v202\n"
    "v_rcp_f32 v203, v203\n"
    "s_nop 0\n"
    "v_pk_mul_f32 v[36:37], v[36:37], v[200:201]\n"
    "v_pk_mul_f32 v[38:39], v[38:39], v[202:203]\n"
    "v_lshlrev_b32 v200, 16, v156\n"
    "v_and_b32 v201, s71, v156\n"
    "v_lshlrev_b32 v202, 16, v157\n"
    "v_and_b32 v203, s71, v157\n"
    "v_lshlrev_b32 v204, 16, v188\n"
    "v_and_b32 v205, s71, v188\n"
    "v_lshlrev_b32 v206, 16, v189\n"
    "v_and_b32 v207, s71, v189\n"
    "v_pk_add_f32 v[40:41], v[200:201], v[204:205]\n"
    "v_pk_add_f32 v[42:43], v[202:203], v[206:207]\n"
    "v_pk_mul_f32 v[200:201], v[40:41], s[72:73]\n"
    "v_pk_mul_f32 v[202:203], v[42:43], s[72:73]\n"
    "v_exp_f32 v200, v200\n"
    "v_exp_f32 v201, v201\n"
    "v_exp_f32 v202, v202\n"
    "v_exp_f32 v203, v203\n"
    "s_nop 0\n"
    "v_pk_add_f32 v[200:201], v[200:201], 1.0 op_sel_hi:[1,0]\n"
    "v_pk_add_f32 v[202:203], v[202:203], 1.0 op_sel_hi:[1,0]\n"
    "v_rcp_f32 v200, v200\n"
    "v_rcp_f32 v201, v201\n"
    "v_rcp_f32 v202, v202\n"
    "v_rcp_f32 v203, v203\n"
    "s_nop 0\n"
    "v_pk_mul_f32 v[40:41], v[40:41], v[200:201]\n"
    "v_pk_mul_f32 v[42:43], v[42:43], v[202:203]\n"
    "v_lshlrev_b32 v200, 16, v158\n"
    "v_and_b32 v201, s71, v158\n"
    "v_lshlrev_b32 v202, 16, v159\n"
    "v_and_b32 v203, s71, v159\n"
    "v_lshlrev_b32 v204, 16, v190\n"
    "v_and_b32 v205, s71, v190\n"
    "v_lshlrev_b32 v206, 16, v191\n"
    "v_and_b32 v207, s71, v191\n"
    "v_pk_add_f32 v[44:45], v[200:201], v[204:205]\n"
    "v_pk_add_f32 v[46:47], v[202:203], v[206:207]\n"
    "v_pk_mul_f32 v[200:201], v[44:45], s[72:73]\n"
    "v_pk_mul_f32 v[202:203], v[46:47], s[72:73]\n"
    "v_exp_f32 v200, v200\n"
    "v_exp_f32 v201, v201\n"
    "v_exp_f32 v202, v202\n"
    "v_exp_f32 v203, v203\n"
    "s_nop 0\n"
    "v_pk_add_f32 v[200:201], v[200:201], 1.0 op_sel_hi:[1,0]\n"
    "v_pk_add_f32 v[202:203], v[202:203], 1.0 op_sel_hi:[1,0]\n"
    "v_rcp_f32 v200, v200\n"
    "v_rcp_f32 v201, v201\n"
    "v_rcp_f32 v202, v202\n"
    "v_rcp_f32 v203, v203\n"
    "s_nop 0\n"
    "v_pk_mul_f32 v[44:45], v[44:45], v[200:201]\n"
    "v_pk_mul_f32 v[46:47], v[46:47], v[202:203]\n"
    "v_lshlrev_b32 v200, 16, v160\n"
    "v_and_b32 v201, s71, v160\n"
    "v_lshlrev_b32 v202, 16, v161\n"
    "v_and_b32 v203, s71, v161\n"
    "v_lshlrev_b32 v204, 16, v192\n"
    "v_and_b32 v205, s71, v192\n"
    "v_lshlrev_b32 v206, 16, v193\n"
    "v_and_b32 v207, s71, v193\n"
    "v_pk_add_f32 v[48:49], v[200:201], v[204:205]\n"
    "v_pk_add_f32 v[50:51], v[202:203], v[206:207]\n"
    "v_pk_mul_f32 v[200:201], v[48:49], s[72:73]\n"
    "v_pk_mul_f32 v[202:203], v[50:51], s[72:73]\n"
    "v_exp_f32 v200, v200\n"
    "v_exp_f32 v201, v201\n"
    "v_exp_f32 v202, v202\n"
    "v_exp_f32 v203, v203\n"
    "s_nop 0\n"
    "v_pk_add_f32 v[200:201], v[200:201], 1.0 op_sel_hi:[1,0]\n"
    "v_pk_add_f32 v[202:203], v[202:203], 1.0 op_sel_hi:[1,0]\n"
    "v_rcp_f32 v200, v200\n"
    "v_rcp_f32 v201, v201\n"
    "v_rcp_f32 v202, v202\n"
    "v_rcp_f32 v203, v203\n"
    "s_nop 0\n"
    "v_pk_mul_f32 v[48:49], v[48:49], v[200:201]\n"
    "v_pk_mul_f32 v[50:51], v[50:51], v[202:203]\n"
    "v_lshlrev_b32 v200, 16, v162\n"
    "v_and_b32 v201, s71, v162\n"
    "v_lshlrev_b32 v202, 16, v163\n"
    "v_and_b32 v203, s71, v163\n"
    "v_lshlrev_b32 v204, 16, v194\n"
    "v_and_b32 v205, s71, v194\n"
    "v_lshlrev_b32 v206, 16, v195\n"
    "v_and_b32 v207, s71, v195\n"
    "v_pk_add_f32 v[52:53], v[200:201], v[204:205]\n"
    "v_pk_add_f32 v[54:55], v[202:203], v[206:207]\n"
    "v_pk_mul_f32 v[200:201], v[52:53], s[72:73]\n"
    "v_pk_mul_f32 v[202:203], v[54:55], s[72:73]\n"
    "v_exp_f32 v200, v200\n"
    "v_exp_f32 v201, v201\n"
    "v_exp_f32 v202, v202\n"
    "v_exp_f32 v203, v203\n"
    "s_nop 0\n"
    "v_pk_add_f32 v[200:201], v[200:201], 1.0 op_sel_hi:[1,0]\n"
    "v_pk_add_f32 v[202:203], v[202:203], 1.0 op_sel_hi:[1,0]\n"
    "v_rcp_f32 v200, v200\n"
    "v_rcp_f32 v201, v201\n"
    "v_rcp_f32 v202, v202\n"
    "v_rcp_f32 v203, v203\n"
    "s_nop 0\n"
    "v_pk_mul_f32 v[52:53], v[52:53], v[200:201]\n"
    "v_pk_mul_f32 v[54:55], v[54:55], v[202:203]\n"
    "v_lshlrev_b32 v200, 16, v164\n"
    "v_and_b32 v201, s71, v164\n"
    "v_lshlrev_b32 v202, 16, v165\n"
    "v_and_b32 v203, s71, v165\n"
    "v_lshlrev_b32 v204, 16, v196\n"
    "v_and_b32 v205, s71, v196\n"
    "v_lshlrev_b32 v206, 16, v197\n"
    "v_and_b32 v207, s71, v197\n"
    "v_pk_add_f32 v[56:57], v[200:201], v[204:205]\n"
    "v_pk_add_f32 v[58:59], v[202:203], v[206:207]\n"
    "v_pk_mul_f32 v[200:201], v[56:57], s[72:73]\n"
    "v_pk_mul_f32 v[202:203], v[58:59], s[72:73]\n"
    "v_exp_f32 v200, v200\n"
    "v_exp_f32 v201, v201\n"
    "v_exp_f32 v202, v202\n"
    "v_exp_f32 v203, v203\n"
    "s_nop 0\n"
    "v_pk_add_f32 v[200:201], v[200:201], 1.0 op_sel_hi:[1,0]\n"
    "v_pk_add_f32 v[202:203], v[202:203], 1.0 op_sel_hi:[1,0]\n"
    "v_rcp_f32 v200, v200\n"
    "v_rcp_f32 v201, v201\n"
    "v_rcp_f32 v202, v202\n"
    "v_rcp_f32 v203, v203\n"
    "s_nop 0\n"
    "v_pk_mul_f32 v[56:57], v[56:57], v[200:201]\n"
    "v_pk_mul_f32 v[58:59], v[58:59], v[202:203]\n"
    "v_lshlrev_b32 v200, 16, v166\n"
    "v_and_b32 v201, s71, v166\n"
    "v_lshlrev_b32 v202, 16, v167\n"
    "v_and_b32 v203, s71, v167\n"
    "v_lshlrev_b32 v204, 16, v198\n"
    "v_and_b32 v205, s71, v198\n"
    "v_lshlrev_b32 v206, 16, v199\n"
    "v_and_b32 v207, s71, v199\n"
    "v_pk_add_f32 v[60:61], v[200:201], v[204:205]\n"
    "v_pk_add_f32 v[62:63], v[202:203], v[206:207]\n"
    "v_pk_mul_f32 v[200:201], v[60:61], s[72:73]\n"
    "v_pk_mul_f32 v[202:203], v[62:63], s[72:73]\n"
    "v_exp_f32 v200, v200\n"
    "v_exp_f32 v201, v201\n"
    "v_exp_f32 v202, v202\n"
    "v_exp_f32 v203, v203\n"
    "s_nop 0\n"
    "v_pk_add_f32 v[200:201], v[200:201], 1.0 op_sel_hi:[1,0]\n"
    "v_pk_add_f32 v[202:203], v[202:203], 1.0 op_sel_hi:[1,0]\n"
    "v_rcp_f32 v200, v200\n"
    "v_rcp_f32 v201, v201\n"
    "v_rcp_f32 v202, v202\n"
    "v_rcp_f32 v203, v203\n"
    "s_nop 0\n"
    "v_pk_mul_f32 v[60:61], v[60:61], v[200:201]\n"
    "v_pk_mul_f32 v[62:63], v[62:63], v[202:203]\n"
    "v_lshlrev_b32 v200, 16, v208\n"
    "v_and_b32 v201, s71, v208\n"
    "v_lshlrev_b32 v202, 16, v209\n"
    "v_and_b32 v203, s71, v209\n"
    "v_pk_fma_f32 v[64:65], v[64:65], v[0:1], v[200:201] neg_lo:[1,0,0] neg_hi:[1,0,0]\n"
    "v_pk_fma_f32 v[66:67], v[66:67], v[2:3], v[202:203] neg_lo:[1,0,0] neg_hi:[1,0,0]\n"
    "v_lshlrev_b32 v200, 16, v210\n"
    "v_and_b32 v201, s71, v210\n"
    "v_lshlrev_b32 v202, 16, v211\n"
    "v_and_b32 v203, s71, v211\n"
    "v_pk_fma_f32 v[68:69], v[68:69], v[4:5], v[200:201] neg_lo:[1,0,0] neg_hi:[1,0,0]\n"
    "v_pk_fma_f32 v[70:71], v[70:71], v[6:7], v[202:203] neg_lo:[1,0,0] neg_hi:[1,0,0]\n"
    "v_lshlrev_b32 v200, 16, v212\n"
    "v_and_b32 v201, s71, v212\n"
    "v_lshlrev_b32 v202, 16, v213\n"
    "v_and_b32 v203, s71, v213\n"
    "v_pk_fma_f32 v[72:73], v[72:73], v[8:9], v[200:201] neg_lo:[1,0,0] neg_hi:[1,0,0]\n"
    "v_pk_fma_f32 v[74:75], v[74:75], v[10:11], v[202:203] neg_lo:[1,0,0] neg_hi:[1,0,0]\n"
    "v_lshlrev_b32 v200, 16, v214\n"
    "v_and_b32 v201, s71, v214\n"
    "v_lshlrev_b32 v202, 16, v215\n"
    "v_and_b32 v203, s71, v215\n"
    "v_pk_fma_f32 v[76:77], v[76:77], v[12:13], v[200:201] neg_lo:[1,0,0] neg_hi:[1,0,0]\n"
    "v_pk_fma_f32 v[78:79], v[78:79], v[14:15], v[202:203] neg_lo:[1,0,0] neg_hi:[1,0,0]\n"
    "v_lshlrev_b32 v200, 16, v216\n"
    "v_and_b32 v201, s71, v216\n"
    "v_lshlrev_b32 v202, 16, v217\n"
    "v_and_b32 v203, s71, v217\n"
    "v_pk_fma_f32 v[80:81], v[80:81], v[16:17], v[200:201] neg_lo:[1,0,0] neg_hi:[1,0,0]\n"
    "v_pk_fma_f32 v[82:83], v[82:83], v[18:19], v[202:203] neg_lo:[1,0,0] neg_hi:[1,0,0]\n"
    "v_lshlrev_b32 v200, 16, v218\n"
    "v_and_b32 v201, s71, v218\n"
    "v_lshlrev_b32 v202, 16, v219\n"
    "v_and_b32 v203, s71, v219\n"
    "v_pk_fma_f32 v[84:85], v[84:85], v[20:21], v[200:201] neg_lo:[1,0,0] neg_hi:[1,0,0]\n"
    "v_pk_fma_f32 v[86:87], v[86:87], v[22:23], v[202:203] neg_lo:[1,0,0] neg_hi:[1,0,0]\n"
    "v_lshlrev_b32 v200, 16, v220\n"
    "v_and_b32 v201, s71, v220\n"
    "v_lshlrev_b32 v202, 16, v221\n"
    "v_and_b32 v203, s71, v221\n"
    "v_pk_fma_f32 v[88:89], v[88:89], v[24:25], v[200:201] neg_lo:[1,0,0] neg_hi:[1,0,0]\n"
    "v_pk_fma_f32 v[90:91], v[90:91], v[26:27], v[202:203] neg_lo:[1,0,0] neg_hi:[1,0,0]\n"
    "v_lshlrev_b32 v200, 16, v222\n"
    "v_and_b32 v201, s71, v222\n"
    "v_lshlrev_b32 v202, 16, v223\n"
    "v_and_b32 v203, s71, v223\n"
    "v_pk_fma_f32 v[92:93], v[92:93], v[28:29], v[200:201] neg_lo:[1,0,0] neg_hi:[1,0,0]\n"
    "v_pk_fma_f32 v[94:95], v[94:95], v[30:31], v[202:203] neg_lo:[1,0,0] neg_hi:[1,0,0]\n"
    "v_lshlrev_b32 v200, 16, v224\n"
    "v_and_b32 v201, s71, v224\n"
    "v_lshlrev_b32 v202, 16, v225\n"
    "v_and_b32 v203, s71, v225\n"
    "v_pk_fma_f32 v[96:97], v[96:97], v[32:33], v[200:201] neg_lo:[1,0,0] neg_hi:[1,0,0]\n"
    "v_pk_fma_f32 v[98:99], v[98:99], v[34:35], v[202:203] neg_lo:[1,0,0] neg_hi:[1,0,0]\n"
    "v_lshlrev_b32 v200, 16, v226\n"
    "v_and_b32 v201, s71, v226\n"
    "v_lshlrev_b32 v202, 16, v227\n"
    "v_and_b32 v203, s71, v227\n"
    "v_pk_fma_f32 v[100:101], v[100:101], v[36:37], v[200:201] neg_lo:[1,0,0] neg_hi:[1,0,0]\n"
    "v_pk_fma_f32 v[102:103], v[102:103], v[38:39], v[202:203] neg_lo:[1,0,0] neg_hi:[1,0,0]\n"
    "v_lshlrev_b32 v200, 16, v228\n"
    "v_and_b32 v201, s71, v228\n"
    "v_lshlrev_b32 v202, 16, v229\n"
    "v_and_b32 v203, s71, v229\n"
    "v_pk_fma_f32 v[104:105], v[104:105], v[40:41], v[200:201] neg_lo:[1,0,0] neg_hi:[1,0,0]\n"
    "v_pk_fma_f32 v[106:107], v[106:107], v[42:43], v[202:203] neg_lo:[1,0,0] neg_hi:[1,0,0]\n"
    "v_lshlrev_b32 v200, 16, v230\n"
    "v_and_b32 v201, s71, v230\n"
    "v_lshlrev_b32 v202, 16, v231\n"
    "v_and_b32 v203, s71, v231\n"
    "v_pk_fma_f32 v[108:109], v[108:109], v[44:45], v[200:201] neg_lo:[1,0,0] neg_hi:[1,0,0]\n"
    "v_pk_fma_f32 v[110:111], v[110:111], v[46:47], v[202:203] neg_lo:[1,0,0] neg_hi:[1,0,0]\n"
    "v_lshlrev_b32 v200, 16, v232\n"
    "v_and_b32 v201, s71, v232\n"
    "v_lshlrev_b32 v202, 16, v233\n"
    "v_and_b32 v203, s71, v233\n"
    "v_pk_fma_f32 v[112:113], v[112:113], v[48:49], v[200:201] neg_lo:[1,0,0] neg_hi:[1,0,0]\n"
    "v_pk_fma_f32 v[114:115], v[114:115], v[50:51], v[202:203] neg_lo:[1,0,0] neg_hi:[1,0,0]\n"
    "v_lshlrev_b32 v200, 16, v234\n"
    "v_and_b32 v201, s71, v234\n"
    "v_lshlrev_b32 v202, 16, v235\n"
    "v_and_b32 v203, s71, v235\n"
    "v_pk_fma_f32 v[116:117], v[116:117], v[52:53], v[200:201] neg_lo:[1,0,0] neg_hi:[1,0,0]\n"
    "v_pk_fma_f32 v[118:119], v[118:119], v[54:55], v[202:203] neg_lo:[1,0,0] neg_hi:[1,0,0]\n"
    "v_lshlrev_b32 v200, 16, v236\n"
    "v_and_b32 v201, s71, v236\n"
    "v_lshlrev_b32 v202, 16, v237\n"
    "v_and_b32 v203, s71, v237\n"
    "v_pk_fma_f32 v[120:121], v[120:121], v[56:57], v[200:201] neg_lo:[1,0,0] neg_hi:[1,0,0]\n"
    "v_pk_fma_f32 v[122:123], v[122:123], v[58:59], v[202:203] neg_lo:[1,0,0] neg_hi:[1,0,0]\n"
    "v_lshlrev_b32 v200, 16, v238\n"
    "v_and_b32 v201, s71, v238\n"
    "v_lshlrev_b32 v202, 16, v239\n"
    "v_and_b32 v203, s71, v239\n"
    "v_pk_fma_f32 v[124:125], v[124:125], v[60:61], v[200:201] neg_lo:[1,0,0] neg_hi:[1,0,0]\n"
    "v_pk_fma_f32 v[126:127], v[126:127], v[62:63], v[202:203] neg_lo:[1,0,0] neg_hi:[1,0,0]\n"
    "s_waitcnt vmcnt(0)\n"
    "v_lshlrev_b32 v200, 16, v136\n"
    "v_and_b32 v201, s71, v136\n"
    "v_lshlrev_b32 v202, 16, v137\n"
    "v_and_b32 v203, s71, v137\n"
    "v_pk_fma_f32 v[64:65], v[200:201], v[0:1], v[64:65]\n"
    "v_pk_fma_f32 v[66:67], v[202:203], v[2:3], v[66:67]\n"
    "v_cvt_pk_bf16_f32 v204, v64, v65\n"
    "v_cvt_pk_bf16_f32 v205, v66, v67\n"
    "ds_write_b64 v244, v[204:205] offset:0\n"
    "v_lshlrev_b32 v200, 16, v138\n"
    "v_and_b32 v201, s71, v138\n"
    "v_lshlrev_b32 v202, 16, v139\n"
    "v_and_b32 v203, s71, v139\n"
    "v_pk_fma_f32 v[68:69], v[200:201], v[4:5], v[68:69]\n"
    "v_pk_fma_f32 v[70:71], v[202:203], v[6:7], v[70:71]\n"
    "v_cvt_pk_bf16_f32 v206, v68, v69\n"
    "v_cvt_pk_bf16_f32 v207, v70, v71\n"
    "ds_write_b64 v245, v[206:207] offset:0\n"
    "v_lshlrev_b32 v200, 16, v140\n"
    "v_and_b32 v201, s71, v140\n"
    "v_lshlrev_b32 v202, 16, v141\n"
    "v_and_b32 v203, s71, v141\n"
    "v_pk_fma_f32 v[72:73], v[200:201], v[8:9], v[72:73]\n"
    "v_pk_fma_f32 v[74:75], v[202:203], v[10:11], v[74:75]\n"
    "v_cvt_pk_bf16_f32 v204, v72, v73\n"
    "v_cvt_pk_bf16_f32 v205, v74, v75\n"
    "ds_write_b64 v246, v[204:205] offset:0\n"
    "v_lshlrev_b32 v200, 16, v142\n"
    "v_and_b32 v201, s71, v142\n"
    "v_lshlrev_b32 v202, 16, v143\n"
    "v_and_b32 v203, s71, v143\n"
    "v_pk_fma_f32 v[76:77], v[200:201], v[12:13], v[76:77]\n"
    "v_pk_fma_f32 v[78:79], v[202:203], v[14:15], v[78:79]\n"
    "v_cvt_pk_bf16_f32 v206, v76, v77\n"
    "v_cvt_pk_bf16_f32 v207, v78, v79\n"
    "ds_write_b64 v247, v[206:207] offset:0\n"
    "v_lshlrev_b32 v200, 16, v144\n"
    "v_and_b32 v201, s71, v144\n"
    "v_lshlrev_b32 v202, 16, v145\n"
    "v_and_b32 v203, s71, v145\n"
    "v_pk_fma_f32 v[80:81], v[200:201], v[16:17], v[80:81]\n"
    "v_pk_fma_f32 v[82:83], v[202:203], v[18:19], v[82:83]\n"
    "v_cvt_pk_bf16_f32 v204, v80, v81\n"
    "v_cvt_pk_bf16_f32 v205, v82, v83\n"
    "ds_write_b64 v244, v[204:205] offset:2048\n"
    "v_lshlrev_b32 v200, 16, v146\n"
    "v_and_b32 v201, s71, v146\n"
    "v_lshlrev_b32 v202, 16, v147\n"
    "v_and_b32 v203, s71, v147\n"
    "v_pk_fma_f32 v[84:85], v[200:201], v[20:21], v[84:85]\n"
    "v_pk_fma_f32 v[86:87], v[202:203], v[22:23], v[86:87]\n"
    "v_cvt_pk_bf16_f32 v206, v84, v85\n"
    "v_cvt_pk_bf16_f32 v207, v86, v87\n"
    "ds_write_b64 v245, v[206:207] offset:2048\n"
    "v_lshlrev_b32 v200, 16, v148\n"
    "v_and_b32 v201, s71, v148\n"
    "v_lshlrev_b32 v202, 16, v149\n"
    "v_and_b32 v203, s71, v149\n"
    "v_pk_fma_f32 v[88:89], v[200:201], v[24:25], v[88:89]\n"
    "v_pk_fma_f32 v[90:91], v[202:203], v[26:27], v[90:91]\n"
    "v_cvt_pk_bf16_f32 v204, v88, v89\n"
    "v_cvt_pk_bf16_f32 v205, v90, v91\n"
    "ds_write_b64 v246, v[204:205] offset:2048\n"
    "v_lshlrev_b32 v200, 16, v150\n"
    "v_and_b32 v201, s71, v150\n"
    "v_lshlrev_b32 v202, 16, v151\n"
    "v_and_b32 v203, s71, v151\n"
    "v_pk_fma_f32 v[92:93], v[200:201], v[28:29], v[92:93]\n"
    "v_pk_fma_f32 v[94:95], v[202:203], v[30:31], v[94:95]\n"
    "v_cvt_pk_bf16_f32 v206, v92, v93\n"
    "v_cvt_pk_bf16_f32 v207, v94, v95\n"
    "ds_write_b64 v247, v[206:207] offset:2048\n"
    "v_lshlrev_b32 v200, 16, v168\n"
    "v_and_b32 v201, s71, v168\n"
    "v_lshlrev_b32 v202, 16, v169\n"
    "v_and_b32 v203, s71, v169\n"
    "v_pk_fma_f32 v[96:97], v[200:201], v[32:33], v[96:97]\n"
    "v_pk_fma_f32 v[98:99], v[202:203], v[34:35], v[98:99]\n"
    "v_cvt_pk_bf16_f32 v204, v96, v97\n"
    "v_cvt_pk_bf16_f32 v205, v98, v99\n"
    "ds_write_b64 v244, v[204:205] offset:4096\n"
    "v_lshlrev_b32 v200, 16, v170\n"
    "v_and_b32 v201, s71, v170\n"
    "v_lshlrev_b32 v202, 16, v171\n"
    "v_and_b32 v203, s71, v171\n"
    "v_pk_fma_f32 v[100:101], v[200:201], v[36:37], v[100:101]\n"
    "v_pk_fma_f32 v[102:103], v[202:203], v[38:39], v[102:103]\n"
    "v_cvt_pk_bf16_f32 v206, v100, v101\n"
    "v_cvt_pk_bf16_f32 v207, v102, v103\n"
    "ds_write_b64 v245, v[206:207] offset:4096\n"
    "v_lshlrev_b32 v200, 16, v172\n"
    "v_and_b32 v201, s71, v172\n"
    "v_lshlrev_b32 v202, 16, v173\n"
    "v_and_b32 v203, s71, v173\n"
    "v_pk_fma_f32 v[104:105], v[200:201], v[40:41], v[104:105]\n"
    "v_pk_fma_f32 v[106:107], v[202:203], v[42:43], v[106:107]\n"
    "v_cvt_pk_bf16_f32 v204, v104, v105\n"
    "v_cvt_pk_bf16_f32 v205, v106, v107\n"
    "ds_write_b64 v246, v[204:205] offset:4096\n"
    "v_lshlrev_b32 v200, 16, v174\n"
    "v_and_b32 v201, s71, v174\n"
    "v_lshlrev_b32 v202, 16, v175\n"
    "v_and_b32 v203, s71, v175\n"
    "v_pk_fma_f32 v[108:109], v[200:201], v[44:45], v[108:109]\n"
    "v_pk_fma_f32 v[110:111], v[202:203], v[46:47], v[110:111]\n"
    "v_cvt_pk_bf16_f32 v206, v108, v109\n"
    "v_cvt_pk_bf16_f32 v207, v110, v111\n"
    "ds_write_b64 v247, v[206:207] offset:4096\n"
    "v_lshlrev_b32 v200, 16, v176\n"
    "v_and_b32 v201, s71, v176\n"
    "v_lshlrev_b32 v202, 16, v177\n"
    "v_and_b32 v203, s71, v177\n"
    "v_pk_fma_f32 v[112:113], v[200:201], v[48:49], v[112:113]\n"
    "v_pk_fma_f32 v[114:115], v[202:203], v[50:51], v[114:115]\n"
    "v_cvt_pk_bf16_f32 v204, v112, v113\n"
    "v_cvt_pk_bf16_f32 v205, v114, v115\n"
    "ds_write_b64 v244, v[204:205] offset:6144\n"
    "v_lshlrev_b32 v200, 16, v178\n"
    "v_and_b32 v201, s71, v178\n"
    "v_lshlrev_b32 v202, 16, v179\n"
    "v_and_b32 v203, s71, v179\n"
    "v_pk_fma_f32 v[116:117], v[200:201], v[52:53], v[116:117]\n"
    "v_pk_fma_f32 v[118:119], v[202:203], v[54:55], v[118:119]\n"
    "v_cvt_pk_bf16_f32 v206, v116, v117\n"
    "v_cvt_pk_bf16_f32 v207, v118, v119\n"
    "ds_write_b64 v245, v[206:207] offset:6144\n"
    "v_lshlrev_b32 v200, 16, v180\n"
    "v_and_b32 v201, s71, v180\n"
    "v_lshlrev_b32 v202, 16, v181\n"
    "v_and_b32 v203, s71, v181\n"
    "v_pk_fma_f32 v[120:121], v[200:201], v[56:57], v[120:121]\n"
    "v_pk_fma_f32 v[122:123], v[202:203], v[58:59], v[122:123]\n"
    "v_cvt_pk_bf16_f32 v204, v120, v121\n"
    "v_cvt_pk_bf16_f32 v205, v122, v123\n"
    "ds_write_b64 v246, v[204:205] offset:6144\n"
    "v_lshlrev_b32 v200, 16, v182\n"
    "v_and_b32 v201, s71, v182\n"
    "v_lshlrev_b32 v202, 16, v183\n"
    "v_and_b32 v203, s71, v183\n"
    "v_pk_fma_f32 v[124:125], v[200:201], v[60:61], v[124:125]\n"
    "v_pk_fma_f32 v[126:127], v[202:203], v[62:63], v[126:127]\n"
    "v_cvt_pk_bf16_f32 v206, v124, v125\n"
    "v_cvt_pk_bf16_f32 v207, v126, v127\n"
    "ds_write_b64 v247, v[206:207] offset:6144\n"
    "s_waitcnt lgkmcnt(0)\n"
    "ds_read_b128 v[136:139], v248 offset:0\n"
    "ds_read_b128 v[140:143], v248 offset:1024\n"
    "ds_read_b128 v[144:147], v248 offset:2048\n"
    "ds_read_b128 v[148:151], v248 offset:3072\n"
    "ds_read_b128 v[152:155], v248 offset:4096\n"
    "ds_read_b128 v[156:159], v248 offset:5120\n"
    "ds_read_b128 v[160:163], v248 offset:6144\n"
    "ds_read_b128 v[164:167], v248 offset:7168\n"
    "s_mov_b32 s56, s30\n"
    "s_and_b32 s57, s31, 0xffff\n"
    "s_mov_b32 s58, 4194304\n"
    "s_mov_b32 s59, 0x20000\n"
    "s_waitcnt lgkmcnt(7)\n"
    "s_mov_b32 s73, 0\n"
    "buffer_store_dwordx4 v[136:139], v254, s[56:59], s73 offen sc1\n"
    "s_waitcnt lgkmcnt(6)\n"
    "s_mov_b32 s73, 16384\n"
    "buffer_store_dwordx4 v[140:143], v254, s[56:59], s73 offen sc1\n"
    "s_waitcnt lgkmcnt(5)\n"
    "s_mov_b32 s73, 32768\n"
    "buffer_store_dwordx4 v[144:147], v254, s[56:59], s73 offen sc1\n"
    "s_waitcnt lgkmcnt(4)\n"
    "s_mov_b32 s73, 49152\n"
    "buffer_store_dwordx4 v[148:151], v254, s[56:59], s73 offen sc1\n"
    "s_waitcnt lgkmcnt(3)\n"
    "s_mov_b32 s73, 65536\n"
    "buffer_store_dwordx4 v[152:155], v254, s[56:59], s73 offen sc1\n"
    "s_waitcnt lgkmcnt(2)\n"
    "s_mov_b32 s73, 81920\n"
    "buffer_store_dwordx4 v[156:159], v254, s[56:59], s73 offen sc1\n"
    "s_waitcnt lgkmcnt(1)\n"
    "s_mov_b32 s73, 98304\n"
    "buffer_store_dwordx4 v[160:163], v254, s[56:59], s73 offen sc1\n"
    "s_waitcnt lgkmcnt(0)\n"
    "s_mov_b32 s73, 114688\n"
    "buffer_store_dwordx4 v[164:167], v254, s[56:59], s73 offen sc1\n"
    "LYE_%=_15:\n"
    "s_branch LP2E_%=\n"
    "LP2D_%=:\n"
    "s_lshl_b32 s71, s60, 15\n"
    "s_lshl_b32 s72, s65, 8\n"
    "s_add_u32 s71, s71, s72\n"
    "v_lshlrev_b32 v246, 11, v242\n"
    "v_lshl_or_b32 v247, v242, 2, 0\n"
    "v_xor_b32 v247, v241, v247\n"
    "v_lshl_add_u32 v247, v247, 4, v246\n"
    "s_add_u32 s72, s71, 0\n"
    "v_add_u32 v160, s72, v247\n"
    "v_lshl_or_b32 v247, v242, 2, 1\n"
    "v_xor_b32 v247, v241, v247\n"
    "v_lshl_add_u32 v247, v247, 4, v246\n"
    "s_add_u32 s72, s71, 7168\n"
    "v_add_u32 v161, s72, v247\n"
    "v_lshl_or_b32 v247, v242, 2, 2\n"
    "v_xor_b32 v247, v241, v247\n"
    "v_lshl_add_u32 v247, v247, 4, v246\n"
    "s_add_u32 s72, s71, 14336\n"
    "v_add_u32 v162, s72, v247\n"
    "v_lshl_or_b32 v247, v242, 2, 3\n"
    "v_xor_b32 v247, v241, v247\n"
    "v_lshl_add_u32 v247, v247, 4, v246\n"
    "s_add_u32 s72, s71, 21504\n"
    "v_add_u32 v163, s72, v247\n"
    "s_lshl_b32 s71, s64, 15\n"
    "s_lshl_b32 s72, s60, 13\n"
    "s_add_u32 s71, s71, s72\n"
    "v_lshlrev_b32 v243, 4, v240\n"
    "v_add_u32 v168, s71, v243\n"
    "v_add_u32 v169, 4096, v168\n"
    "s_and_b32 s71, s63, 1\n"
    "s_mul_i32 s71, s71, 28\n"
    "s_lshl_b32 s66, s71, 17\n"
    "s_lshl_b32 s67, s71, 18\n"
    "s_mov_b32 s69, 28\n"
    "s_mov_b32 s44, s26\n"
    "s_and_b32 s45, s27, 0xffff\n"
    "s_mov_b32 s46, 7340032\n"
    "s_mov_b32 s47, 0x20000\n"
    "s_mov_b32 s48, s24\n"
    "s_and_b32 s49, s25, 0xffff\n"
    "s_mov_b32 s50, 14680064\n"
    "s_mov_b32 s51, 0x20000\n"
    "s_lshl_b32 s74, s60, 12\n"
    "s_mov_b32 s78, 5\n"
    "s_mov_b32 s56, s48\n"
    "s_mov_b32 s57, s49\n"
    "s_mov_b32 s58, 0\n"
    "s_mov_b32 s59, 0x20000\n"
    "v_lshrrev_b32 v243, 2, v241\n"
    "v_and_b32 v244, 3, v240\n"
    "v_lshl_add_u32 v245, v242, 3, v243\n"
    "v_lshlrev_b32 v246, 1, v242\n"
    "v_and_b32 v246, 3, v246\n"
    "v_lshl_or_b32 v246, v243, 2, v246\n"
    "v_and_b32 v247, 1, v244\n"
    "v_lshlrev_b32 v247, 3, v247\n"
    "v_lshl_add_u32 v247, v245, 8, v247\n"
    "v_lshrrev_b32 v248, 1, v244\n"
    "v_add_u32 v248, 0, v248\n"
    "v_xor_b32 v248, v248, v246\n"
    "v_lshl_add_u32 v128, v248, 4, v247\n"
    "v_add_u32 v144, 49152, v128\n"
    "v_lshrrev_b32 v248, 1, v244\n"
    "v_add_u32 v248, 2, v248\n"
    "v_xor_b32 v248, v248, v246\n"
    "v_lshl_add_u32 v130, v248, 4, v247\n"
    "v_add_u32 v146, 49152, v130\n"
    "v_lshrrev_b32 v248, 1, v244\n"
    "v_add_u32 v248, 4, v248\n"
    "v_xor_b32 v248, v248, v246\n"
    "v_lshl_add_u32 v132, v248, 4, v247\n"
    "v_add_u32 v148, 49152, v132\n"
    "v_lshrrev_b32 v248, 1, v244\n"
    "v_add_u32 v248, 6, v248\n"
    "v_xor_b32 v248, v248, v246\n"
    "v_lshl_add_u32 v134, v248, 4, v247\n"
    "v_add_u32 v150, 49152, v134\n"
    "v_lshrrev_b32 v248, 1, v244\n"
    "v_add_u32 v248, 8, v248\n"
    "v_xor_b32 v248, v248, v246\n"
    "v_lshl_add_u32 v136, v248, 4, v247\n"
    "v_add_u32 v152, 49152, v136\n"
    "v_lshrrev_b32 v248, 1, v244\n"
    "v_add_u32 v248, 10, v248\n"
    "v_xor_b32 v248, v248, v246\n"
    "v_lshl_add_u32 v138, v248, 4, v247\n"
    "v_add_u32 v154, 49152, v138\n"
    "v_lshrrev_b32 v248, 1, v244\n"
    "v_add_u32 v248, 12, v248\n"
    "v_xor_b32 v248, v248, v246\n"
    "v_lshl_add_u32 v140, v248, 4, v247\n"
    "v_add_u32 v156, 49152, v140\n"
    "v_lshrrev_b32 v248, 1, v244\n"
    "v_add_u32 v248, 14, v248\n"
    "v_xor_b32 v248, v248, v246\n"
    "v_lshl_add_u32 v142, v248, 4, v247\n"
    "v_add_u32 v158, 49152, v142\n"
    "v_lshl_add_u32 v245, v242, 3, v243\n"
    "v_add_u32 v245, 4, v245\n"
    "v_lshlrev_b32 v246, 1, v242\n"
    "v_add_u32 v246, 1, v246\n"
    "v_and_b32 v246, 3, v246\n"
    "v_lshl_or_b32 v246, v243, 2, v246\n"
    "v_and_b32 v247, 1, v244\n"
    "v_lshlrev_b32 v247, 3, v247\n"
    "v_lshl_add_u32 v247, v245, 8, v247\n"
    "v_lshrrev_b32 v248, 1, v244\n"
    "v_add_u32 v248, 0, v248\n"
    "v_xor_b32 v248, v248, v246\n"
    "v_lshl_add_u32 v129, v248, 4, v247\n"
    "v_add_u32 v145, 49152, v129\n"
    "v_lshrrev_b32 v248, 1, v244\n"
    "v_add_u32 v248, 2, v248\n"
    "v_xor_b32 v248, v248, v246\n"
    "v_lshl_add_u32 v131, v248, 4, v247\n"
    "v_add_u32 v147, 49152, v131\n"
    "v_lshrrev_b32 v248, 1, v244\n"
    "v_add_u32 v248, 4, v248\n"
    "v_xor_b32 v248, v248, v246\n"
    "v_lshl_add_u32 v133, v248, 4, v247\n"
    "v_add_u32 v149, 49152, v133\n"
    "v_lshrrev_b32 v248, 1, v244\n"
    "v_add_u32 v248, 6, v248\n"
    "v_xor_b32 v248, v248, v246\n"
    "v_lshl_add_u32 v135, v248, 4, v247\n"
    "v_add_u32 v151, 49152, v135\n"
    "v_lshrrev_b32 v248, 1, v244\n"
    "v_add_u32 v248, 8, v248\n"
    "v_xor_b32 v248, v248, v246\n"
    "v_lshl_add_u32 v137, v248, 4, v247\n"
    "v_add_u32 v153, 49152, v137\n"
    "v_lshrrev_b32 v248, 1, v244\n"
    "v_add_u32 v248, 10, v248\n"
    "v_xor_b32 v248, v248, v246\n"
    "v_lshl_add_u32 v139, v248, 4, v247\n"
    "v_add_u32 v155, 49152, v139\n"
    "v_lshrrev_b32 v248, 1, v244\n"
    "v_add_u32 v248, 12, v248\n"
    "v_xor_b32 v248, v248, v246\n"
    "v_lshl_add_u32 v141, v248, 4, v247\n"
    "v_add_u32 v157, 49152, v141\n"
    "v_lshrrev_b32 v248, 1, v244\n"
    "v_add_u32 v248, 14, v248\n"
    "v_xor_b32 v248, v248, v246\n"
    "v_lshl_add_u32 v143, v248, 4, v247\n"
    "v_add_u32 v159, 49152, v143\n"
    "v_mov_b32 v0, 0\n"
    "v_mov_b32 v1, 0\n"
    "v_mov_b32 v2, 0\n"
    "v_mov_b32 v3, 0\n"
    "v_mov_b32 v4, 0\n"
    "v_mov_b32 v5, 0\n"
    "v_mov_b32 v6, 0\n"
    "v_mov_b32 v7, 0\n"
    "v_mov_b32 v8, 0\n"
    "v_mov_b32 v9, 0\n"
    "v_mov_b32 v10, 0\n"
    "v_mov_b32 v11, 0\n"
    "v_mov_b32 v12, 0\n"
    "v_mov_b32 v13, 0\n"
    "v_mov_b32 v14, 0\n"
    "v_mov_b32 v15, 0\n"
    "v_mov_b32 v16, 0\n"
    "v_mov_b32 v17, 0\n"
    "v_mov_b32 v18, 0\n"
    "v_mov_b32 v19, 0\n"
    "v_mov_b32 v20, 0\n"
    "v_mov_b32 v21, 0\n"
    "v_mov_b32 v22, 0\n"
    "v_mov_b32 v23, 0\n"
    "v_mov_b32 v24, 0\n"
    "v_mov_b32 v25, 0\n"
    "v_mov_b32 v26, 0\n"
    "v_mov_b32 v27, 0\n"
    "v_mov_b32 v28, 0\n"
    "v_mov_b32 v29, 0\n"
    "v_mov_b32 v30, 0\n"
    "v_mov_b32 v31, 0\n"
    "v_mov_b32 v32, 0\n"
    "v_mov_b32 v33, 0\n"
    "v_mov_b32 v34, 0\n"
    "v_mov_b32 v35, 0\n"
    "v_mov_b32 v36, 0\n"
    "v_mov_b32 v37, 0\n"
    "v_mov_b32 v38, 0\n"
    "v_mov_b32 v39, 0\n"
    "v_mov_b32 v40, 0\n"
    "v_mov_b32 v41, 0\n"
    "v_mov_b32 v42, 0\n"
    "v_mov_b32 v43, 0\n"
    "v_mov_b32 v44, 0\n"
    "v_mov_b32 v45, 0\n"
    "v_mov_b32 v46, 0\n"
    "v_mov_b32 v47, 0\n"
    "v_mov_b32 v48, 0\n"
    "v_mov_b32 v49, 0\n"
    "v_mov_b32 v50, 0\n"
    "v_mov_b32 v51, 0\n"
    "v_mov_b32 v52, 0\n"
    "v_mov_b32 v53, 0\n"
    "v_mov_b32 v54, 0\n"
    "v_mov_b32 v55, 0\n"
    "v_mov_b32 v56, 0\n"
    "v_mov_b32 v57, 0\n"
    "v_mov_b32 v58, 0\n"
    "v_mov_b32 v59, 0\n"
    "v_mov_b32 v60, 0\n"
    "v_mov_b32 v61, 0\n"
    "v_mov_b32 v62, 0\n"
    "v_mov_b32 v63, 0\n"
    "v_mov_b32 v64, 0\n"
    "v_mov_b32 v65, 0\n"
    "v_mov_b32 v66, 0\n"
    "v_mov_b32 v67, 0\n"
    "v_mov_b32 v68, 0\n"
    "v_mov_b32 v69, 0\n"
    "v_mov_b32 v70, 0\n"
    "v_mov_b32 v71, 0\n"
    "v_mov_b32 v72, 0\n"
    "v_mov_b32 v73, 0\n"
    "v_mov_b32 v74, 0\n"
    "v_mov_b32 v75, 0\n"
    "v_mov_b32 v76, 0\n"
    "v_mov_b32 v77, 0\n"
    "v_mov_b32 v78, 0\n"
    "v_mov_b32 v79, 0\n"
    "v_mov_b32 v80, 0\n"
    "v_mov_b32 v81, 0\n"
    "v_mov_b32 v82, 0\n"
    "v_mov_b32 v83, 0\n"
    "v_mov_b32 v84, 0\n"
    "v_mov_b32 v85, 0\n"
    "v_mov_b32 v86, 0\n"
    "v_mov_b32 v87, 0\n"
    "v_mov_b32 v88, 0\n"
    "v_mov_b32 v89, 0\n"
    "v_mov_b32 v90, 0\n"
    "v_mov_b32 v91, 0\n"
    "v_mov_b32 v92, 0\n"
    "v_mov_b32 v93, 0\n"
    "v_mov_b32 v94, 0\n"
    "v_mov_b32 v95, 0\n"
    "v_mov_b32 v96, 0\n"
    "v_mov_b32 v97, 0\n"
    "v_mov_b32 v98, 0\n"
    "v_mov_b32 v99, 0\n"
    "v_mov_b32 v100, 0\n"
    "v_mov_b32 v101, 0\n"
    "v_mov_b32 v102, 0\n"
    "v_mov_b32 v103, 0\n"
    "v_mov_b32 v104, 0\n"
    "v_mov_b32 v105, 0\n"
    "v_mov_b32 v106, 0\n"
    "v_mov_b32 v107, 0\n"
    "v_mov_b32 v108, 0\n"
    "v_mov_b32 v109, 0\n"
    "v_mov_b32 v110, 0\n"
    "v_mov_b32 v111, 0\n"
    "v_mov_b32 v112, 0\n"
    "v_mov_b32 v113, 0\n"
    "v_mov_b32 v114, 0\n"
    "v_mov_b32 v115, 0\n"
    "v_mov_b32 v116, 0\n"
    "v_mov_b32 v117, 0\n"
    "v_mov_b32 v118, 0\n"
    "v_mov_b32 v119, 0\n"
    "v_mov_b32 v120, 0\n"
    "v_mov_b32 v121, 0\n"
    "v_mov_b32 v122, 0\n"
    "v_mov_b32 v123, 0\n"
    "v_mov_b32 v124, 0\n"
    "v_mov_b32 v125, 0\n"
    "v_mov_b32 v126, 0\n"
    "v_mov_b32 v127, 0\n"
    "s_waitcnt vmcnt(0)\n"
    "s_barrier\n"
    "s_cmp_lg_u32 s60, 0\n"
    "s_cbranch_scc1 LPP_%=_24\n"
    "v_mov_b32 v249, s99\n"
    "v_mov_b32 v252, s4\n"
    "v_mov_b32 v253, s5\n"
    "s_mov_b64 s[10:11], exec\n"
    "s_mov_b64 exec, 1\n"
    "global_store_dwordx2 v249, v[252:253], s[6:7] sc1\n"
    "s_mov_b64 exec, s[10:11]\n"
    "s_sub_u32 s99, s99, 2048\n"
    "v_mov_b32 v249, s99\n"
    "v_mov_b32 v252, s4\n"
    "v_mov_b32 v253, s5\n"
    "s_mov_b64 s[10:11], exec\n"
    "s_mov_b64 exec, 1\n"
    "global_store_dwordx2 v249, v[252:253], s[6:7] sc1\n"
    "s_mov_b64 exec, s[10:11]\n"
    "LPP_%=_24:\n"
    "s_and_b32 s70, s63, 1\n"
    "s_mul_i32 s70, s70, 14\n"
    "s_lshl_b32 s71, s64, 5\n"
    "s_add_u32 s70, s70, s71\n"
    "v_min_u32 v249, 13, v240\n"
    "v_add_u32 v249, s70, v249\n"
    "v_lshlrev_b32 v249, 3, v249\n"
    "v_add_u32 v249, 4096, v249\n"
    "v_mov_b32 v203, v249\n"
    "s_cmp_lg_u32 s60, 0\n"
    "s_cbranch_scc1 LPA_%=_25\n"
    "global_load_dwordx2 v[252:253], v249, s[6:7] sc1\n"
    "global_load_dwordx2 v[204:205], v203, s[6:7] sc1\n"
    "LPA_%=_25:\n"
    "s_add_u32 m0, s74, 65536\n"
    "s_nop 0\n"
    "buffer_load_dwordx4 v160, s[44:47], s66 offen offset:0  lds\n"
    "buffer_load_dwordx4 v161, s[44:47], s66 offen offset:1024  lds\n"
    "buffer_load_dwordx4 v162, s[44:47], s66 offen offset:2048  lds\n"
    "buffer_load_dwordx4 v163, s[44:47], s66 offen offset:3072  lds\n"
    "s_add_u32 s66, s66, 131072\n"
    "s_add_u32 m0, s74, 81920\n"
    "s_nop 0\n"
    "buffer_load_dwordx4 v160, s[44:47], s66 offen offset:0  lds\n"
    "buffer_load_dwordx4 v161, s[44:47], s66 offen offset:1024  lds\n"
    "buffer_load_dwordx4 v162, s[44:47], s66 offen offset:2048  lds\n"
    "buffer_load_dwordx4 v163, s[44:47], s66 offen offset:3072  lds\n"
    "s_add_u32 s66, s66, 131072\n"
    "s_mov_b32 s58, 0\n"
    "s_cmp_lg_u32 s60, 0\n"
    "s_cbranch_scc1 LPB_%=_25\n"
    "s_waitcnt vmcnt(8)\n"
    "v_cmp_ne_u64 vcc, s[4:5], v[252:253]\n"
    "v_cmp_ne_u64 s[10:11], s[4:5], v[204:205]\n"
    "s_nop 4\n"
    "s_or_b64 vcc, vcc, s[10:11]\n"
    "s_cmp_eq_u64 vcc, 0\n"
    "s_cselect_b32 s70, 1, 0\n"
    "v_mov_b32 v200, s70\n"
    "v_mov_b32 v201, 131072\n"
    "ds_write_b32 v201, v200\n"
    "s_waitcnt lgkmcnt(0)\n"
    "LPB_%=_25:\n"
    "s_barrier\n"
    "v_mov_b32 v201, 131072\n"
    "ds_read_b32 v200, v201\n"
    "s_waitcnt lgkmcnt(0)\n"
    "v_readfirstlane_b32 s70, v200\n"
    "s_nop 3\n"
    "s_cmp_eq_u32 s70, 1\n"
    "s_cbranch_scc1 LPR_%=_25\n"
    "s_cmp_lg_u32 s60, 0\n"
    "s_cbranch_scc1 LPC_%=_25\n"
    "s_mov_b32 s3, 0\n"
    "global_load_dwordx2 v[252:253], v249, s[6:7] sc1\n"
    "global_load_dwordx2 v[204:205], v203, s[6:7] sc1\n"
    "LW2_%=_26:\n"
    "global_load_dwordx2 v[206:207], v249, s[6:7] sc1\n"
    "global_load_dwordx2 v[196:197], v203, s[6:7] sc1\n"
    "s_waitcnt vmcnt(2)\n"
    "v_cmp_ne_u64 vcc, s[4:5], v[252:253]\n"
    "v_cmp_ne_u64 s[10:11], s[4:5], v[204:205]\n"
    "s_nop 4\n"
    "s_or_b64 vcc, vcc, s[10:11]\n"
    "s_cbranch_vccz LW2D_%=_26\n"
    "global_load_dwordx2 v[252:253], v249, s[6:7] sc1\n"
    "global_load_dwordx2 v[204:205], v203, s[6:7] sc1\n"
    "s_waitcnt vmcnt(2)\n"
    "v_cmp_ne_u64 vcc, s[4:5], v[206:207]\n"
    "v_cmp_ne_u64 s[10:11], s[4:5], v[196:197]\n"
    "s_nop 4\n"
    "s_or_b64 vcc, vcc, s[10:11]\n"
    "s_cbranch_vccz LW2D_%=_26\n"
    "s_add_u32 s3, s3, 1\n"
    "s_cmp_lt_u32 s3, 0x10000\n"
    "s_cbranch_scc1 LW2_%=_26\n"
    "LW2D_%=_26:\n"
    "LPC_%=_25:\n"
    "s_barrier\n"
    "LPR_%=_25:\n"
    "s_add_u32 m0, s74, 0\n"
    "s_nop 0\n"
    "buffer_load_dwordx4 v160, s[44:47], s66 offen offset:0  lds\n"
    "buffer_load_dwordx4 v161, s[44:47], s66 offen offset:1024  lds\n"
    "buffer_load_dwordx4 v162, s[44:47], s66 offen offset:2048  lds\n"
    "buffer_load_dwordx4 v163, s[44:47], s66 offen offset:3072  lds\n"
    "s_add_u32 s66, s66, 131072\n"
    "buffer_load_dwordx4 a[192:195], v168, s[48:51], s67 offen offset:0 sc1\n"
    "buffer_load_dwordx4 a[196:199], v168, s[48:51], s67 offen offset:1024 sc1\n"
    "buffer_load_dwordx4 a[200:203], v168, s[48:51], s67 offen offset:2048 sc1\n"
    "buffer_load_dwordx4 a[204:207], v168, s[48:51], s67 offen offset:3072 sc1\n"
    "buffer_load_dwordx4 a[208:211], v169, s[48:51], s67 offen offset:0 sc1\n"
    "buffer_load_dwordx4 a[212:215], v169, s[48:51], s67 offen offset:1024 sc1\n"
    "buffer_load_dwordx4 a[216:219], v169, s[48:51], s67 offen offset:2048 sc1\n"
    "buffer_load_dwordx4 a[220:223], v169, s[48:51], s67 offen offset:3072 sc1\n"
    "s_add_u32 s67, s67, 262144\n"
    "s_waitcnt vmcnt(12)\n"
    "s_barrier\n"
    "ds_read_b64_tr_b16 a[0:1], v144 offset:16384\n"
    "ds_read_b64_tr_b16 a[2:3], v145 offset:16384\n"
    "ds_read_b64_tr_b16 a[4:5], v144 offset:24576\n"
    "ds_read_b64_tr_b16 a[6:7], v145 offset:24576\n"
    "ds_read_b64_tr_b16 a[8:9], v146 offset:16384\n"
    "ds_read_b64_tr_b16 a[10:11], v147 offset:16384\n"
    "ds_read_b64_tr_b16 a[12:13], v146 offset:24576\n"
    "ds_read_b64_tr_b16 a[14:15], v147 offset:24576\n"
    "ds_read_b64_tr_b16 a[16:17], v148 offset:16384\n"
    "ds_read_b64_tr_b16 a[18:19], v149 offset:16384\n"
    "ds_read_b64_tr_b16 a[20:21], v148 offset:24576\n"
    "ds_read_b64_tr_b16 a[22:23], v149 offset:24576\n"
    "ds_read_b64_tr_b16 a[24:25], v150 offset:16384\n"
    "ds_read_b64_tr_b16 a[26:27], v151 offset:16384\n"
    "ds_read_b64_tr_b16 a[28:29], v150 offset:24576\n"
    "ds_read_b64_tr_b16 a[30:31], v151 offset:24576\n"
    "ds_read_b64_tr_b16 a[32:33], v152 offset:16384\n"
    "ds_read_b64_tr_b16 a[34:35], v153 offset:16384\n"
    "ds_read_b64_tr_b16 a[36:37], v152 offset:24576\n"
    "ds_read_b64_tr_b16 a[38:39], v153 offset:24576\n"
    "ds_read_b64_tr_b16 a[40:41], v154 offset:16384\n"
    "ds_read_b64_tr_b16 a[42:43], v155 offset:16384\n"
    "ds_read_b64_tr_b16 a[44:45], v154 offset:24576\n"
    "ds_read_b64_tr_b16 a[46:47], v155 offset:24576\n"
    "ds_read_b64_tr_b16 a[48:49], v156 offset:16384\n"
    "ds_read_b64_tr_b16 a[50:51], v157 offset:16384\n"
    "ds_read_b64_tr_b16 a[52:53], v156 offset:24576\n"
    "ds_read_b64_tr_b16 a[54:55], v157 offset:24576\n"
    "s_add_u32 m0, s74, 16384\n"
    "s_nop 0\n"
    "buffer_load_dwordx4 v160, s[44:47], s66 offen offset:0  lds\n"
    "buffer_load_dwordx4 v161, s[44:47], s66 offen offset:1024  lds\n"
    "buffer_load_dwordx4 v162, s[44:47], s66 offen offset:2048  lds\n"
    "buffer_load_dwordx4 v163, s[44:47], s66 offen offset:3072  lds\n"
    "s_add_u32 s66, s66, 131072\n"
    "buffer_load_dwordx4 a[224:227], v168, s[48:51], s67 offen offset:0 sc1\n"
    "buffer_load_dwordx4 a[228:231], v168, s[48:51], s67 offen offset:1024 sc1\n"
    "buffer_load_dwordx4 a[232:235], v168, s[48:51], s67 offen offset:2048 sc1\n"
    "buffer_load_dwordx4 a[236:239], v168, s[48:51], s67 offen offset:3072 sc1\n"
    "buffer_load_dwordx4 a[240:243], v169, s[48:51], s67 offen offset:0 sc1\n"
    "buffer_load_dwordx4 a[244:247], v169, s[48:51], s67 offen offset:1024 sc1\n"
    "buffer_load_dwordx4 a[248:251], v169, s[48:51], s67 offen offset:2048 sc1\n"
    "buffer_load_dwordx4 a[252:255], v169, s[48:51], s67 offen offset:3072 sc1\n"
    "s_add_u32 s67, s67, 262144\n"
    "s_add_u32 m0, s74, 32768\n"
    "s_nop 0\n"
    "buffer_load_dwordx4 v160, s[44:47], s66 offen offset:0  lds\n"
    "buffer_load_dwordx4 v161, s[44:47], s66 offen offset:1024  lds\n"
    "buffer_load_dwordx4 v162, s[44:47], s66 offen offset:2048  lds\n"
    "buffer_load_dwordx4 v163, s[44:47], s66 offen offset:3072  lds\n"
    "s_add_u32 s66, s66, 131072\n"
    "buffer_load_dwordx4 a[64:67], v168, s[48:51], s67 offen offset:0 sc1\n"
    "buffer_load_dwordx4 a[68:71], v168, s[48:51], s67 offen offset:1024 sc1\n"
    "buffer_load_dwordx4 a[72:75], v168, s[48:51], s67 offen offset:2048 sc1\n"
    "buffer_load_dwordx4 a[76:79], v168, s[48:51], s67 offen offset:3072 sc1\n"
    "buffer_load_dwordx4 a[80:83], v169, s[48:51], s67 offen offset:0 sc1\n"
    "buffer_load_dwordx4 a[84:87], v169, s[48:51], s67 offen offset:1024 sc1\n"
    "buffer_load_dwordx4 a[88:91], v169, s[48:51], s67 offen offset:2048 sc1\n"
    "buffer_load_dwordx4 a[92:95], v169, s[48:51], s67 offen offset:3072 sc1\n"
    "s_add_u32 s67, s67, 262144\n"
    "s_lshr_b32 s70, s8, 3\n"
    "s_lshr_b32 s71, s8, 2\n"
    "s_and_b32 s71, s71, 1\n"
    "s_lshl_b32 s71, s71, 5\n"
    "s_add_u32 s70, s70, s71\n"
    "s_lshl_b32 s70, s70, 2\n"
    "s_add_u32 s70, s70, s60\n"
    "s_lshl_b32 s70, s70, 6\n"
    "v_add_u32 v200, s70, v240\n"
    "s_mov_b32 s52, s44\n"
    "s_mov_b32 s53, s45\n"
    "s_mov_b32 s55, 0x20000\n"
    "s_mov_b32 s54, 7340032\n"
    "s_and_b32 s71, s63, 1\n"
    "s_mul_i32 s71, s71, 1792\n"
    "v_lshrrev_b32 v201, 4, v200\n"
    "v_min_u32 v202, 511, v201\n"
    "v_add_u32 v202, s71, v202\n"
    "v_add_u32 v202, 512, v202\n"
    "v_lshlrev_b32 v202, 11, v202\n"
    "v_and_b32 v200, 15, v200\n"
    "v_lshl_add_u32 v202, v200, 7, v202\n"
    "v_min_u32 v201, 767, v201\n"
    "v_add_u32 v201, s71, v201\n"
    "v_add_u32 v201, 1024, v201\n"
    "v_lshlrev_b32 v201, 11, v201\n"
    "v_lshl_add_u32 v201, v200, 7, v201\n"
    "buffer_load_dword v250, v201, s[52:55], 0 offen\n"
    "s_waitcnt lgkmcnt(0)\n"
    "s_mov_b32 s68, 5\n"
    "s_branch LD_pos4_%=\n"
    "LD_loop_%=:\n"
    "LD_pos0_%=:\n"
    "s_waitcnt vmcnt(24)\n"
    "s_barrier\n"
    "v_mfma_f32_16x16x32_bf16 v[0:3], a[0:3], a[64:67], v[0:3]\n"
    "ds_read_b64_tr_b16 a[56:57], v142 offset:0\n"
    "v_mfma_f32_16x16x32_bf16 v[0:3], a[4:7], a[68:71], v[0:3]\n"
    "ds_read_b64_tr_b16 a[58:59], v143 offset:0\n"
    "v_mfma_f32_16x16x32_bf16 v[4:7], a[0:3], a[72:75], v[4:7]\n"
    "ds_read_b64_tr_b16 a[60:61], v142 offset:8192\n"
    "v_mfma_f32_16x16x32_bf16 v[4:7], a[4:7], a[76:79], v[4:7]\n"
    "ds_read_b64_tr_b16 a[62:63], v143 offset:8192\n"
    "v_mfma_f32_16x16x32_bf16 v[8:11], a[0:3], a[80:83], v[8:11]\n"
    "s_add_u32 m0, s74, 81920\n"
    "s_nop 0\n"
    "buffer_load_dwordx4 v160, s[44:47], s66 offen offset:0  lds\n"
    "v_mfma_f32_16x16x32_bf16 v[8:11], a[4:7], a[84:87], v[8:11]\n"
    "v_mfma_f32_16x16x32_bf16 v[12:15], a[0:3], a[88:91], v[12:15]\n"
    "v_mfma_f32_16x16x32_bf16 v[12:15], a[4:7], a[92:95], v[12:15]\n"
    "s_waitcnt lgkmcnt(15)\n"
    "v_mfma_f32_16x16x32_bf16 v[16:19], a[8:11], a[64:67], v[16:19]\n"
    "ds_read_b64_tr_b16 a[0:1], v128 offset:16384\n"
    "v_mfma_f32_16x16x32_bf16 v[16:19], a[12:15], a[68:71], v[16:19]\n"
    "buffer_load_dwordx4 v161, s[44:47], s66 offen offset:1024  lds\n"
    "v_mfma_f32_16x16x32_bf16 v[20:23], a[8:11], a[72:75], v[20:23]\n"
    "ds_read_b64_tr_b16 a[2:3], v129 offset:16384\n"
    "v_mfma_f32_16x16x32_bf16 v[20:23], a[12:15], a[76:79], v[20:23]\n"
    "v_mfma_f32_16x16x32_bf16 v[24:27], a[8:11], a[80:83], v[24:27]\n"
    "ds_read_b64_tr_b16 a[4:5], v128 offset:24576\n"
    "v_mfma_f32_16x16x32_bf16 v[24:27], a[12:15], a[84:87], v[24:27]\n"
    "v_mfma_f32_16x16x32_bf16 v[28:31], a[8:11], a[88:91], v[28:31]\n"
    "ds_read_b64_tr_b16 a[6:7], v129 offset:24576\n"
    "buffer_load_dwordx4 v162, s[44:47], s66 offen offset:2048  lds\n"
    "v_mfma_f32_16x16x32_bf16 v[28:31], a[12:15], a[92:95], v[28:31]\n"
    "v_mfma_f32_16x16x32_bf16 v[32:35], a[16:19], a[64:67], v[32:35]\n"
    "ds_read_b64_tr_b16 a[8:9], v130 offset:16384\n"
    "v_mfma_f32_16x16x32_bf16 v[32:35], a[20:23], a[68:71], v[32:35]\n"
    "v_mfma_f32_16x16x32_bf16 v[36:39], a[16:19], a[72:75], v[36:39]\n"
    "ds_read_b64_tr_b16 a[10:11], v131 offset:16384\n"
    "v_mfma_f32_16x16x32_bf16 v[36:39], a[20:23], a[76:79], v[36:39]\n"
    "buffer_load_dwordx4 v163, s[44:47], s66 offen offset:3072  lds\n"
    "v_mfma_f32_16x16x32_bf16 v[40:43], a[16:19], a[80:83], v[40:43]\n"
    "ds_read_b64_tr_b16 a[12:13], v130 offset:24576\n"
    "v_mfma_f32_16x16x32_bf16 v[40:43], a[20:23], a[84:87], v[40:43]\n"
    "v_mfma_f32_16x16x32_bf16 v[44:47], a[16:19], a[88:91], v[44:47]\n"
    "ds_read_b64_tr_b16 a[14:15], v131 offset:24576\n"
    "v_mfma_f32_16x16x32_bf16 v[44:47], a[20:23], a[92:95], v[44:47]\n"
    "v_mfma_f32_16x16x32_bf16 v[48:51], a[24:27], a[64:67], v[48:51]\n"
    "ds_read_b64_tr_b16 a[16:17], v132 offset:16384\n"
    "v_mfma_f32_16x16x32_bf16 v[48:51], a[28:31], a[68:71], v[48:51]\n"
    "s_add_u32 s66, s66, 131072\n"
    "v_mfma_f32_16x16x32_bf16 v[52:55], a[24:27], a[72:75], v[52:55]\n"
    "ds_read_b64_tr_b16 a[18:19], v133 offset:16384\n"
    "v_mfma_f32_16x16x32_bf16 v[52:55], a[28:31], a[76:79], v[52:55]\n"
    "v_mfma_f32_16x16x32_bf16 v[56:59], a[24:27], a[80:83], v[56:59]\n"
    "ds_read_b64_tr_b16 a[20:21], v132 offset:24576\n"
    "buffer_load_dwordx4 a[160:163], v168, s[48:51], s67 offen offset:0 sc1\n"
    "v_mfma_f32_16x16x32_bf16 v[56:59], a[28:31], a[84:87], v[56:59]\n"
    "v_mfma_f32_16x16x32_bf16 v[60:63], a[24:27], a[88:91], v[60:63]\n"
    "ds_read_b64_tr_b16 a[22:23], v133 offset:24576\n"
    "v_mfma_f32_16x16x32_bf16 v[60:63], a[28:31], a[92:95], v[60:63]\n"
    "s_waitcnt lgkmcnt(15)\n"
    "v_mfma_f32_16x16x32_bf16 v[64:67], a[32:35], a[64:67], v[64:67]\n"
    "ds_read_b64_tr_b16 a[24:25], v134 offset:16384\n"
    "buffer_load_dwordx4 a[164:167], v168, s[48:51], s67 offen offset:1024 sc1\n"
    "v_mfma_f32_16x16x32_bf16 v[64:67], a[36:39], a[68:71], v[64:67]\n"
    "v_mfma_f32_16x16x32_bf16 v[68:71], a[32:35], a[72:75], v[68:71]\n"
    "ds_read_b64_tr_b16 a[26:27], v135 offset:16384\n"
    "v_mfma_f32_16x16x32_bf16 v[68:71], a[36:39], a[76:79], v[68:71]\n"
    "v_mfma_f32_16x16x32_bf16 v[72:75], a[32:35], a[80:83], v[72:75]\n"
    "ds_read_b64_tr_b16 a[28:29], v134 offset:24576\n"
    "buffer_load_dwordx4 a[168:171], v168, s[48:51], s67 offen offset:2048 sc1\n"
    "v_mfma_f32_16x16x32_bf16 v[72:75], a[36:39], a[84:87], v[72:75]\n"
    "v_mfma_f32_16x16x32_bf16 v[76:79], a[32:35], a[88:91], v[76:79]\n"
    "ds_read_b64_tr_b16 a[30:31], v135 offset:24576\n"
    "v_mfma_f32_16x16x32_bf16 v[76:79], a[36:39], a[92:95], v[76:79]\n"
    "v_mfma_f32_16x16x32_bf16 v[80:83], a[40:43], a[64:67], v[80:83]\n"
    "ds_read_b64_tr_b16 a[32:33], v136 offset:16384\n"
    "buffer_load_dwordx4 a[172:175], v168, s[48:51], s67 offen offset:3072 sc1\n"
    "v_mfma_f32_16x16x32_bf16 v[80:83], a[44:47], a[68:71], v[80:83]\n"
    "v_mfma_f32_16x16x32_bf16 v[84:87], a[40:43], a[72:75], v[84:87]\n"
    "ds_read_b64_tr_b16 a[34:35], v137 offset:16384\n"
    "v_mfma_f32_16x16x32_bf16 v[84:87], a[44:47], a[76:79], v[84:87]\n"
    "v_mfma_f32_16x16x32_bf16 v[88:91], a[40:43], a[80:83], v[88:91]\n"
    "ds_read_b64_tr_b16 a[36:37], v136 offset:24576\n"
    "buffer_load_dwordx4 a[176:179], v169, s[48:51], s67 offen offset:0 sc1\n"
    "v_mfma_f32_16x16x32_bf16 v[88:91], a[44:47], a[84:87], v[88:91]\n"
    "v_mfma_f32_16x16x32_bf16 v[92:95], a[40:43], a[88:91], v[92:95]\n"
    "ds_read_b64_tr_b16 a[38:39], v137 offset:24576\n"
    "v_mfma_f32_16x16x32_bf16 v[92:95], a[44:47], a[92:95], v[92:95]\n"
    "v_mfma_f32_16x16x32_bf16 v[96:99], a[48:51], a[64:67], v[96:99]\n"
    "ds_read_b64_tr_b16 a[40:41], v138 offset:16384\n"
    "buffer_load_dwordx4 a[180:183], v169, s[48:51], s67 offen offset:1024 sc1\n"
    "v_mfma_f32_16x16x32_bf16 v[96:99], a[52:55], a[68:71], v[96:99]\n"
    "v_mfma_f32_16x16x32_bf16 v[100:103], a[48:51], a[72:75], v[100:103]\n"
    "ds_read_b64_tr_b16 a[42:43], v139 offset:16384\n"
    "v_mfma_f32_16x16x32_bf16 v[100:103], a[52:55], a[76:79], v[100:103]\n"
    "v_mfma_f32_16x16x32_bf16 v[104:107], a[48:51], a[80:83], v[104:107]\n"
    "ds_read_b64_tr_b16 a[44:45], v138 offset:24576\n"
    "buffer_load_dwordx4 a[184:187], v169, s[48:51], s67 offen offset:2048 sc1\n"
    "v_mfma_f32_16x16x32_bf16 v[104:107], a[52:55], a[84:87], v[104:107]\n"
    "v_mfma_f32_16x16x32_bf16 v[108:111], a[48:51], a[88:91], v[108:111]\n"
    "ds_read_b64_tr_b16 a[46:47], v139 offset:24576\n"
    "v_mfma_f32_16x16x32_bf16 v[108:111], a[52:55], a[92:95], v[108:111]\n"
    "s_waitcnt lgkmcnt(15)\n"
    "v_mfma_f32_16x16x32_bf16 v[112:115], a[56:59], a[64:67], v[112:115]\n"
    "ds_read_b64_tr_b16 a[48:49], v140 offset:16384\n"
    "buffer_load_dwordx4 a[188:191], v169, s[48:51], s67 offen offset:3072 sc1\n"
    "v_mfma_f32_16x16x32_bf16 v[112:115], a[60:63], a[68:71], v[112:115]\n"
    "ds_read_b64_tr_b16 a[50:51], v141 offset:16384\n"
    "v_mfma_f32_16x16x32_bf16 v[116:119], a[56:59], a[72:75], v[116:119]\n"
    "v_mfma_f32_16x16x32_bf16 v[116:119], a[60:63], a[76:79], v[116:119]\n"
    "ds_read_b64_tr_b16 a[52:53], v140 offset:24576\n"
    "v_mfma_f32_16x16x32_bf16 v[120:123], a[56:59], a[80:83], v[120:123]\n"
    "v_mfma_f32_16x16x32_bf16 v[120:123], a[60:63], a[84:87], v[120:123]\n"
    "ds_read_b64_tr_b16 a[54:55], v141 offset:24576\n"
    "s_add_u32 s67, s67, 262144\n"
    "v_mfma_f32_16x16x32_bf16 v[124:127], a[56:59], a[88:91], v[124:127]\n"
    "s_add_u32 s78, s78, 1\n"
    "s_cmp_ge_u32 s78, s69\n"
    "s_cselect_b32 s46, 0, s46\n"
    "s_sub_u32 s70, s78, 2\n"
    "s_cmp_ge_u32 s70, s69\n"
    "s_cselect_b32 s50, 0, s50\n"
    "v_mfma_f32_16x16x32_bf16 v[124:127], a[60:63], a[92:95], v[124:127]\n"
    "LD_pos1_%=:\n"
    "s_waitcnt vmcnt(24)\n"
    "s_barrier\n"
    "v_mfma_f32_16x16x32_bf16 v[0:3], a[0:3], a[96:99], v[0:3]\n"
    "ds_read_b64_tr_b16 a[56:57], v142 offset:16384\n"
    "v_mfma_f32_16x16x32_bf16 v[0:3], a[4:7], a[100:103], v[0:3]\n"
    "ds_read_b64_tr_b16 a[58:59], v143 offset:16384\n"
    "v_mfma_f32_16x16x32_bf16 v[4:7], a[0:3], a[104:107], v[4:7]\n"
    "ds_read_b64_tr_b16 a[60:61], v142 offset:24576\n"
    "v_mfma_f32_16x16x32_bf16 v[4:7], a[4:7], a[108:111], v[4:7]\n"
    "ds_read_b64_tr_b16 a[62:63], v143 offset:24576\n"
    "v_mfma_f32_16x16x32_bf16 v[8:11], a[0:3], a[112:115], v[8:11]\n"
    "s_add_u32 m0, s74, 0\n"
    "s_nop 0\n"
    "buffer_load_dwordx4 v160, s[44:47], s66 offen offset:0  lds\n"
    "v_mfma_f32_16x16x32_bf16 v[8:11], a[4:7], a[116:119], v[8:11]\n"
    "v_mfma_f32_16x16x32_bf16 v[12:15], a[0:3], a[120:123], v[12:15]\n"
    "v_mfma_f32_16x16x32_bf16 v[12:15], a[4:7], a[124:127], v[12:15]\n"
    "v_mfma_f32_16x16x32_bf16 v[16:19], a[8:11], a[96:99], v[16:19]\n"
    "ds_read_b64_tr_b16 a[0:1], v128 offset:32768\n"
    "v_mfma_f32_16x16x32_bf16 v[16:19], a[12:15], a[100:103], v[16:19]\n"
    "buffer_load_dwordx4 v161, s[44:47], s66 offen offset:1024  lds\n"
    "v_mfma_f32_16x16x32_bf16 v[20:23], a[8:11], a[104:107], v[20:23]\n"
    "ds_read_b64_tr_b16 a[2:3], v129 offset:32768\n"
    "v_mfma_f32_16x16x32_bf16 v[20:23], a[12:15], a[108:111], v[20:23]\n"
    "v_mfma_f32_16x16x32_bf16 v[24:27], a[8:11], a[112:115], v[24:27]\n"
    "ds_read_b64_tr_b16 a[4:5], v128 offset:40960\n"
    "v_mfma_f32_16x16x32_bf16 v[24:27], a[12:15], a[116:119], v[24:27]\n"
    "v_mfma_f32_16x16x32_bf16 v[28:31], a[8:11], a[120:123], v[28:31]\n"
    "ds_read_b64_tr_b16 a[6:7], v129 offset:40960\n"
    "buffer_load_dwordx4 v162, s[44:47], s66 offen offset:2048  lds\n"
    "v_mfma_f32_16x16x32_bf16 v[28:31], a[12:15], a[124:127], v[28:31]\n"
    "s_waitcnt lgkmcnt(15)\n"
    "v_mfma_f32_16x16x32_bf16 v[32:35], a[16:19], a[96:99], v[32:35]\n"
    "ds_read_b64_tr_b16 a[8:9], v130 offset:32768\n"
    "v_mfma_f32_16x16x32_bf16 v[32:35], a[20:23], a[100:103], v[32:35]\n"
    "v_mfma_f32_16x16x32_bf16 v[36:39], a[16:19], a[104:107], v[36:39]\n"
    "ds_read_b64_tr_b16 a[10:11], v131 offset:32768\n"
    "v_mfma_f32_16x16x32_bf16 v[36:39], a[20:23], a[108:111], v[36:39]\n"
    "buffer_load_dwordx4 v163, s[44:47], s66 offen offset:3072  lds\n"
    "v_mfma_f32_16x16x32_bf16 v[40:43], a[16:19], a[112:115], v[40:43]\n"
    "ds_read_b64_tr_b16 a[12:13], v130 offset:40960\n"
    "v_mfma_f32_16x16x32_bf16 v[40:43], a[20:23], a[116:119], v[40:43]\n"
    "v_mfma_f32_16x16x32_bf16 v[44:47], a[16:19], a[120:123], v[44:47]\n"
    "ds_read_b64_tr_b16 a[14:15], v131 offset:40960\n"
    "v_mfma_f32_16x16x32_bf16 v[44:47], a[20:23], a[124:127], v[44:47]\n"
    "v_mfma_f32_16x16x32_bf16 v[48:51], a[24:27], a[96:99], v[48:51]\n"
    "ds_read_b64_tr_b16 a[16:17], v132 offset:32768\n"
    "v_mfma_f32_16x16x32_bf16 v[48:51], a[28:31], a[100:103], v[48:51]\n"
    "s_add_u32 s66, s66, 131072\n"
    "v_mfma_f32_16x16x32_bf16 v[52:55], a[24:27], a[104:107], v[52:55]\n"
    "ds_read_b64_tr_b16 a[18:19], v133 offset:32768\n"
    "v_mfma_f32_16x16x32_bf16 v[52:55], a[28:31], a[108:111], v[52:55]\n"
    "v_mfma_f32_16x16x32_bf16 v[56:59], a[24:27], a[112:115], v[56:59]\n"
    "ds_read_b64_tr_b16 a[20:21], v132 offset:40960\n"
    "buffer_load_dwordx4 a[192:195], v168, s[48:51], s67 offen offset:0 sc1\n"
    "v_mfma_f32_16x16x32_bf16 v[56:59], a[28:31], a[116:119], v[56:59]\n"
    "v_mfma_f32_16x16x32_bf16 v[60:63], a[24:27], a[120:123], v[60:63]\n"
    "ds_read_b64_tr_b16 a[22:23], v133 offset:40960\n"
    "v_mfma_f32_16x16x32_bf16 v[60:63], a[28:31], a[124:127], v[60:63]\n"
    "v_mfma_f32_16x16x32_bf16 v[64:67], a[32:35], a[96:99], v[64:67]\n"
    "ds_read_b64_tr_b16 a[24:25], v134 offset:32768\n"
    "buffer_load_dwordx4 a[196:199], v168, s[48:51], s67 offen offset:1024 sc1\n"
    "v_mfma_f32_16x16x32_bf16 v[64:67], a[36:39], a[100:103], v[64:67]\n"
    "v_mfma_f32_16x16x32_bf16 v[68:71], a[32:35], a[104:107], v[68:71]\n"
    "ds_read_b64_tr_b16 a[26:27], v135 offset:32768\n"
    "v_mfma_f32_16x16x32_bf16 v[68:71], a[36:39], a[108:111], v[68:71]\n"
    "v_mfma_f32_16x16x32_bf16 v[72:75], a[32:35], a[112:115], v[72:75]\n"
    "ds_read_b64_tr_b16 a[28:29], v134 offset:40960\n"
    "buffer_load_dwordx4 a[200:203], v168, s[48:51], s67 offen offset:2048 sc1\n"
    "v_mfma_f32_16x16x32_bf16 v[72:75], a[36:39], a[116:119], v[72:75]\n"
    "v_mfma_f32_16x16x32_bf16 v[76:79], a[32:35], a[120:123], v[76:79]\n"
    "ds_read_b64_tr_b16 a[30:31], v135 offset:40960\n"
    "v_mfma_f32_16x16x32_bf16 v[76:79], a[36:39], a[124:127], v[76:79]\n"
    "s_waitcnt lgkmcnt(15)\n"
    "v_mfma_f32_16x16x32_bf16 v[80:83], a[40:43], a[96:99], v[80:83]\n"
    "ds_read_b64_tr_b16 a[32:33], v136 offset:32768\n"
    "buffer_load_dwordx4 a[204:207], v168, s[48:51], s67 offen offset:3072 sc1\n"
    "v_mfma_f32_16x16x32_bf16 v[80:83], a[44:47], a[100:103], v[80:83]\n"
    "v_mfma_f32_16x16x32_bf16 v[84:87], a[40:43], a[104:107], v[84:87]\n"
    "ds_read_b64_tr_b16 a[34:35], v137 offset:32768\n"
    "v_mfma_f32_16x16x32_bf16 v[84:87], a[44:47], a[108:111], v[84:87]\n"
    "v_mfma_f32_16x16x32_bf16 v[88:91], a[40:43], a[112:115], v[88:91]\n"
    "ds_read_b64_tr_b16 a[36:37], v136 offset:40960\n"
    "buffer_load_dwordx4 a[208:211], v169, s[48:51], s67 offen offset:0 sc1\n"
    "v_mfma_f32_16x16x32_bf16 v[88:91], a[44:47], a[116:119], v[88:91]\n"
    "v_mfma_f32_16x16x32_bf16 v[92:95], a[40:43], a[120:123], v[92:95]\n"
    "ds_read_b64_tr_b16 a[38:39], v137 offset:40960\n"
    "v_mfma_f32_16x16x32_bf16 v[92:95], a[44:47], a[124:127], v[92:95]\n"
    "v_mfma_f32_16x16x32_bf16 v[96:99], a[48:51], a[96:99], v[96:99]\n"
    "ds_read_b64_tr_b16 a[40:41], v138 offset:32768\n"
    "buffer_load_dwordx4 a[212:215], v169, s[48:51], s67 offen offset:1024 sc1\n"
    "v_mfma_f32_16x16x32_bf16 v[96:99], a[52:55], a[100:103], v[96:99]\n"
    "v_mfma_f32_16x16x32_bf16 v[100:103], a[48:51], a[104:107], v[100:103]\n"
    "ds_read_b64_tr_b16 a[42:43], v139 offset:32768\n"
    "v_mfma_f32_16x16x32_bf16 v[100:103], a[52:55], a[108:111], v[100:103]\n"
    "v_mfma_f32_16x16x32_bf16 v[104:107], a[48:51], a[112:115], v[104:107]\n"
    "ds_read_b64_tr_b16 a[44:45], v138 offset:40960\n"
    "buffer_load_dwordx4 a[216:219], v169, s[48:51], s67 offen offset:2048 sc1\n"
    "v_mfma_f32_16x16x32_bf16 v[104:107], a[52:55], a[116:119], v[104:107]\n"
    "v_mfma_f32_16x16x32_bf16 v[108:111], a[48:51], a[120:123], v[108:111]\n"
    "ds_read_b64_tr_b16 a[46:47], v139 offset:40960\n"
    "v_mfma_f32_16x16x32_bf16 v[108:111], a[52:55], a[124:127], v[108:111]\n"
    "v_mfma_f32_16x16x32_bf16 v[112:115], a[56:59], a[96:99], v[112:115]\n"
    "ds_read_b64_tr_b16 a[48:49], v140 offset:32768\n"
    "buffer_load_dwordx4 a[220:223], v169, s[48:51], s67 offen offset:3072 sc1\n"
    "v_mfma_f32_16x16x32_bf16 v[112:115], a[60:63], a[100:103], v[112:115]\n"
    "ds_read_b64_tr_b16 a[50:51], v141 offset:32768\n"
    "v_mfma_f32_16x16x32_bf16 v[116:119], a[56:59], a[104:107], v[116:119]\n"
    "v_mfma_f32_16x16x32_bf16 v[116:119], a[60:63], a[108:111], v[116:119]\n"
    "ds_read_b64_tr_b16 a[52:53], v140 offset:40960\n"
    "v_mfma_f32_16x16x32_bf16 v[120:123], a[56:59], a[112:115], v[120:123]\n"
    "v_mfma_f32_16x16x32_bf16 v[120:123], a[60:63], a[116:119], v[120:123]\n"
    "ds_read_b64_tr_b16 a[54:55], v141 offset:40960\n"
    "s_add_u32 s67, s67, 262144\n"
    "v_mfma_f32_16x16x32_bf16 v[124:127], a[56:59], a[120:123], v[124:127]\n"
    "s_add_u32 s78, s78, 1\n"
    "s_cmp_ge_u32 s78, s69\n"
    "s_cselect_b32 s46, 0, s46\n"
    "s_sub_u32 s70, s78, 2\n"
    "s_cmp_ge_u32 s70, s69\n"
    "s_cselect_b32 s50, 0, s50\n"
    "v_mfma_f32_16x16x32_bf16 v[124:127], a[60:63], a[124:127], v[124:127]\n"
    "LD_pos2_%=:\n"
    "s_waitcnt vmcnt(24)\n"
    "s_barrier\n"
    "s_waitcnt lgkmcnt(15)\n"
    "v_mfma_f32_16x16x32_bf16 v[0:3], a[0:3], a[128:131], v[0:3]\n"
    "ds_read_b64_tr_b16 a[56:57], v142 offset:32768\n"
    "v_mfma_f32_16x16x32_bf16 v[0:3], a[4:7], a[132:135], v[0:3]\n"
    "ds_read_b64_tr_b16 a[58:59], v143 offset:32768\n"
    "v_mfma_f32_16x16x32_bf16 v[4:7], a[0:3], a[136:139], v[4:7]\n"
    "ds_read_b64_tr_b16 a[60:61], v142 offset:40960\n"
    "v_mfma_f32_16x16x32_bf16 v[4:7], a[4:7], a[140:143], v[4:7]\n"
    "ds_read_b64_tr_b16 a[62:63], v143 offset:40960\n"
    "v_mfma_f32_16x16x32_bf16 v[8:11], a[0:3], a[144:147], v[8:11]\n"
    "s_add_u32 m0, s74, 16384\n"
    "s_nop 0\n"
    "buffer_load_dwordx4 v160, s[44:47], s66 offen offset:0  lds\n"
    "v_mfma_f32_16x16x32_bf16 v[8:11], a[4:7], a[148:151], v[8:11]\n"
    "v_mfma_f32_16x16x32_bf16 v[12:15], a[0:3], a[152:155], v[12:15]\n"
    "v_mfma_f32_16x16x32_bf16 v[12:15], a[4:7], a[156:159], v[12:15]\n"
    "v_mfma_f32_16x16x32_bf16 v[16:19], a[8:11], a[128:131], v[16:19]\n"
    "ds_read_b64_tr_b16 a[0:1], v144 offset:0\n"
    "v_mfma_f32_16x16x32_bf16 v[16:19], a[12:15], a[132:135], v[16:19]\n"
    "buffer_load_dwordx4 v161, s[44:47], s66 offen offset:1024  lds\n"
    "v_mfma_f32_16x16x32_bf16 v[20:23], a[8:11], a[136:139], v[20:23]\n"
    "ds_read_b64_tr_b16 a[2:3], v145 offset:0\n"
    "v_mfma_f32_16x16x32_bf16 v[20:23], a[12:15], a[140:143], v[20:23]\n"
    "v_mfma_f32_16x16x32_bf16 v[24:27], a[8:11], a[144:147], v[24:27]\n"
    "ds_read_b64_tr_b16 a[4:5], v144 offset:8192\n"
    "v_mfma_f32_16x16x32_bf16 v[24:27], a[12:15], a[148:151], v[24:27]\n"
    "v_mfma_f32_16x16x32_bf16 v[28:31], a[8:11], a[152:155], v[28:31]\n"
    "ds_read_b64_tr_b16 a[6:7], v145 offset:8192\n"
    "buffer_load_dwordx4 v162, s[44:47], s66 offen offset:2048  lds\n"
    "v_mfma_f32_16x16x32_bf16 v[28:31], a[12:15], a[156:159], v[28:31]\n"
    "v_mfma_f32_16x16x32_bf16 v[32:35], a[16:19], a[128:131], v[32:35]\n"
    "ds_read_b64_tr_b16 a[8:9], v146 offset:0\n"
    "v_mfma_f32_16x16x32_bf16 v[32:35], a[20:23], a[132:135], v[32:35]\n"
    "v_mfma_f32_16x16x32_bf16 v[36:39], a[16:19], a[136:139], v[36:39]\n"
    "ds_read_b64_tr_b16 a[10:11], v147 offset:0\n"
    "v_mfma_f32_16x16x32_bf16 v[36:39], a[20:23], a[140:143], v[36:39]\n"
    "buffer_load_dwordx4 v163, s[44:47], s66 offen offset:3072  lds\n"
    "v_mfma_f32_16x16x32_bf16 v[40:43], a[16:19], a[144:147], v[40:43]\n"
    "ds_read_b64_tr_b16 a[12:13], v146 offset:8192\n"
    "v_mfma_f32_16x16x32_bf16 v[40:43], a[20:23], a[148:151], v[40:43]\n"
    "v_mfma_f32_16x16x32_bf16 v[44:47], a[16:19], a[152:155], v[44:47]\n"
    "ds_read_b64_tr_b16 a[14:15], v147 offset:8192\n"
    "v_mfma_f32_16x16x32_bf16 v[44:47], a[20:23], a[156:159], v[44:47]\n"
    "s_waitcnt lgkmcnt(15)\n"
    "v_mfma_f32_16x16x32_bf16 v[48:51], a[24:27], a[128:131], v[48:51]\n"
    "ds_read_b64_tr_b16 a[16:17], v148 offset:0\n"
    "v_mfma_f32_16x16x32_bf16 v[48:51], a[28:31], a[132:135], v[48:51]\n"
    "s_add_u32 s66, s66, 131072\n"
    "v_mfma_f32_16x16x32_bf16 v[52:55], a[24:27], a[136:139], v[52:55]\n"
    "ds_read_b64_tr_b16 a[18:19], v149 offset:0\n"
    "v_mfma_f32_16x16x32_bf16 v[52:55], a[28:31], a[140:143], v[52:55]\n"
    "v_mfma_f32_16x16x32_bf16 v[56:59], a[24:27], a[144:147], v[56:59]\n"
    "ds_read_b64_tr_b16 a[20:21], v148 offset:8192\n"
    "buffer_load_dwordx4 a[224:227], v168, s[48:51], s67 offen offset:0 sc1\n"
    "v_mfma_f32_16x16x32_bf16 v[56:59], a[28:31], a[148:151], v[56:59]\n"
    "v_mfma_f32_16x16x32_bf16 v[60:63], a[24:27], a[152:155], v[60:63]\n"
    "ds_read_b64_tr_b16 a[22:23], v149 offset:8192\n"
    "v_mfma_f32_16x16x32_bf16 v[60:63], a[28:31], a[156:159], v[60:63]\n"
    "v_mfma_f32_16x16x32_bf16 v[64:67], a[32:35], a[128:131], v[64:67]\n"
    "ds_read_b64_tr_b16 a[24:25], v150 offset:0\n"
    "buffer_load_dwordx4 a[228:231], v168, s[48:51], s67 offen offset:1024 sc1\n"
    "v_mfma_f32_16x16x32_bf16 v[64:67], a[36:39], a[132:135], v[64:67]\n"
    "v_mfma_f32_16x16x32_bf16 v[68:71], a[32:35], a[136:139], v[68:71]\n"
    "ds_read_b64_tr_b16 a[26:27], v151 offset:0\n"
    "v_mfma_f32_16x16x32_bf16 v[68:71], a[36:39], a[140:143], v[68:71]\n"
    "v_mfma_f32_16x16x32_bf16 v[72:75], a[32:35], a[144:147], v[72:75]\n"
    "ds_read_b64_tr_b16 a[28:29], v150 offset:8192\n"
    "buffer_load_dwordx4 a[232:235], v168, s[48:51], s67 offen offset:2048 sc1\n"
    "v_mfma_f32_16x16x32_bf16 v[72:75], a[36:39], a[148:151], v[72:75]\n"
    "v_mfma_f32_16x16x32_bf16 v[76:79], a[32:35], a[152:155], v[76:79]\n"
    "ds_read_b64_tr_b16 a[30:31], v151 offset:8192\n"
    "v_mfma_f32_16x16x32_bf16 v[76:79], a[36:39], a[156:159], v[76:79]\n"
    "v_mfma_f32_16x16x32_bf16 v[80:83], a[40:43], a[128:131], v[80:83]\n"
    "ds_read_b64_tr_b16 a[32:33], v152 offset:0\n"
    "buffer_load_dwordx4 a[236:239], v168, s[48:51], s67 offen offset:3072 sc1\n"
    "v_mfma_f32_16x16x32_bf16 v[80:83], a[44:47], a[132:135], v[80:83]\n"
    "v_mfma_f32_16x16x32_bf16 v[84:87], a[40:43], a[136:139], v[84:87]\n"
    "ds_read_b64_tr_b16 a[34:35], v153 offset:0\n"
    "v_mfma_f32_16x16x32_bf16 v[84:87], a[44:47], a[140:143], v[84:87]\n"
    "v_mfma_f32_16x16x32_bf16 v[88:91], a[40:43], a[144:147], v[88:91]\n"
    "ds_read_b64_tr_b16 a[36:37], v152 offset:8192\n"
    "buffer_load_dwordx4 a[240:243], v169, s[48:51], s67 offen offset:0 sc1\n"
    "v_mfma_f32_16x16x32_bf16 v[88:91], a[44:47], a[148:151], v[88:91]\n"
    "v_mfma_f32_16x16x32_bf16 v[92:95], a[40:43], a[152:155], v[92:95]\n"
    "ds_read_b64_tr_b16 a[38:39], v153 offset:8192\n"
    "v_mfma_f32_16x16x32_bf16 v[92:95], a[44:47], a[156:159], v[92:95]\n"
    "s_waitcnt lgkmcnt(15)\n"
    "v_mfma_f32_16x16x32_bf16 v[96:99], a[48:51], a[128:131], v[96:99]\n"
    "ds_read_b64_tr_b16 a[40:41], v154 offset:0\n"
    "buffer_load_dwordx4 a[244:247], v169, s[48:51], s67 offen offset:1024 sc1\n"
    "v_mfma_f32_16x16x32_bf16 v[96:99], a[52:55], a[132:135], v[96:99]\n"
    "v_mfma_f32_16x16x32_bf16 v[100:103], a[48:51], a[136:139], v[100:103]\n"
    "ds_read_b64_tr_b16 a[42:43], v155 offset:0\n"
    "v_mfma_f32_16x16x32_bf16 v[100:103], a[52:55], a[140:143], v[100:103]\n"
    "v_mfma_f32_16x16x32_bf16 v[104:107], a[48:51], a[144:147], v[104:107]\n"
    "ds_read_b64_tr_b16 a[44:45], v154 offset:8192\n"
    "buffer_load_dwordx4 a[248:251], v169, s[48:51], s67 offen offset:2048 sc1\n"
    "v_mfma_f32_16x16x32_bf16 v[104:107], a[52:55], a[148:151], v[104:107]\n"
    "v_mfma_f32_16x16x32_bf16 v[108:111], a[48:51], a[152:155], v[108:111]\n"
    "ds_read_b64_tr_b16 a[46:47], v155 offset:8192\n"
    "v_mfma_f32_16x16x32_bf16 v[108:111], a[52:55], a[156:159], v[108:111]\n"
    "v_mfma_f32_16x16x32_bf16 v[112:115], a[56:59], a[128:131], v[112:115]\n"
    "ds_read_b64_tr_b16 a[48:49], v156 offset:0\n"
    "buffer_load_dwordx4 a[252:255], v169, s[48:51], s67 offen offset:3072 sc1\n"
    "v_mfma_f32_16x16x32_bf16 v[112:115], a[60:63], a[132:135], v[112:115]\n"
    "ds_read_b64_tr_b16 a[50:51], v157 offset:0\n"
    "v_mfma_f32_16x16x32_bf16 v[116:119], a[56:59], a[136:139], v[116:119]\n"
    "v_mfma_f32_16x16x32_bf16 v[116:119], a[60:63], a[140:143], v[116:119]\n"
    "ds_read_b64_tr_b16 a[52:53], v156 offset:8192\n"
    "v_mfma_f32_16x16x32_bf16 v[120:123], a[56:59], a[144:147], v[120:123]\n"
    "v_mfma_f32_16x16x32_bf16 v[120:123], a[60:63], a[148:151], v[120:123]\n"
    "ds_read_b64_tr_b16 a[54:55], v157 offset:8192\n"
    "s_add_u32 s67, s67, 262144\n"
    "v_mfma_f32_16x16x32_bf16 v[124:127], a[56:59], a[152:155], v[124:127]\n"
    "s_add_u32 s78, s78, 1\n"
    "s_cmp_ge_u32 s78, s69\n"
    "s_cselect_b32 s46, 0, s46\n"
    "s_sub_u32 s70, s78, 2\n"
    "s_cmp_ge_u32 s70, s69\n"
    "s_cselect_b32 s50, 0, s50\n"
    "v_mfma_f32_16x16x32_bf16 v[124:127], a[60:63], a[156:159], v[124:127]\n"
    "LD_pos3_%=:\n"
    "s_waitcnt vmcnt(24)\n"
    "s_barrier\n"
    "v_mfma_f32_16x16x32_bf16 v[0:3], a[0:3], a[160:163], v[0:3]\n"
    "ds_read_b64_tr_b16 a[56:57], v158 offset:0\n"
    "v_mfma_f32_16x16x32_bf16 v[0:3], a[4:7], a[164:167], v[0:3]\n"
    "ds_read_b64_tr_b16 a[58:59], v159 offset:0\n"
    "v_mfma_f32_16x16x32_bf16 v[4:7], a[0:3], a[168:171], v[4:7]\n"
    "ds_read_b64_tr_b16 a[60:61], v158 offset:8192\n"
    "v_mfma_f32_16x16x32_bf16 v[4:7], a[4:7], a[172:175], v[4:7]\n"
    "ds_read_b64_tr_b16 a[62:63], v159 offset:8192\n"
    "v_mfma_f32_16x16x32_bf16 v[8:11], a[0:3], a[176:179], v[8:11]\n"
    "s_add_u32 m0, s74, 32768\n"
    "s_nop 0\n"
    "buffer_load_dwordx4 v160, s[44:47], s66 offen offset:0  lds\n"
    "v_mfma_f32_16x16x32_bf16 v[8:11], a[4:7], a[180:183], v[8:11]\n"
    "v_mfma_f32_16x16x32_bf16 v[12:15], a[0:3], a[184:187], v[12:15]\n"
    "v_mfma_f32_16x16x32_bf16 v[12:15], a[4:7], a[188:191], v[12:15]\n"
    "s_waitcnt lgkmcnt(15)\n"
    "v_mfma_f32_16x16x32_bf16 v[16:19], a[8:11], a[160:163], v[16:19]\n"
    "ds_read_b64_tr_b16 a[0:1], v144 offset:16384\n"
    "v_mfma_f32_16x16x32_bf16 v[16:19], a[12:15], a[164:167], v[16:19]\n"
    "buffer_load_dwordx4 v161, s[44:47], s66 offen offset:1024  lds\n"
    "v_mfma_f32_16x16x32_bf16 v[20:23], a[8:11], a[168:171], v[20:23]\n"
    "ds_read_b64_tr_b16 a[2:3], v145 offset:16384\n"
    "v_mfma_f32_16x16x32_bf16 v[20:23], a[12:15], a[172:175], v[20:23]\n"
    "v_mfma_f32_16x16x32_bf16 v[24:27], a[8:11], a[176:179], v[24:27]\n"
    "ds_read_b64_tr_b16 a[4:5], v144 offset:24576\n"
    "v_mfma_f32_16x16x32_bf16 v[24:27], a[12:15], a[180:183], v[24:27]\n"
    "v_mfma_f32_16x16x32_bf16 v[28:31], a[8:11], a[184:187], v[28:31]\n"
    "ds_read_b64_tr_b16 a[6:7], v145 offset:24576\n"
    "buffer_load_dwordx4 v162, s[44:47], s66 offen offset:2048  lds\n"
    "v_mfma_f32_16x16x32_bf16 v[28:31], a[12:15], a[188:191], v[28:31]\n"
    "v_mfma_f32_16x16x32_bf16 v[32:35], a[16:19], a[160:163], v[32:35]\n"
    "ds_read_b64_tr_b16 a[8:9], v146 offset:16384\n"
    "v_mfma_f32_16x16x32_bf16 v[32:35], a[20:23], a[164:167], v[32:35]\n"
    "v_mfma_f32_16x16x32_bf16 v[36:39], a[16:19], a[168:171], v[36:39]\n"
    "ds_read_b64_tr_b16 a[10:11], v147 offset:16384\n"
    "v_mfma_f32_16x16x32_bf16 v[36:39], a[20:23], a[172:175], v[36:39]\n"
    "buffer_load_dwordx4 v163, s[44:47], s66 offen offset:3072  lds\n"
    "v_mfma_f32_16x16x32_bf16 v[40:43], a[16:19], a[176:179], v[40:43]\n"
    "ds_read_b64_tr_b16 a[12:13], v146 offset:24576\n"
    "v_mfma_f32_16x16x32_bf16 v[40:43], a[20:23], a[180:183], v[40:43]\n"
    "v_mfma_f32_16x16x32_bf16 v[44:47], a[16:19], a[184:187], v[44:47]\n"
    "ds_read_b64_tr_b16 a[14:15], v147 offset:24576\n"
    "v_mfma_f32_16x16x32_bf16 v[44:47], a[20:23], a[188:191], v[44:47]\n"
    "v_mfma_f32_16x16x32_bf16 v[48:51], a[24:27], a[160:163], v[48:51]\n"
    "ds_read_b64_tr_b16 a[16:17], v148 offset:16384\n"
    "v_mfma_f32_16x16x32_bf16 v[48:51], a[28:31], a[164:167], v[48:51]\n"
    "s_add_u32 s66, s66, 131072\n"
    "v_mfma_f32_16x16x32_bf16 v[52:55], a[24:27], a[168:171], v[52:55]\n"
    "ds_read_b64_tr_b16 a[18:19], v149 offset:16384\n"
    "v_mfma_f32_16x16x32_bf16 v[52:55], a[28:31], a[172:175], v[52:55]\n"
    "v_mfma_f32_16x16x32_bf16 v[56:59], a[24:27], a[176:179], v[56:59]\n"
    "ds_read_b64_tr_b16 a[20:21], v148 offset:24576\n"
    "buffer_load_dwordx4 a[64:67], v168, s[48:51], s67 offen offset:0 sc1\n"
    "v_mfma_f32_16x16x32_bf16 v[56:59], a[28:31], a[180:183], v[56:59]\n"
    "v_mfma_f32_16x16x32_bf16 v[60:63], a[24:27], a[184:187], v[60:63]\n"
    "ds_read_b64_tr_b16 a[22:23], v149 offset:24576\n"
    "v_mfma_f32_16x16x32_bf16 v[60:63], a[28:31], a[188:191], v[60:63]\n"
    "s_waitcnt lgkmcnt(15)\n"
    "v_mfma_f32_16x16x32_bf16 v[64:67], a[32:35], a[160:163], v[64:67]\n"
    "ds_read_b64_tr_b16 a[24:25], v150 offset:16384\n"
    "buffer_load_dwordx4 a[68:71], v168, s[48:51], s67 offen offset:1024 sc1\n"
    "v_mfma_f32_16x16x32_bf16 v[64:67], a[36:39], a[164:167], v[64:67]\n"
    "v_mfma_f32_16x16x32_bf16 v[68:71], a[32:35], a[168:171], v[68:71]\n"
    "ds_read_b64_tr_b16 a[26:27], v151 offset:16384\n"
    "v_mfma_f32_16x16x32_bf16 v[68:71], a[36:39], a[172:175], v[68:71]\n"
    "v_mfma_f32_16x16x32_bf16 v[72:75], a[32:35], a[176:179], v[72:75]\n"
    "ds_read_b64_tr_b16 a[28:29], v150 offset:24576\n"
    "buffer_load_dwordx4 a[72:75], v168, s[48:51], s67 offen offset:2048 sc1\n"
    "v_mfma_f32_16x16x32_bf16 v[72:75], a[36:39], a[180:183], v[72:75]\n"
    "v_mfma_f32_16x16x32_bf16 v[76:79], a[32:35], a[184:187], v[76:79]\n"
    "ds_read_b64_tr_b16 a[30:31], v151 offset:24576\n"
    "v_mfma_f32_16x16x32_bf16 v[76:79], a[36:39], a[188:191], v[76:79]\n"
    "v_mfma_f32_16x16x32_bf16 v[80:83], a[40:43], a[160:163], v[80:83]\n"
    "ds_read_b64_tr_b16 a[32:33], v152 offset:16384\n"
    "buffer_load_dwordx4 a[76:79], v168, s[48:51], s67 offen offset:3072 sc1\n"
    "v_mfma_f32_16x16x32_bf16 v[80:83], a[44:47], a[164:167], v[80:83]\n"
    "v_mfma_f32_16x16x32_bf16 v[84:87], a[40:43], a[168:171], v[84:87]\n"
    "ds_read_b64_tr_b16 a[34:35], v153 offset:16384\n"
    "v_mfma_f32_16x16x32_bf16 v[84:87], a[44:47], a[172:175], v[84:87]\n"
    "v_mfma_f32_16x16x32_bf16 v[88:91], a[40:43], a[176:179], v[88:91]\n"
    "ds_read_b64_tr_b16 a[36:37], v152 offset:24576\n"
    "buffer_load_dwordx4 a[80:83], v169, s[48:51], s67 offen offset:0 sc1\n"
    "v_mfma_f32_16x16x32_bf16 v[88:91], a[44:47], a[180:183], v[88:91]\n"
    "v_mfma_f32_16x16x32_bf16 v[92:95], a[40:43], a[184:187], v[92:95]\n"
    "ds_read_b64_tr_b16 a[38:39], v153 offset:24576\n"
    "v_mfma_f32_16x16x32_bf16 v[92:95], a[44:47], a[188:191], v[92:95]\n"
    "v_mfma_f32_16x16x32_bf16 v[96:99], a[48:51], a[160:163], v[96:99]\n"
    "ds_read_b64_tr_b16 a[40:41], v154 offset:16384\n"
    "buffer_load_dwordx4 a[84:87], v169, s[48:51], s67 offen offset:1024 sc1\n"
    "v_mfma_f32_16x16x32_bf16 v[96:99], a[52:55], a[164:167], v[96:99]\n"
    "v_mfma_f32_16x16x32_bf16 v[100:103], a[48:51], a[168:171], v[100:103]\n"
    "ds_read_b64_tr_b16 a[42:43], v155 offset:16384\n"
    "v_mfma_f32_16x16x32_bf16 v[100:103], a[52:55], a[172:175], v[100:103]\n"
    "v_mfma_f32_16x16x32_bf16 v[104:107], a[48:51], a[176:179], v[104:107]\n"
    "ds_read_b64_tr_b16 a[44:45], v154 offset:24576\n"
    "buffer_load_dwordx4 a[88:91], v169, s[48:51], s67 offen offset:2048 sc1\n"
    "v_mfma_f32_16x16x32_bf16 v[104:107], a[52:55], a[180:183], v[104:107]\n"
    "v_mfma_f32_16x16x32_bf16 v[108:111], a[48:51], a[184:187], v[108:111]\n"
    "ds_read_b64_tr_b16 a[46:47], v155 offset:24576\n"
    "v_mfma_f32_16x16x32_bf16 v[108:111], a[52:55], a[188:191], v[108:111]\n"
    "s_waitcnt lgkmcnt(15)\n"
    "v_mfma_f32_16x16x32_bf16 v[112:115], a[56:59], a[160:163], v[112:115]\n"
    "ds_read_b64_tr_b16 a[48:49], v156 offset:16384\n"
    "buffer_load_dwordx4 a[92:95], v169, s[48:51], s67 offen offset:3072 sc1\n"
    "v_mfma_f32_16x16x32_bf16 v[112:115], a[60:63], a[164:167], v[112:115]\n"
    "ds_read_b64_tr_b16 a[50:51], v157 offset:16384\n"
    "v_mfma_f32_16x16x32_bf16 v[116:119], a[56:59], a[168:171], v[116:119]\n"
    "v_mfma_f32_16x16x32_bf16 v[116:119], a[60:63], a[172:175], v[116:119]\n"
    "ds_read_b64_tr_b16 a[52:53], v156 offset:24576\n"
    "v_mfma_f32_16x16x32_bf16 v[120:123], a[56:59], a[176:179], v[120:123]\n"
    "v_mfma_f32_16x16x32_bf16 v[120:123], a[60:63], a[180:183], v[120:123]\n"
    "ds_read_b64_tr_b16 a[54:55], v157 offset:24576\n"
    "s_add_u32 s67, s67, 262144\n"
    "v_mfma_f32_16x16x32_bf16 v[124:127], a[56:59], a[184:187], v[124:127]\n"
    "s_add_u32 s78, s78, 1\n"
    "s_cmp_ge_u32 s78, s69\n"
    "s_cselect_b32 s46, 0, s46\n"
    "s_sub_u32 s70, s78, 2\n"
    "s_cmp_ge_u32 s70, s69\n"
    "s_cselect_b32 s50, 0, s50\n"
    "v_mfma_f32_16x16x32_bf16 v[124:127], a[60:63], a[188:191], v[124:127]\n"
    "LD_pos4_%=:\n"
    "s_waitcnt vmcnt(24)\n"
    "s_barrier\n"
    "v_mfma_f32_16x16x32_bf16 v[0:3], a[0:3], a[192:195], v[0:3]\n"
    "ds_read_b64_tr_b16 a[56:57], v158 offset:16384\n"
    "v_mfma_f32_16x16x32_bf16 v[0:3], a[4:7], a[196:199], v[0:3]\n"
    "ds_read_b64_tr_b16 a[58:59], v159 offset:16384\n"
    "v_mfma_f32_16x16x32_bf16 v[4:7], a[0:3], a[200:203], v[4:7]\n"
    "ds_read_b64_tr_b16 a[60:61], v158 offset:24576\n"
    "v_mfma_f32_16x16x32_bf16 v[4:7], a[4:7], a[204:207], v[4:7]\n"
    "ds_read_b64_tr_b16 a[62:63], v159 offset:24576\n"
    "v_mfma_f32_16x16x32_bf16 v[8:11], a[0:3], a[208:211], v[8:11]\n"
    "s_add_u32 m0, s74, 49152\n"
    "s_nop 0\n"
    "buffer_load_dwordx4 v160, s[44:47], s66 offen offset:0  lds\n"
    "v_mfma_f32_16x16x32_bf16 v[8:11], a[4:7], a[212:215], v[8:11]\n"
    "v_mfma_f32_16x16x32_bf16 v[12:15], a[0:3], a[216:219], v[12:15]\n"
    "v_mfma_f32_16x16x32_bf16 v[12:15], a[4:7], a[220:223], v[12:15]\n"
    "v_mfma_f32_16x16x32_bf16 v[16:19], a[8:11], a[192:195], v[16:19]\n"
    "ds_read_b64_tr_b16 a[0:1], v144 offset:32768\n"
    "v_mfma_f32_16x16x32_bf16 v[16:19], a[12:15], a[196:199], v[16:19]\n"
    "buffer_load_dwordx4 v161, s[44:47], s66 offen offset:1024  lds\n"
    "v_mfma_f32_16x16x32_bf16 v[20:23], a[8:11], a[200:203], v[20:23]\n"
    "ds_read_b64_tr_b16 a[2:3], v145 offset:32768\n"
    "v_mfma_f32_16x16x32_bf16 v[20:23], a[12:15], a[204:207], v[20:23]\n"
    "v_mfma_f32_16x16x32_bf16 v[24:27], a[8:11], a[208:211], v[24:27]\n"
    "ds_read_b64_tr_b16 a[4:5], v144 offset:40960\n"
    "v_mfma_f32_16x16x32_bf16 v[24:27], a[12:15], a[212:215], v[24:27]\n"
    "v_mfma_f32_16x16x32_bf16 v[28:31], a[8:11], a[216:219], v[28:31]\n"
    "ds_read_b64_tr_b16 a[6:7], v145 offset:40960\n"
    "buffer_load_dwordx4 v162, s[44:47], s66 offen offset:2048  lds\n"
    "v_mfma_f32_16x16x32_bf16 v[28:31], a[12:15], a[220:223], v[28:31]\n"
    "s_waitcnt lgkmcnt(15)\n"
    "v_mfma_f32_16x16x32_bf16 v[32:35], a[16:19], a[192:195], v[32:35]\n"
    "ds_read_b64_tr_b16 a[8:9], v146 offset:32768\n"
    "v_mfma_f32_16x16x32_bf16 v[32:35], a[20:23], a[196:199], v[32:35]\n"
    "v_mfma_f32_16x16x32_bf16 v[36:39], a[16:19], a[200:203], v[36:39]\n"
    "ds_read_b64_tr_b16 a[10:11], v147 offset:32768\n"
    "v_mfma_f32_16x16x32_bf16 v[36:39], a[20:23], a[204:207], v[36:39]\n"
    "buffer_load_dwordx4 v163, s[44:47], s66 offen offset:3072  lds\n"
    "v_mfma_f32_16x16x32_bf16 v[40:43], a[16:19], a[208:211], v[40:43]\n"
    "ds_read_b64_tr_b16 a[12:13], v146 offset:40960\n"
    "v_mfma_f32_16x16x32_bf16 v[40:43], a[20:23], a[212:215], v[40:43]\n"
    "v_mfma_f32_16x16x32_bf16 v[44:47], a[16:19], a[216:219], v[44:47]\n"
    "ds_read_b64_tr_b16 a[14:15], v147 offset:40960\n"
    "v_mfma_f32_16x16x32_bf16 v[44:47], a[20:23], a[220:223], v[44:47]\n"
    "v_mfma_f32_16x16x32_bf16 v[48:51], a[24:27], a[192:195], v[48:51]\n"
    "ds_read_b64_tr_b16 a[16:17], v148 offset:32768\n"
    "v_mfma_f32_16x16x32_bf16 v[48:51], a[28:31], a[196:199], v[48:51]\n"
    "s_add_u32 s66, s66, 131072\n"
    "v_mfma_f32_16x16x32_bf16 v[52:55], a[24:27], a[200:203], v[52:55]\n"
    "ds_read_b64_tr_b16 a[18:19], v149 offset:32768\n"
    "v_mfma_f32_16x16x32_bf16 v[52:55], a[28:31], a[204:207], v[52:55]\n"
    "v_mfma_f32_16x16x32_bf16 v[56:59], a[24:27], a[208:211], v[56:59]\n"
    "ds_read_b64_tr_b16 a[20:21], v148 offset:40960\n"
    "buffer_load_dwordx4 a[96:99], v168, s[48:51], s67 offen offset:0 sc1\n"
    "v_mfma_f32_16x16x32_bf16 v[56:59], a[28:31], a[212:215], v[56:59]\n"
    "v_mfma_f32_16x16x32_bf16 v[60:63], a[24:27], a[216:219], v[60:63]\n"
    "ds_read_b64_tr_b16 a[22:23], v149 offset:40960\n"
    "v_mfma_f32_16x16x32_bf16 v[60:63], a[28:31], a[220:223], v[60:63]\n"
    "v_mfma_f32_16x16x32_bf16 v[64:67], a[32:35], a[192:195], v[64:67]\n"
    "ds_read_b64_tr_b16 a[24:25], v150 offset:32768\n"
    "buffer_load_dwordx4 a[100:103], v168, s[48:51], s67 offen offset:1024 sc1\n"
    "v_mfma_f32_16x16x32_bf16 v[64:67], a[36:39], a[196:199], v[64:67]\n"
    "v_mfma_f32_16x16x32_bf16 v[68:71], a[32:35], a[200:203], v[68:71]\n"
    "ds_read_b64_tr_b16 a[26:27], v151 offset:32768\n"
    "v_mfma_f32_16x16x32_bf16 v[68:71], a[36:39], a[204:207], v[68:71]\n"
    "v_mfma_f32_16x16x32_bf16 v[72:75], a[32:35], a[208:211], v[72:75]\n"
    "ds_read_b64_tr_b16 a[28:29], v150 offset:40960\n"
    "buffer_load_dwordx4 a[104:107], v168, s[48:51], s67 offen offset:2048 sc1\n"
    "v_mfma_f32_16x16x32_bf16 v[72:75], a[36:39], a[212:215], v[72:75]\n"
    "v_mfma_f32_16x16x32_bf16 v[76:79], a[32:35], a[216:219], v[76:79]\n"
    "ds_read_b64_tr_b16 a[30:31], v151 offset:40960\n"
    "v_mfma_f32_16x16x32_bf16 v[76:79], a[36:39], a[220:223], v[76:79]\n"
    "s_waitcnt lgkmcnt(15)\n"
    "v_mfma_f32_16x16x32_bf16 v[80:83], a[40:43], a[192:195], v[80:83]\n"
    "ds_read_b64_tr_b16 a[32:33], v152 offset:32768\n"
    "buffer_load_dwordx4 a[108:111], v168, s[48:51], s67 offen offset:3072 sc1\n"
    "v_mfma_f32_16x16x32_bf16 v[80:83], a[44:47], a[196:199], v[80:83]\n"
    "v_mfma_f32_16x16x32_bf16 v[84:87], a[40:43], a[200:203], v[84:87]\n"
    "ds_read_b64_tr_b16 a[34:35], v153 offset:32768\n"
    "v_mfma_f32_16x16x32_bf16 v[84:87], a[44:47], a[204:207], v[84:87]\n"
    "v_mfma_f32_16x16x32_bf16 v[88:91], a[40:43], a[208:211], v[88:91]\n"
    "ds_read_b64_tr_b16 a[36:37], v152 offset:40960\n"
    "buffer_load_dwordx4 a[112:115], v169, s[48:51], s67 offen offset:0 sc1\n"
    "v_mfma_f32_16x16x32_bf16 v[88:91], a[44:47], a[212:215], v[88:91]\n"
    "v_mfma_f32_16x16x32_bf16 v[92:95], a[40:43], a[216:219], v[92:95]\n"
    "ds_read_b64_tr_b16 a[38:39], v153 offset:40960\n"
    "v_mfma_f32_16x16x32_bf16 v[92:95], a[44:47], a[220:223], v[92:95]\n"
    "v_mfma_f32_16x16x32_bf16 v[96:99], a[48:51], a[192:195], v[96:99]\n"
    "ds_read_b64_tr_b16 a[40:41], v154 offset:32768\n"
    "buffer_load_dwordx4 a[116:119], v169, s[48:51], s67 offen offset:1024 sc1\n"
    "v_mfma_f32_16x16x32_bf16 v[96:99], a[52:55], a[196:199], v[96:99]\n"
    "v_mfma_f32_16x16x32_bf16 v[100:103], a[48:51], a[200:203], v[100:103]\n"
    "ds_read_b64_tr_b16 a[42:43], v155 offset:32768\n"
    "v_mfma_f32_16x16x32_bf16 v[100:103], a[52:55], a[204:207], v[100:103]\n"
    "v_mfma_f32_16x16x32_bf16 v[104:107], a[48:51], a[208:211], v[104:107]\n"
    "ds_read_b64_tr_b16 a[44:45], v154 offset:40960\n"
    "buffer_load_dwordx4 a[120:123], v169, s[48:51], s67 offen offset:2048 sc1\n"
    "v_mfma_f32_16x16x32_bf16 v[104:107], a[52:55], a[212:215], v[104:107]\n"
    "v_mfma_f32_16x16x32_bf16 v[108:111], a[48:51], a[216:219], v[108:111]\n"
    "ds_read_b64_tr_b16 a[46:47], v155 offset:40960\n"
    "v_mfma_f32_16x16x32_bf16 v[108:111], a[52:55], a[220:223], v[108:111]\n"
    "v_mfma_f32_16x16x32_bf16 v[112:115], a[56:59], a[192:195], v[112:115]\n"
    "ds_read_b64_tr_b16 a[48:49], v156 offset:32768\n"
    "buffer_load_dwordx4 a[124:127], v169, s[48:51], s67 offen offset:3072 sc1\n"
    "v_mfma_f32_16x16x32_bf16 v[112:115], a[60:63], a[196:199], v[112:115]\n"
    "ds_read_b64_tr_b16 a[50:51], v157 offset:32768\n"
    "v_mfma_f32_16x16x32_bf16 v[116:119], a[56:59], a[200:203], v[116:119]\n"
    "v_mfma_f32_16x16x32_bf16 v[116:119], a[60:63], a[204:207], v[116:119]\n"
    "ds_read_b64_tr_b16 a[52:53], v156 offset:40960\n"
    "v_mfma_f32_16x16x32_bf16 v[120:123], a[56:59], a[208:211], v[120:123]\n"
    "v_mfma_f32_16x16x32_bf16 v[120:123], a[60:63], a[212:215], v[120:123]\n"
    "ds_read_b64_tr_b16 a[54:55], v157 offset:40960\n"
    "s_add_u32 s67, s67, 262144\n"
    "v_mfma_f32_16x16x32_bf16 v[124:127], a[56:59], a[216:219], v[124:127]\n"
    "s_add_u32 s78, s78, 1\n"
    "s_cmp_ge_u32 s78, s69\n"
    "s_cselect_b32 s46, 0, s46\n"
    "s_sub_u32 s70, s78, 2\n"
    "s_cmp_ge_u32 s70, s69\n"
    "s_cselect_b32 s50, 0, s50\n"
    "v_mfma_f32_16x16x32_bf16 v[124:127], a[60:63], a[220:223], v[124:127]\n"
    "LD_pos5_%=:\n"
    "s_waitcnt vmcnt(24)\n"
    "s_barrier\n"
    "s_waitcnt lgkmcnt(15)\n"
    "v_mfma_f32_16x16x32_bf16 v[0:3], a[0:3], a[224:227], v[0:3]\n"
    "ds_read_b64_tr_b16 a[56:57], v158 offset:32768\n"
    "v_mfma_f32_16x16x32_bf16 v[0:3], a[4:7], a[228:231], v[0:3]\n"
    "ds_read_b64_tr_b16 a[58:59], v159 offset:32768\n"
    "v_mfma_f32_16x16x32_bf16 v[4:7], a[0:3], a[232:235], v[4:7]\n"
    "ds_read_b64_tr_b16 a[60:61], v158 offset:40960\n"
    "v_mfma_f32_16x16x32_bf16 v[4:7], a[4:7], a[236:239], v[4:7]\n"
    "ds_read_b64_tr_b16 a[62:63], v159 offset:40960\n"
    "v_mfma_f32_16x16x32_bf16 v[8:11], a[0:3], a[240:243], v[8:11]\n"
    "s_add_u32 m0, s74, 65536\n"
    "s_nop 0\n"
    "buffer_load_dwordx4 v160, s[44:47], s66 offen offset:0  lds\n"
    "v_mfma_f32_16x16x32_bf16 v[8:11], a[4:7], a[244:247], v[8:11]\n"
    "v_mfma_f32_16x16x32_bf16 v[12:15], a[0:3], a[248:251], v[12:15]\n"
    "v_mfma_f32_16x16x32_bf16 v[12:15], a[4:7], a[252:255], v[12:15]\n"
    "v_mfma_f32_16x16x32_bf16 v[16:19], a[8:11], a[224:227], v[16:19]\n"
    "ds_read_b64_tr_b16 a[0:1], v128 offset:0\n"
    "v_mfma_f32_16x16x32_bf16 v[16:19], a[12:15], a[228:231], v[16:19]\n"
    "buffer_load_dwordx4 v161, s[44:47], s66 offen offset:1024  lds\n"
    "v_mfma_f32_16x16x32_bf16 v[20:23], a[8:11], a[232:235], v[20:23]\n"
    "ds_read_b64_tr_b16 a[2:3], v129 offset:0\n"
    "v_mfma_f32_16x16x32_bf16 v[20:23], a[12:15], a[236:239], v[20:23]\n"
    "v_mfma_f32_16x16x32_bf16 v[24:27], a[8:11], a[240:243], v[24:27]\n"
    "ds_read_b64_tr_b16 a[4:5], v128 offset:8192\n"
    "v_mfma_f32_16x16x32_bf16 v[24:27], a[12:15], a[244:247], v[24:27]\n"
    "v_mfma_f32_16x16x32_bf16 v[28:31], a[8:11], a[248:251], v[28:31]\n"
    "ds_read_b64_tr_b16 a[6:7], v129 offset:8192\n"
    "buffer_load_dwordx4 v162, s[44:47], s66 offen offset:2048  lds\n"
    "v_mfma_f32_16x16x32_bf16 v[28:31], a[12:15], a[252:255], v[28:31]\n"
    "v_mfma_f32_16x16x32_bf16 v[32:35], a[16:19], a[224:227], v[32:35]\n"
    "ds_read_b64_tr_b16 a[8:9], v130 offset:0\n"
    "v_mfma_f32_16x16x32_bf16 v[32:35], a[20:23], a[228:231], v[32:35]\n"
    "v_mfma_f32_16x16x32_bf16 v[36:39], a[16:19], a[232:235], v[36:39]\n"
    "ds_read_b64_tr_b16 a[10:11], v131 offset:0\n"
    "v_mfma_f32_16x16x32_bf16 v[36:39], a[20:23], a[236:239], v[36:39]\n"
    "buffer_load_dwordx4 v163, s[44:47], s66 offen offset:3072  lds\n"
    "v_mfma_f32_16x16x32_bf16 v[40:43], a[16:19], a[240:243], v[40:43]\n"
    "ds_read_b64_tr_b16 a[12:13], v130 offset:8192\n"
    "v_mfma_f32_16x16x32_bf16 v[40:43], a[20:23], a[244:247], v[40:43]\n"
    "v_mfma_f32_16x16x32_bf16 v[44:47], a[16:19], a[248:251], v[44:47]\n"
    "ds_read_b64_tr_b16 a[14:15], v131 offset:8192\n"
    "v_mfma_f32_16x16x32_bf16 v[44:47], a[20:23], a[252:255], v[44:47]\n"
    "s_waitcnt lgkmcnt(15)\n"
    "v_mfma_f32_16x16x32_bf16 v[48:51], a[24:27], a[224:227], v[48:51]\n"
    "ds_read_b64_tr_b16 a[16:17], v132 offset:0\n"
    "v_mfma_f32_16x16x32_bf16 v[48:51], a[28:31], a[228:231], v[48:51]\n"
    "s_add_u32 s66, s66, 131072\n"
    "v_mfma_f32_16x16x32_bf16 v[52:55], a[24:27], a[232:235], v[52:55]\n"
    "ds_read_b64_tr_b16 a[18:19], v133 offset:0\n"
    "v_mfma_f32_16x16x32_bf16 v[52:55], a[28:31], a[236:239], v[52:55]\n"
    "v_mfma_f32_16x16x32_bf16 v[56:59], a[24:27], a[240:243], v[56:59]\n"
    "ds_read_b64_tr_b16 a[20:21], v132 offset:8192\n"
    "buffer_load_dwordx4 a[128:131], v168, s[48:51], s67 offen offset:0 sc1\n"
    "v_mfma_f32_16x16x32_bf16 v[56:59], a[28:31], a[244:247], v[56:59]\n"
    "v_mfma_f32_16x16x32_bf16 v[60:63], a[24:27], a[248:251], v[60:63]\n"
    "ds_read_b64_tr_b16 a[22:23], v133 offset:8192\n"
    "v_mfma_f32_16x16x32_bf16 v[60:63], a[28:31], a[252:255], v[60:63]\n"
    "v_mfma_f32_16x16x32_bf16 v[64:67], a[32:35], a[224:227], v[64:67]\n"
    "ds_read_b64_tr_b16 a[24:25], v134 offset:0\n"
    "buffer_load_dwordx4 a[132:135], v168, s[48:51], s67 offen offset:1024 sc1\n"
    "v_mfma_f32_16x16x32_bf16 v[64:67], a[36:39], a[228:231], v[64:67]\n"
    "v_mfma_f32_16x16x32_bf16 v[68:71], a[32:35], a[232:235], v[68:71]\n"
    "ds_read_b64_tr_b16 a[26:27], v135 offset:0\n"
    "v_mfma_f32_16x16x32_bf16 v[68:71], a[36:39], a[236:239], v[68:71]\n"
    "v_mfma_f32_16x16x32_bf16 v[72:75], a[32:35], a[240:243], v[72:75]\n"
    "ds_read_b64_tr_b16 a[28:29], v134 offset:8192\n"
    "buffer_load_dwordx4 a[136:139], v168, s[48:51], s67 offen offset:2048 sc1\n"
    "v_mfma_f32_16x16x32_bf16 v[72:75], a[36:39], a[244:247], v[72:75]\n"
    "v_mfma_f32_16x16x32_bf16 v[76:79], a[32:35], a[248:251], v[76:79]\n"
    "ds_read_b64_tr_b16 a[30:31], v135 offset:8192\n"
    "v_mfma_f32_16x16x32_bf16 v[76:79], a[36:39], a[252:255], v[76:79]\n"
    "v_mfma_f32_16x16x32_bf16 v[80:83], a[40:43], a[224:227], v[80:83]\n"
    "ds_read_b64_tr_b16 a[32:33], v136 offset:0\n"
    "buffer_load_dwordx4 a[140:143], v168, s[48:51], s67 offen offset:3072 sc1\n"
    "v_mfma_f32_16x16x32_bf16 v[80:83], a[44:47], a[228:231], v[80:83]\n"
    "v_mfma_f32_16x16x32_bf16 v[84:87], a[40:43], a[232:235], v[84:87]\n"
    "ds_read_b64_tr_b16 a[34:35], v137 offset:0\n"
    "v_mfma_f32_16x16x32_bf16 v[84:87], a[44:47], a[236:239], v[84:87]\n"
    "v_mfma_f32_16x16x32_bf16 v[88:91], a[40:43], a[240:243], v[88:91]\n"
    "ds_read_b64_tr_b16 a[36:37], v136 offset:8192\n"
    "buffer_load_dwordx4 a[144:147], v169, s[48:51], s67 offen offset:0 sc1\n"
    "v_mfma_f32_16x16x32_bf16 v[88:91], a[44:47], a[244:247], v[88:91]\n"
    "v_mfma_f32_16x16x32_bf16 v[92:95], a[40:43], a[248:251], v[92:95]\n"
    "ds_read_b64_tr_b16 a[38:39], v137 offset:8192\n"
    "v_mfma_f32_16x16x32_bf16 v[92:95], a[44:47], a[252:255], v[92:95]\n"
    "s_waitcnt lgkmcnt(15)\n"
    "v_mfma_f32_16x16x32_bf16 v[96:99], a[48:51], a[224:227], v[96:99]\n"
    "ds_read_b64_tr_b16 a[40:41], v138 offset:0\n"
    "buffer_load_dwordx4 a[148:151], v169, s[48:51], s67 offen offset:1024 sc1\n"
    "v_mfma_f32_16x16x32_bf16 v[96:99], a[52:55], a[228:231], v[96:99]\n"
    "v_mfma_f32_16x16x32_bf16 v[100:103], a[48:51], a[232:235], v[100:103]\n"
    "ds_read_b64_tr_b16 a[42:43], v139 offset:0\n"
    "v_mfma_f32_16x16x32_bf16 v[100:103], a[52:55], a[236:239], v[100:103]\n"
    "v_mfma_f32_16x16x32_bf16 v[104:107], a[48:51], a[240:243], v[104:107]\n"
    "ds_read_b64_tr_b16 a[44:45], v138 offset:8192\n"
    "buffer_load_dwordx4 a[152:155], v169, s[48:51], s67 offen offset:2048 sc1\n"
    "v_mfma_f32_16x16x32_bf16 v[104:107], a[52:55], a[244:247], v[104:107]\n"
    "v_mfma_f32_16x16x32_bf16 v[108:111], a[48:51], a[248:251], v[108:111]\n"
    "ds_read_b64_tr_b16 a[46:47], v139 offset:8192\n"
    "v_mfma_f32_16x16x32_bf16 v[108:111], a[52:55], a[252:255], v[108:111]\n"
    "v_mfma_f32_16x16x32_bf16 v[112:115], a[56:59], a[224:227], v[112:115]\n"
    "ds_read_b64_tr_b16 a[48:49], v140 offset:0\n"
    "buffer_load_dwordx4 a[156:159], v169, s[48:51], s67 offen offset:3072 sc1\n"
    "v_mfma_f32_16x16x32_bf16 v[112:115], a[60:63], a[228:231], v[112:115]\n"
    "ds_read_b64_tr_b16 a[50:51], v141 offset:0\n"
    "v_mfma_f32_16x16x32_bf16 v[116:119], a[56:59], a[232:235], v[116:119]\n"
    "v_mfma_f32_16x16x32_bf16 v[116:119], a[60:63], a[236:239], v[116:119]\n"
    "ds_read_b64_tr_b16 a[52:53], v140 offset:8192\n"
    "v_mfma_f32_16x16x32_bf16 v[120:123], a[56:59], a[240:243], v[120:123]\n"
    "v_mfma_f32_16x16x32_bf16 v[120:123], a[60:63], a[244:247], v[120:123]\n"
    "ds_read_b64_tr_b16 a[54:55], v141 offset:8192\n"
    "s_add_u32 s67, s67, 262144\n"
    "v_mfma_f32_16x16x32_bf16 v[124:127], a[56:59], a[248:251], v[124:127]\n"
    "s_add_u32 s78, s78, 1\n"
    "s_cmp_ge_u32 s78, s69\n"
    "s_cselect_b32 s46, 0, s46\n"
    "s_sub_u32 s70, s78, 2\n"
    "s_cmp_ge_u32 s70, s69\n"
    "s_cselect_b32 s50, 0, s50\n"
    "v_mfma_f32_16x16x32_bf16 v[124:127], a[60:63], a[252:255], v[124:127]\n"
    "s_sub_u32 s68, s68, 1\n"
    "s_cmp_lg_u32 s68, 0\n"
    "s_cbranch_scc1 LD_loop_%=\n"
    "v_lshlrev_b32 v251, 4, v240\n"
    "s_mov_b32 s52, s38\n"
    "s_and_b32 s53, s39, 0xffff\n"
    "s_mov_b32 s54, 8388608\n"
    "s_mov_b32 s55, 0x20000\n"
    "s_and_b32 s70, s63, 1\n"
    "s_lshl_b32 s71, s79, 1\n"
    "s_add_u32 s82, s70, s71\n"
    "s_lshl_b32 s70, s82, 16\n"
    "s_lshl_b32 s71, s60, 14\n"
    "s_add_u32 s66, s70, s71\n"
    "s_lshl_b32 s82, s82, 2\n"
    "s_add_u32 s82, s82, s60\n"
    "s_lshl_b32 s82, s82, 5\n"
    "s_add_u32 s82, s82, 8192\n"
    "s_waitcnt vmcnt(12)\n"
    "s_barrier\n"
    "v_mfma_f32_16x16x32_bf16 v[0:3], a[0:3], a[64:67], v[0:3]\n"
    "ds_read_b64_tr_b16 a[56:57], v142 offset:0\n"
    "v_mfma_f32_16x16x32_bf16 v[0:3], a[4:7], a[68:71], v[0:3]\n"
    "ds_read_b64_tr_b16 a[58:59], v143 offset:0\n"
    "v_mfma_f32_16x16x32_bf16 v[4:7], a[0:3], a[72:75], v[4:7]\n"
    "ds_read_b64_tr_b16 a[60:61], v142 offset:8192\n"
    "v_mfma_f32_16x16x32_bf16 v[4:7], a[4:7], a[76:79], v[4:7]\n"
    "ds_read_b64_tr_b16 a[62:63], v143 offset:8192\n"
    "v_mfma_f32_16x16x32_bf16 v[8:11], a[0:3], a[80:83], v[8:11]\n"
    "v_mfma_f32_16x16x32_bf16 v[8:11], a[4:7], a[84:87], v[8:11]\n"
    "v_mfma_f32_16x16x32_bf16 v[12:15], a[0:3], a[88:91], v[12:15]\n"
    "v_mfma_f32_16x16x32_bf16 v[12:15], a[4:7], a[92:95], v[12:15]\n"
    "s_waitcnt lgkmcnt(15)\n"
    "v_mfma_f32_16x16x32_bf16 v[16:19], a[8:11], a[64:67], v[16:19]\n"
    "ds_read_b64_tr_b16 a[0:1], v128 offset:16384\n"
    "v_mfma_f32_16x16x32_bf16 v[16:19], a[12:15], a[68:71], v[16:19]\n"
    "v_mfma_f32_16x16x32_bf16 v[20:23], a[8:11], a[72:75], v[20:23]\n"
    "ds_read_b64_tr_b16 a[2:3], v129 offset:16384\n"
    "v_mfma_f32_16x16x32_bf16 v[20:23], a[12:15], a[76:79], v[20:23]\n"
    "v_mfma_f32_16x16x32_bf16 v[24:27], a[8:11], a[80:83], v[24:27]\n"
    "ds_read_b64_tr_b16 a[4:5], v128 offset:24576\n"
    "v_mfma_f32_16x16x32_bf16 v[24:27], a[12:15], a[84:87], v[24:27]\n"
    "v_mfma_f32_16x16x32_bf16 v[28:31], a[8:11], a[88:91], v[28:31]\n"
    "ds_read_b64_tr_b16 a[6:7], v129 offset:24576\n"
    "v_mfma_f32_16x16x32_bf16 v[28:31], a[12:15], a[92:95], v[28:31]\n"
    "v_mfma_f32_16x16x32_bf16 v[32:35], a[16:19], a[64:67], v[32:35]\n"
    "ds_read_b64_tr_b16 a[8:9], v130 offset:16384\n"
    "v_mfma_f32_16x16x32_bf16 v[32:35], a[20:23], a[68:71], v[32:35]\n"
    "v_mfma_f32_16x16x32_bf16 v[36:39], a[16:19], a[72:75], v[36:39]\n"
    "ds_read_b64_tr_b16 a[10:11], v131 offset:16384\n"
    "v_mfma_f32_16x16x32_bf16 v[36:39], a[20:23], a[76:79], v[36:39]\n"
    "v_mfma_f32_16x16x32_bf16 v[40:43], a[16:19], a[80:83], v[40:43]\n"
    "ds_read_b64_tr_b16 a[12:13], v130 offset:24576\n"
    "v_mfma_f32_16x16x32_bf16 v[40:43], a[20:23], a[84:87], v[40:43]\n"
    "v_mfma_f32_16x16x32_bf16 v[44:47], a[16:19], a[88:91], v[44:47]\n"
    "ds_read_b64_tr_b16 a[14:15], v131 offset:24576\n"
    "v_mfma_f32_16x16x32_bf16 v[44:47], a[20:23], a[92:95], v[44:47]\n"
    "v_mfma_f32_16x16x32_bf16 v[48:51], a[24:27], a[64:67], v[48:51]\n"
    "ds_read_b64_tr_b16 a[16:17], v132 offset:16384\n"
    "v_mfma_f32_16x16x32_bf16 v[48:51], a[28:31], a[68:71], v[48:51]\n"
    "v_mfma_f32_16x16x32_bf16 v[52:55], a[24:27], a[72:75], v[52:55]\n"
    "ds_read_b64_tr_b16 a[18:19], v133 offset:16384\n"
    "v_mfma_f32_16x16x32_bf16 v[52:55], a[28:31], a[76:79], v[52:55]\n"
    "v_mfma_f32_16x16x32_bf16 v[56:59], a[24:27], a[80:83], v[56:59]\n"
    "ds_read_b64_tr_b16 a[20:21], v132 offset:24576\n"
    "v_mfma_f32_16x16x32_bf16 v[56:59], a[28:31], a[84:87], v[56:59]\n"
    "v_mfma_f32_16x16x32_bf16 v[60:63], a[24:27], a[88:91], v[60:63]\n"
    "ds_read_b64_tr_b16 a[22:23], v133 offset:24576\n"
    "v_mfma_f32_16x16x32_bf16 v[60:63], a[28:31], a[92:95], v[60:63]\n"
    "ds_read_b64_tr_b16 a[24:25], v134 offset:16384\n"
    "ds_read_b64_tr_b16 a[26:27], v135 offset:16384\n"
    "ds_read_b64_tr_b16 a[28:29], v134 offset:24576\n"
    "ds_read_b64_tr_b16 a[30:31], v135 offset:24576\n"
    "s_waitcnt lgkmcnt(8)\n"
    "v_mfma_f32_16x16x32_bf16 v[0:3], a[0:3], a[96:99], v[0:3]\n"
    "v_mfma_f32_16x16x32_bf16 v[0:3], a[4:7], a[100:103], v[0:3]\n"
    "v_mfma_f32_16x16x32_bf16 v[4:7], a[0:3], a[104:107], v[4:7]\n"
    "v_mfma_f32_16x16x32_bf16 v[4:7], a[4:7], a[108:111], v[4:7]\n"
    "v_mfma_f32_16x16x32_bf16 v[8:11], a[0:3], a[112:115], v[8:11]\n"
    "v_mfma_f32_16x16x32_bf16 v[8:11], a[4:7], a[116:119], v[8:11]\n"
    "v_mfma_f32_16x16x32_bf16 v[12:15], a[0:3], a[120:123], v[12:15]\n"
    "v_mfma_f32_16x16x32_bf16 v[12:15], a[4:7], a[124:127], v[12:15]\n"
    "v_mfma_f32_16x16x32_bf16 v[16:19], a[8:11], a[96:99], v[16:19]\n"
    "v_mfma_f32_16x16x32_bf16 v[16:19], a[12:15], a[100:103], v[16:19]\n"
    "v_mfma_f32_16x16x32_bf16 v[20:23], a[8:11], a[104:107], v[20:23]\n"
    "v_mfma_f32_16x16x32_bf16 v[20:23], a[12:15], a[108:111], v[20:23]\n"
    "v_mfma_f32_16x16x32_bf16 v[24:27], a[8:11], a[112:115], v[24:27]\n"
    "v_mfma_f32_16x16x32_bf16 v[24:27], a[12:15], a[116:119], v[24:27]\n"
    "v_mfma_f32_16x16x32_bf16 v[28:31], a[8:11], a[120:123], v[28:31]\n"
    "v_mfma_f32_16x16x32_bf16 v[28:31], a[12:15], a[124:127], v[28:31]\n"
    "s_waitcnt lgkmcnt(0)\n"
    "v_mfma_f32_16x16x32_bf16 v[32:35], a[16:19], a[96:99], v[32:35]\n"
    "v_mfma_f32_16x16x32_bf16 v[32:35], a[20:23], a[100:103], v[32:35]\n"
    "v_mfma_f32_16x16x32_bf16 v[36:39], a[16:19], a[104:107], v[36:39]\n"
    "v_mfma_f32_16x16x32_bf16 v[36:39], a[20:23], a[108:111], v[36:39]\n"
    "v_mfma_f32_16x16x32_bf16 v[40:43], a[16:19], a[112:115], v[40:43]\n"
    "v_mfma_f32_16x16x32_bf16 v[40:43], a[20:23], a[116:119], v[40:43]\n"
    "v_mfma_f32_16x16x32_bf16 v[44:47], a[16:19], a[120:123], v[44:47]\n"
    "v_mfma_f32_16x16x32_bf16 v[44:47], a[20:23], a[124:127], v[44:47]\n"
    "v_mfma_f32_16x16x32_bf16 v[48:51], a[24:27], a[96:99], v[48:51]\n"
    "v_mfma_f32_16x16x32_bf16 v[48:51], a[28:31], a[100:103], v[48:51]\n"
    "v_mfma_f32_16x16x32_bf16 v[52:55], a[24:27], a[104:107], v[52:55]\n"
    "v_mfma_f32_16x16x32_bf16 v[52:55], a[28:31], a[108:111], v[52:55]\n"
    "v_mfma_f32_16x16x32_bf16 v[56:59], a[24:27], a[112:115], v[56:59]\n"
    "v_mfma_f32_16x16x32_bf16 v[56:59], a[28:31], a[116:119], v[56:59]\n"
    "v_mfma_f32_16x16x32_bf16 v[60:63], a[24:27], a[120:123], v[60:63]\n"
    "v_mfma_f32_16x16x32_bf16 v[60:63], a[28:31], a[124:127], v[60:63]\n"
    "v_mfma_f32_16x16x32_bf16 v[64:67], a[32:35], a[64:67], v[64:67]\n"
    "v_mfma_f32_16x16x32_bf16 v[64:67], a[36:39], a[68:71], v[64:67]\n"
    "v_mfma_f32_16x16x32_bf16 v[68:71], a[32:35], a[72:75], v[68:71]\n"
    "v_mfma_f32_16x16x32_bf16 v[68:71], a[36:39], a[76:79], v[68:71]\n"
    "v_mfma_f32_16x16x32_bf16 v[72:75], a[32:35], a[80:83], v[72:75]\n"
    "v_mfma_f32_16x16x32_bf16 v[72:75], a[36:39], a[84:87], v[72:75]\n"
    "v_mfma_f32_16x16x32_bf16 v[76:79], a[32:35], a[88:91], v[76:79]\n"
    "v_mfma_f32_16x16x32_bf16 v[76:79], a[36:39], a[92:95], v[76:79]\n"
    "v_mfma_f32_16x16x32_bf16 v[80:83], a[40:43], a[64:67], v[80:83]\n"
    "v_cvt_pk_bf16_f32 v160, v0, v1\n"
    "v_cvt_pk_bf16_f32 v161, v2, v3\n"
    "v_cvt_pk_bf16_f32 v162, v16, v17\n"
    "v_cvt_pk_bf16_f32 v163, v18, v19\n"
    "s_add_u32 s71, s66, 0\n"
    "buffer_store_dwordx4 v[160:163], v251, s[52:55], s71 offen offset:0 sc1\n"
    "ds_read_b64_tr_b16 a[32:33], v136 offset:16384\n"
    "v_mfma_f32_16x16x32_bf16 v[80:83], a[44:47], a[68:71], v[80:83]\n"
    "v_mfma_f32_16x16x32_bf16 v[84:87], a[40:43], a[72:75], v[84:87]\n"
    "v_cvt_pk_bf16_f32 v164, v32, v33\n"
    "v_cvt_pk_bf16_f32 v165, v34, v35\n"
    "v_cvt_pk_bf16_f32 v166, v48, v49\n"
    "v_cvt_pk_bf16_f32 v167, v50, v51\n"
    "s_add_u32 s71, s66, 0\n"
    "buffer_store_dwordx4 v[164:167], v251, s[52:55], s71 offen offset:1024 sc1\n"
    "ds_read_b64_tr_b16 a[34:35], v137 offset:16384\n"
    "v_mfma_f32_16x16x32_bf16 v[84:87], a[44:47], a[76:79], v[84:87]\n"
    "v_mfma_f32_16x16x32_bf16 v[88:91], a[40:43], a[80:83], v[88:91]\n"
    "ds_read_b64_tr_b16 a[36:37], v136 offset:24576\n"
    "v_mfma_f32_16x16x32_bf16 v[88:91], a[44:47], a[84:87], v[88:91]\n"
    "v_cvt_pk_bf16_f32 v168, v4, v5\n"
    "v_cvt_pk_bf16_f32 v169, v6, v7\n"
    "v_cvt_pk_bf16_f32 v170, v20, v21\n"
    "v_cvt_pk_bf16_f32 v171, v22, v23\n"
    "s_add_u32 s71, s66, 0\n"
    "buffer_store_dwordx4 v[168:171], v251, s[52:55], s71 offen offset:2048 sc1\n"
    "v_mfma_f32_16x16x32_bf16 v[92:95], a[40:43], a[88:91], v[92:95]\n"
    "ds_read_b64_tr_b16 a[38:39], v137 offset:24576\n"
    "v_mfma_f32_16x16x32_bf16 v[92:95], a[44:47], a[92:95], v[92:95]\n"
    "v_mfma_f32_16x16x32_bf16 v[96:99], a[48:51], a[64:67], v[96:99]\n"
    "v_cvt_pk_bf16_f32 v172, v36, v37\n"
    "v_cvt_pk_bf16_f32 v173, v38, v39\n"
    "v_cvt_pk_bf16_f32 v174, v52, v53\n"
    "v_cvt_pk_bf16_f32 v175, v54, v55\n"
    "s_add_u32 s71, s66, 0\n"
    "buffer_store_dwordx4 v[172:175], v251, s[52:55], s71 offen offset:3072 sc1\n"
    "ds_read_b64_tr_b16 a[40:41], v138 offset:16384\n"
    "v_mfma_f32_16x16x32_bf16 v[96:99], a[52:55], a[68:71], v[96:99]\n"
    "v_mfma_f32_16x16x32_bf16 v[100:103], a[48:51], a[72:75], v[100:103]\n"
    "ds_read_b64_tr_b16 a[42:43], v139 offset:16384\n"
    "v_mfma_f32_16x16x32_bf16 v[100:103], a[52:55], a[76:79], v[100:103]\n"
    "v_cvt_pk_bf16_f32 v160, v8, v9\n"
    "v_cvt_pk_bf16_f32 v161, v10, v11\n"
    "v_cvt_pk_bf16_f32 v162, v24, v25\n"
    "v_cvt_pk_bf16_f32 v163, v26, v27\n"
    "s_add_u32 s71, s66, 4096\n"
    "buffer_store_dwordx4 v[160:163], v251, s[52:55], s71 offen offset:0 sc1\n"
    "v_mfma_f32_16x16x32_bf16 v[104:107], a[48:51], a[80:83], v[104:107]\n"
    "ds_read_b64_tr_b16 a[44:45], v138 offset:24576\n"
    "v_mfma_f32_16x16x32_bf16 v[104:107], a[52:55], a[84:87], v[104:107]\n"
    "v_mfma_f32_16x16x32_bf16 v[108:111], a[48:51], a[88:91], v[108:111]\n"
    "v_cvt_pk_bf16_f32 v164, v40, v41\n"
    "v_cvt_pk_bf16_f32 v165, v42, v43\n"
    "v_cvt_pk_bf16_f32 v166, v56, v57\n"
    "v_cvt_pk_bf16_f32 v167, v58, v59\n"
    "s_add_u32 s71, s66, 4096\n"
    "buffer_store_dwordx4 v[164:167], v251, s[52:55], s71 offen offset:1024 sc1\n"
    "ds_read_b64_tr_b16 a[46:47], v139 offset:24576\n"
    "v_mfma_f32_16x16x32_bf16 v[108:111], a[52:55], a[92:95], v[108:111]\n"
    "v_mfma_f32_16x16x32_bf16 v[112:115], a[56:59], a[64:67], v[112:115]\n"
    "ds_read_b64_tr_b16 a[48:49], v140 offset:16384\n"
    "v_mfma_f32_16x16x32_bf16 v[112:115], a[60:63], a[68:71], v[112:115]\n"
    "v_cvt_pk_bf16_f32 v168, v12, v13\n"
    "v_cvt_pk_bf16_f32 v169, v14, v15\n"
    "v_cvt_pk_bf16_f32 v170, v28, v29\n"
    "v_cvt_pk_bf16_f32 v171, v30, v31\n"
    "s_add_u32 s71, s66, 4096\n"
    "buffer_store_dwordx4 v[168:171], v251, s[52:55], s71 offen offset:2048 sc1\n"
    "v_mfma_f32_16x16x32_bf16 v[116:119], a[56:59], a[72:75], v[116:119]\n"
    "ds_read_b64_tr_b16 a[50:51], v141 offset:16384\n"
    "v_mfma_f32_16x16x32_bf16 v[116:119], a[60:63], a[76:79], v[116:119]\n"
    "v_mfma_f32_16x16x32_bf16 v[120:123], a[56:59], a[80:83], v[120:123]\n"
    "v_cvt_pk_bf16_f32 v172, v44, v45\n"
    "v_cvt_pk_bf16_f32 v173, v46, v47\n"
    "v_cvt_pk_bf16_f32 v174, v60, v61\n"
    "v_cvt_pk_bf16_f32 v175, v62, v63\n"
    "s_add_u32 s71, s66, 4096\n"
    "buffer_store_dwordx4 v[172:175], v251, s[52:55], s71 offen offset:3072 sc1\n"
    "ds_read_b64_tr_b16 a[52:53], v140 offset:24576\n"
    "v_mfma_f32_16x16x32_bf16 v[120:123], a[60:63], a[84:87], v[120:123]\n"
    "v_mfma_f32_16x16x32_bf16 v[124:127], a[56:59], a[88:91], v[124:127]\n"
    "ds_read_b64_tr_b16 a[54:55], v141 offset:24576\n"
    "v_mfma_f32_16x16x32_bf16 v[124:127], a[60:63], a[92:95], v[124:127]\n"
    "ds_read_b64_tr_b16 a[56:57], v142 offset:16384\n"
    "ds_read_b64_tr_b16 a[58:59], v143 offset:16384\n"
    "ds_read_b64_tr_b16 a[60:61], v142 offset:24576\n"
    "ds_read_b64_tr_b16 a[62:63], v143 offset:24576\n"
    "s_waitcnt lgkmcnt(8)\n"
    "v_mfma_f32_16x16x32_bf16 v[64:67], a[32:35], a[96:99], v[64:67]\n"
    "v_mfma_f32_16x16x32_bf16 v[64:67], a[36:39], a[100:103], v[64:67]\n"
    "v_mfma_f32_16x16x32_bf16 v[68:71], a[32:35], a[104:107], v[68:71]\n"
    "v_mfma_f32_16x16x32_bf16 v[68:71], a[36:39], a[108:111], v[68:71]\n"
    "v_mfma_f32_16x16x32_bf16 v[72:75], a[32:35], a[112:115], v[72:75]\n"
    "v_mfma_f32_16x16x32_bf16 v[72:75], a[36:39], a[116:119], v[72:75]\n"
    "v_mfma_f32_16x16x32_bf16 v[76:79], a[32:35], a[120:123], v[76:79]\n"
    "v_mfma_f32_16x16x32_bf16 v[76:79], a[36:39], a[124:127], v[76:79]\n"
    "v_mfma_f32_16x16x32_bf16 v[80:83], a[40:43], a[96:99], v[80:83]\n"
    "v_mfma_f32_16x16x32_bf16 v[80:83], a[44:47], a[100:103], v[80:83]\n"
    "v_mfma_f32_16x16x32_bf16 v[84:87], a[40:43], a[104:107], v[84:87]\n"
    "v_mfma_f32_16x16x32_bf16 v[84:87], a[44:47], a[108:111], v[84:87]\n"
    "v_mfma_f32_16x16x32_bf16 v[88:91], a[40:43], a[112:115], v[88:91]\n"
    "v_mfma_f32_16x16x32_bf16 v[88:91], a[44:47], a[116:119], v[88:91]\n"
    "v_mfma_f32_16x16x32_bf16 v[92:95], a[40:43], a[120:123], v[92:95]\n"
    "v_mfma_f32_16x16x32_bf16 v[92:95], a[44:47], a[124:127], v[92:95]\n"
    "s_waitcnt lgkmcnt(2)\n"
    "v_mfma_f32_16x16x32_bf16 v[96:99], a[48:51], a[96:99], v[96:99]\n"
    "v_mfma_f32_16x16x32_bf16 v[96:99], a[52:55], a[100:103], v[96:99]\n"
    "v_mfma_f32_16x16x32_bf16 v[100:103], a[48:51], a[104:107], v[100:103]\n"
    "v_mfma_f32_16x16x32_bf16 v[100:103], a[52:55], a[108:111], v[100:103]\n"
    "v_mfma_f32_16x16x32_bf16 v[104:107], a[48:51], a[112:115], v[104:107]\n"
    "v_mfma_f32_16x16x32_bf16 v[104:107], a[52:55], a[116:119], v[104:107]\n"
    "v_mfma_f32_16x16x32_bf16 v[108:111], a[48:51], a[120:123], v[108:111]\n"
    "v_mfma_f32_16x16x32_bf16 v[108:111], a[52:55], a[124:127], v[108:111]\n"
    "v_mfma_f32_16x16x32_bf16 v[112:115], a[56:59], a[96:99], v[112:115]\n"
    "v_cvt_pk_bf16_f32 v160, v64, v65\n"
    "v_cvt_pk_bf16_f32 v161, v66, v67\n"
    "v_cvt_pk_bf16_f32 v162, v80, v81\n"
    "v_cvt_pk_bf16_f32 v163, v82, v83\n"
    "s_add_u32 s71, s66, 8192\n"
    "buffer_store_dwordx4 v[160:163], v251, s[52:55], s71 offen offset:0 sc1\n"
    "s_waitcnt lgkmcnt(0)\n"
    "v_mfma_f32_16x16x32_bf16 v[112:115], a[60:63], a[100:103], v[112:115]\n"
    "v_mfma_f32_16x16x32_bf16 v[116:119], a[56:59], a[104:107], v[116:119]\n"
    "v_cvt_pk_bf16_f32 v168, v68, v69\n"
    "v_cvt_pk_bf16_f32 v169, v70, v71\n"
    "v_cvt_pk_bf16_f32 v170, v84, v85\n"
    "v_cvt_pk_bf16_f32 v171, v86, v87\n"
    "s_add_u32 s71, s66, 8192\n"
    "buffer_store_dwordx4 v[168:171], v251, s[52:55], s71 offen offset:2048 sc1\n"
    "v_mfma_f32_16x16x32_bf16 v[116:119], a[60:63], a[108:111], v[116:119]\n"
    "v_mfma_f32_16x16x32_bf16 v[120:123], a[56:59], a[112:115], v[120:123]\n"
    "v_cvt_pk_bf16_f32 v160, v72, v73\n"
    "v_cvt_pk_bf16_f32 v161, v74, v75\n"
    "v_cvt_pk_bf16_f32 v162, v88, v89\n"
    "v_cvt_pk_bf16_f32 v163, v90, v91\n"
    "s_add_u32 s71, s66, 12288\n"
    "buffer_store_dwordx4 v[160:163], v251, s[52:55], s71 offen offset:0 sc1\n"
    "v_mfma_f32_16x16x32_bf16 v[120:123], a[60:63], a[116:119], v[120:123]\n"
    "v_mfma_f32_16x16x32_bf16 v[124:127], a[56:59], a[120:123], v[124:127]\n"
    "v_cvt_pk_bf16_f32 v168, v76, v77\n"
    "v_cvt_pk_bf16_f32 v169, v78, v79\n"
    "v_cvt_pk_bf16_f32 v170, v92, v93\n"
    "v_cvt_pk_bf16_f32 v171, v94, v95\n"
    "s_add_u32 s71, s66, 12288\n"
    "buffer_store_dwordx4 v[168:171], v251, s[52:55], s71 offen offset:2048 sc1\n"
    "v_mfma_f32_16x16x32_bf16 v[124:127], a[60:63], a[124:127], v[124:127]\n"
    "s_nop 7\n"
    "v_cvt_pk_bf16_f32 v164, v96, v97\n"
    "v_cvt_pk_bf16_f32 v165, v98, v99\n"
    "v_cvt_pk_bf16_f32 v166, v112, v113\n"
    "v_cvt_pk_bf16_f32 v167, v114, v115\n"
    "s_add_u32 s71, s66, 8192\n"
    "buffer_store_dwordx4 v[164:167], v251, s[52:55], s71 offen offset:1024 sc1\n"
    "v_cvt_pk_bf16_f32 v172, v100, v101\n"
    "v_cvt_pk_bf16_f32 v173, v102, v103\n"
    "v_cvt_pk_bf16_f32 v174, v116, v117\n"
    "v_cvt_pk_bf16_f32 v175, v118, v119\n"
    "s_add_u32 s71, s66, 8192\n"
    "buffer_store_dwordx4 v[172:175], v251, s[52:55], s71 offen offset:3072 sc1\n"
    "v_cvt_pk_bf16_f32 v164, v104, v105\n"
    "v_cvt_pk_bf16_f32 v165, v106, v107\n"
    "v_cvt_pk_bf16_f32 v166, v120, v121\n"
    "v_cvt_pk_bf16_f32 v167, v122, v123\n"
    "s_add_u32 s71, s66, 12288\n"
    "buffer_store_dwordx4 v[164:167], v251, s[52:55], s71 offen offset:1024 sc1\n"
    "v_cvt_pk_bf16_f32 v172, v108, v109\n"
    "v_cvt_pk_bf16_f32 v173, v110, v111\n"
    "v_cvt_pk_bf16_f32 v174, v124, v125\n"
    "v_cvt_pk_bf16_f32 v175, v126, v127\n"
    "s_add_u32 s71, s66, 12288\n"
    "buffer_store_dwordx4 v[172:175], v251, s[52:55], s71 offen offset:3072 sc1\n"
    "s_waitcnt vmcnt(8)\n"
    "v_mov_b32 v249, s82\n"
    "v_mov_b32 v252, s4\n"
    "v_mov_b32 v253, s5\n"
    "s_mov_b64 s[10:11], exec\n"
    "s_mov_b64 exec, 1\n"
    "global_store_dwordx2 v249, v[252:253], s[6:7] sc1\n"
    "s_mov_b64 exec, s[10:11]\n"
    "s_waitcnt vmcnt(1)\n"
    "s_add_u32 s82, s82, 16\n"
    "v_mov_b32 v249, s82\n"
    "v_mov_b32 v252, s4\n"
    "v_mov_b32 v253, s5\n"
    "s_mov_b64 s[10:11], exec\n"
    "s_mov_b64 exec, 1\n"
    "global_store_dwordx2 v249, v[252:253], s[6:7] sc1\n"
    "s_mov_b64 exec, s[10:11]\n"
    "s_cmp_lg_u32 s8, 1\n"
    "s_cbranch_scc1 LNU_%=_27\n"
    "s_cmp_lg_u32 s60, 0\n"
    "s_cbranch_scc1 LNU_%=_27\n"
    "v_lshlrev_b32 v249, 3, v240\n"
    "v_add_u32 v249, 4096, v249\n"
    "s_mov_b64 s[10:11], exec\n"
    "s_mov_b64 exec, -1\n"
    "s_mov_b32 s3, 0\n"
    "global_load_dwordx2 v[252:253], v249, s[6:7] sc1\n"
    "LWP_%=_28:\n"
    "global_load_dwordx2 v[206:207], v249, s[6:7] sc1\n"
    "s_waitcnt vmcnt(1)\n"
    "v_cmp_ne_u64 vcc, s[4:5], v[252:253]\n"
    "s_nop 4\n"
    "s_cbranch_vccz LWD_%=_28\n"
    "global_load_dwordx2 v[252:253], v249, s[6:7] sc1\n"
    "s_waitcnt vmcnt(1)\n"
    "v_cmp_ne_u64 vcc, s[4:5], v[206:207]\n"
    "s_nop 4\n"
    "s_cbranch_vccz LWD_%=_28\n"
    "s_add_u32 s3, s3, 1\n"
    "s_cmp_lt_u32 s3, 0x10000\n"
    "s_cbranch_scc1 LWP_%=_28\n"
    "LWD_%=_28:\n"
    "s_mov_b64 exec, s[10:11]\n"
    "v_lshlrev_b32 v249, 3, v240\n"
    "v_add_u32 v249, 4608, v249\n"
    "s_mov_b64 s[10:11], exec\n"
    "s_mov_b64 exec, -1\n"
    "s_mov_b32 s3, 0\n"
    "global_load_dwordx2 v[252:253], v249, s[6:7] sc1\n"
    "LWP_%=_29:\n"
    "global_load_dwordx2 v[206:207], v249, s[6:7] sc1\n"
    "s_waitcnt vmcnt(1)\n"
    "v_cmp_ne_u64 vcc, s[4:5], v[252:253]\n"
    "s_nop 4\n"
    "s_cbranch_vccz LWD_%=_29\n"
    "global_load_dwordx2 v[252:253], v249, s[6:7] sc1\n"
    "s_waitcnt vmcnt(1)\n"
    "v_cmp_ne_u64 vcc, s[4:5], v[206:207]\n"
    "s_nop 4\n"
    "s_cbranch_vccz LWD_%=_29\n"
    "s_add_u32 s3, s3, 1\n"
    "s_cmp_lt_u32 s3, 0x10000\n"
    "s_cbranch_scc1 LWP_%=_29\n"
    "LWD_%=_29:\n"
    "s_mov_b64 exec, s[10:11]\n"
    "v_lshlrev_b32 v249, 3, v240\n"
    "v_add_u32 v249, 5120, v249\n"
    "s_mov_b64 s[10:11], exec\n"
    "s_mov_b64 exec, -1\n"
    "s_mov_b32 s3, 0\n"
    "global_load_dwordx2 v[252:253], v249, s[6:7] sc1\n"
    "LWP_%=_30:\n"
    "global_load_dwordx2 v[206:207], v249, s[6:7] sc1\n"
    "s_waitcnt vmcnt(1)\n"
    "v_cmp_ne_u64 vcc, s[4:5], v[252:253]\n"
    "s_nop 4\n"
    "s_cbranch_vccz LWD_%=_30\n"
    "global_load_dwordx2 v[252:253], v249, s[6:7] sc1\n"
    "s_waitcnt vmcnt(1)\n"
    "v_cmp_ne_u64 vcc, s[4:5], v[206:207]\n"
    "s_nop 4\n"
    "s_cbranch_vccz LWD_%=_30\n"
    "s_add_u32 s3, s3, 1\n"
    "s_cmp_lt_u32 s3, 0x10000\n"
    "s_cbranch_scc1 LWP_%=_30\n"
    "LWD_%=_30:\n"
    "s_mov_b64 exec, s[10:11]\n"
    "v_lshlrev_b32 v249, 3, v240\n"
    "v_add_u32 v249, 5632, v249\n"
    "s_mov_b64 s[10:11], exec\n"
    "s_mov_b64 exec, -1\n"
    "s_mov_b32 s3, 0\n"
    "global_load_dwordx2 v[252:253], v249, s[6:7] sc1\n"
    "LWP_%=_31:\n"
    "global_load_dwordx2 v[206:207], v249, s[6:7] sc1\n"
    "s_waitcnt vmcnt(1)\n"
    "v_cmp_ne_u64 vcc, s[4:5], v[252:253]\n"
    "s_nop 4\n"
    "s_cbranch_vccz LWD_%=_31\n"
    "global_load_dwordx2 v[252:253], v249, s[6:7] sc1\n"
    "s_waitcnt vmcnt(1)\n"
    "v_cmp_ne_u64 vcc, s[4:5], v[206:207]\n"
    "s_nop 4\n"
    "s_cbranch_vccz LWD_%=_31\n"
    "s_add_u32 s3, s3, 1\n"
    "s_cmp_lt_u32 s3, 0x10000\n"
    "s_cbranch_scc1 LWP_%=_31\n"
    "LWD_%=_31:\n"
    "s_mov_b64 exec, s[10:11]\n"
    "s_waitcnt vmcnt(0)\n"
    "s_mov_b32 s99, 40960\n"
    "v_mov_b32 v249, s99\n"
    "v_mov_b32 v252, s4\n"
    "v_mov_b32 v253, s5\n"
    "s_mov_b64 s[10:11], exec\n"
    "s_mov_b64 exec, 1\n"
    "global_store_dwordx2 v249, v[252:253], s[6:7] sc1\n"
    "s_mov_b64 exec, s[10:11]\n"
    "LNU_%=_27:\n"
    "LP2E_%=:\n"
    "LCODE_END_%=:\n"
   : "+v"(tid) : "s"(__builtin_amdgcn_kernarg_segment_ptr()), "s"(blockIdx.x), "s"((unsigned)(size_t)smem) : "v1", "v2", "v3", "v4", "v5", "v6", "v7", "v8", "v9", "v10", "v11", "v12", "v13", "v14", "v15", "v16", "v17", "v18", "v19", "v20", "v21", "v22", "v23", "v24", "v25", "v26", "v27", "v28", "v29", "v30", "v31", "v32", "v33", "v34", "v35", "v36", "v37", "v38", "v39", "v40", "v41", "v42", "v43", "v44", "v45", "v46", "v47", "v48", "v49", "v50", "v51", "v52", "v53", "v54", "v55", "v56", "v57", "v58", "v59", "v60", "v61", "v62", "v63", "v64", "v65", "v66", "v67", "v68", "v69", "v70", "v71", "v72", "v73", "v74", "v75", "v76", "v77", "v78", "v79", "v80", "v81", "v82", "v83", "v84", "v85", "v86", "v87", "v88", "v89", "v90", "v91", "v92", "v93", "v94", "v95", "v96", "v97", "v98", "v99", "v100", "v101", "v102", "v103", "v104", "v105", "v106", "v107", "v108", "v109", "v110", "v111", "v112", "v113", "v114", "v115", "v116", "v117", "v118", "v119", "v120", "v121", "v122", "v123", "v124", "v125", "v126", "v127", "v128", "v129", "v130", "v131", "v132", "v133", "v134", "v135", "v136", "v137", "v138", "v139", "v140", "v141", "v142", "v143", "v144", "v145", "v146", "v147", "v148", "v149", "v150", "v151", "v152", "v153", "v154", "v155", "v156", "v157", "v158", "v159", "v160", "v161", "v162", "v163", "v164", "v165", "v166", "v167", "v168", "v169", "v170", "v171", "v172", "v173", "v174", "v175", "v176", "v177", "v178", "v179", "v180", "v181", "v182", "v183", "v184", "v185", "v186", "v187", "v188", "v189", "v190", "v191", "v192", "v193", "v194", "v195", "v196", "v197", "v198", "v199", "v200", "v201", "v202", "v203", "v204", "v205", "v206", "v207", "v208", "v209", "v210", "v211", "v212", "v213", "v214", "v215", "v216", "v217", "v218", "v219", "v220", "v221", "v222", "v223", "v224", "v225", "v226", "v227", "v228", "v229", "v230", "v231", "v232", "v233", "v234", "v235", "v236", "v237", "v238", "v239", "v240", "v241", "v242", "v243", "v244", "v245", "v246", "v247", "v248", "v249", "v250", "v251", "v252", "v253", "v254", "v255", "a0", "a1", "a2", "a3", "a4", "a5", "a6", "a7", "a8", "a9", "a10", "a11", "a12", "a13", "a14", "a15", "a16", "a17", "a18", "a19", "a20", "a21", "a22", "a23", "a24", "a25", "a26", "a27", "a28", "a29", "a30", "a31", "a32", "a33", "a34", "a35", "a36", "a37", "a38", "a39", "a40", "a41", "a42", "a43", "a44", "a45", "a46", "a47", "a48", "a49", "a50", "a51", "a52", "a53", "a54", "a55", "a56", "a57", "a58", "a59", "a60", "a61", "a62", "a63", "a64", "a65", "a66", "a67", "a68", "a69", "a70", "a71", "a72", "a73", "a74", "a75", "a76", "a77", "a78", "a79", "a80", "a81", "a82", "a83", "a84", "a85", "a86", "a87", "a88", "a89", "a90", "a91", "a92", "a93", "a94", "a95", "a96", "a97", "a98", "a99", "a100", "a101", "a102", "a103", "a104", "a105", "a106", "a107", "a108", "a109", "a110", "a111", "a112", "a113", "a114", "a115", "a116", "a117", "a118", "a119", "a120", "a121", "a122", "a123", "a124", "a125", "a126", "a127", "a128", "a129", "a130", "a131", "a132", "a133", "a134", "a135", "a136", "a137", "a138", "a139", "a140", "a141", "a142", "a143", "a144", "a145", "a146", "a147", "a148", "a149", "a150", "a151", "a152", "a153", "a154", "a155", "a156", "a157", "a158", "a159", "a160", "a161", "a162", "a163", "a164", "a165", "a166", "a167", "a168", "a169", "a170", "a171", "a172", "a173", "a174", "a175", "a176", "a177", "a178", "a179", "a180", "a181", "a182", "a183", "a184", "a185", "a186", "a187", "a188", "a189", "a190", "a191", "a192", "a193", "a194", "a195", "a196", "a197", "a198", "a199", "a200", "a201", "a202", "a203", "a204", "a205", "a206", "a207", "a208", "a209", "a210", "a211", "a212", "a213", "a214", "a215", "a216", "a217", "a218", "a219", "a220", "a221", "a222", "a223", "a224", "a225", "a226", "a227", "a228", "a229", "a230", "a231", "a232", "a233", "a234", "a235", "a236", "a237", "a238", "a239", "a240", "a241", "a242", "a243", "a244", "a245", "a246", "a247", "a248", "a249", "a250", "a251", "a252", "a253", "a254", "a255", "s3", "s4", "s5", "s6", "s7", "s8", "s9", "s10", "s11", "s12", "s13", "s14", "s15", "s16", "s17", "s18", "s19", "s20", "s21", "s22", "s23", "s24", "s25", "s26", "s27", "s28", "s29", "s30", "s31", "s34", "s35", "s36", "s37", "s38", "s39", "s40", "s41", "s42", "s43", "s44", "s45", "s46", "s47", "s48", "s49", "s50", "s51", "s52", "s53", "s54", "s55", "s56", "s57", "s58", "s59", "s60", "s61", "s62", "s63", "s64", "s65", "s66", "s67", "s68", "s69", "s70", "s71", "s72", "s73", "s74", "s75", "s76", "s77", "s78", "s79", "s80", "s81", "s82", "s83", "s84", "s85", "s86", "s87", "s88", "s89", "s90", "s91", "s92", "s93", "s94", "s95", "s96", "s97", "s98", "s99", "vcc", "scc", "memory");
}

extern "C" void kernel_launch(void* const* d_in, const int* in_sizes, int n_in, void* d_out, int out_size, void* d_ws, size_t ws_size, hipStream_t stream){
  (void)in_sizes; (void)n_in; (void)out_size; (void)ws_size;
  char* ws=(char*)d_ws;
  void* xn=ws; void* a=ws+(4u<<20); void* b=ws+(8u<<20); void* h=ws+(12u<<20); void* epq=ws+(28u<<20); void* ed=ws+(32u<<20); void* fl=ws+(40u<<20); void* st=ws+(41u<<20);
  MegaArgs g;
  g.p[0]=xn; g.p[1]=d_in[6]; g.p[2]=d_in[7]; g.p[3]=d_in[3]; g.p[4]=d_in[4]; g.p[5]=h; g.p[6]=a; g.p[7]=b;
  g.p[8]=d_in[1]; g.p[9]=d_in[2]; g.p[10]=a; g.p[11]=b; g.p[12]=h; g.p[13]=d_in[8]; g.p[14]=d_in[0]; g.p[15]=d_out;
  g.p[16]=epq; g.p[17]=ed; g.p[18]=fl; g.p[19]=st;
  fused_kernel<<<256,256,0,stream>>>(g);
}
```
